```python
import torch

def _softmax(x, axis=-1):
    return torch.softmax(x, dim=axis)
def _gelu(x):
    return torch.nn.functional.gelu(x, approximate="tanh")
def _silu(x):
    return torch.nn.functional.silu(x)
def _sigmoid(x):
    return torch.sigmoid(x)
def _layernorm(x, axis=-1, eps=1e-5):
    m = x.mean(dim=axis, keepdim=True)
    v = x.var(dim=axis, keepdim=True, unbiased=False)
    return (x - m) / torch.sqrt(v + eps)
def _scatter_add(tgt, idx, src, axis=0):
    return tgt.clone().index_add_(axis, idx, src)
def _segment_sum(src, seg, n):
    out = torch.zeros((n,) + src.shape[1:], dtype=src.dtype)
    return out.index_add_(0, seg, src)
def _pad(x, pw):
    flat = tuple(v for pair in reversed(pw) for v in pair)
    return torch.nn.functional.pad(x, flat)

kernel_name = 'parallel_sum__ssm_conv_h3__mixer_token_mlp__prompt'

def setup_inputs():

    _w = (lambda a: (lambda t: t.to(torch.bfloat16) if t.is_floating_point() else t)(torch.as_tensor(a)))
    def _A_setup():
        B, S, D = 1, 2048, 640
        ks = 4
        return {
            "q": _w((lambda *s: torch.randn(*s, dtype=torch.bfloat16) * 0.05)(B, S, D)),
            "k": _w((lambda *s: torch.randn(*s, dtype=torch.bfloat16) * 0.2)(B, S, D)),
            "v": _w((lambda *s: torch.randn(*s, dtype=torch.bfloat16) * 0.2)(B, S, D)),
            "shift_k": _w((lambda *s: torch.randn(*s, dtype=torch.bfloat16) * 0.2)(D, ks) * 0.5),
            "ssm_k": _w((lambda *s: torch.randn(*s, dtype=torch.bfloat16) * 0.2)(D, S) * (1.0 / S ** 0.5)),
            "ssm_D": _w((lambda *s: torch.randn(*s, dtype=torch.bfloat16) * 0.2)(D)),
        }
    _A_in = _A_setup()

    def _B_setup():
        B, S, D, F = 1, 2048, 640, 1536
        return {
            "x": _w((lambda *s: torch.randn(*s, dtype=torch.bfloat16) * 0.05)(B, S, D)),
            "w1": _w((lambda *s: torch.randn(*s, dtype=torch.bfloat16) * 0.4)(S, F) * (S ** -0.5)),
            "b1": _w((lambda *s: torch.randn(*s, dtype=torch.bfloat16) * 3.2)(F) * 0.02),
            "w2": _w((lambda *s: torch.randn(*s, dtype=torch.bfloat16) * 0.1)(F, S) * (F ** -0.5)),
            "b2": _w((lambda *s: torch.randn(*s, dtype=torch.bfloat16) * 0.4)(S) * 0.02),
        }
    _B_in = _B_setup()

    return {
        "A_q": _A_in["q"],
        "A_k": _A_in["k"],
        "A_v": _A_in["v"],
        "A_shift_k": _A_in["shift_k"],
        "A_ssm_k": _A_in["ssm_k"],
        "A_ssm_D": _A_in["ssm_D"],
        "B_x": _B_in["x"],
        "B_w1": _B_in["w1"],
        "B_b1": _B_in["b1"],
        "B_w2": _B_in["w2"],
        "B_b2": _B_in["b2"],
    }

def reference(A_q, A_k, A_v, A_shift_k, A_ssm_k, A_ssm_D, B_x, B_w1, B_b1, B_w2, B_b2):
    def _A_ref(q, k, v, shift_k, ssm_k, ssm_D):
        B, S, D = q.shape
        ks = shift_k.shape[-1]
        k_pad = _pad(k, ((0, 0), (ks - 1, 0), (0, 0)))
        k_shift = torch.zeros_like(k)
        for t in range(ks):
            k_shift = k_shift + k_pad[:, t:t + S, :] * shift_k[:, ks - 1 - t][None, None, :]
        kv = k_shift * v
        L = 2 * S
        kv_pad = _pad(kv, ((0, 0), (0, S), (0, 0)))
        kf_pad = _pad(ssm_k, ((0, 0), (0, S)))
        kv_f = (lambda x, axes=(-2,-1): torch.fft.rfft2(x.float(), dim=axes))(kv_pad[:, None, :, :], axes=(1, 2))
        kf_f = (lambda x, axes=(-2,-1): torch.fft.rfft2(x.float(), dim=axes))(kf_pad[:, None, :], axes=(1, 2))
        kf_f = (lambda x, *d: x.permute(*d))(kf_f, 1, 2, 0)
        y_f = kv_f * kf_f[None, :, :, :]
        y = (lambda x, s, axes=(-2,-1): torch.fft.irfft2(x, s=s, dim=axes).to(torch.bfloat16))(y_f, (1, L), axes=(1, 2))[:, 0, :S, :]
        y = y + kv * ssm_D[None, None, :]
        return q * y

    def _B_ref(x, w1, b1, w2, b2):
        B, S, D = x.shape
        h = _layernorm(x, axis=-1)
        u = torch.einsum("bsd,sf->bdf", h, w1) + b1
        u = _gelu(u)
        y = torch.einsum("bdf,fs->bds", u, w2) + b2
        return (lambda x, *d: x.permute(*d))(y, 0, 2, 1)

    x = A_q
    xn = _layernorm(x, axis=-1)
    return x + _A_ref(xn, A_k, A_v, A_shift_k, A_ssm_k, A_ssm_D) + _B_ref(xn, B_w1, B_b1, B_w2, B_b2)
```

```cpp
#include <hip/hip_runtime.h>
#include <hip/hip_bf16.h>
#include <stdint.h>

typedef __attribute__((ext_vector_type(8))) __bf16 bf8;
typedef __attribute__((ext_vector_type(2))) __bf16 bf2;
typedef __attribute__((ext_vector_type(4))) short s4;
typedef __attribute__((ext_vector_type(4))) float f4;
typedef __attribute__((ext_vector_type(2))) float f2;
typedef __attribute__((ext_vector_type(4))) unsigned u4;
typedef __attribute__((ext_vector_type(2))) unsigned u2;
#define LAS __attribute__((address_space(3)))
#define DEVI __device__ __forceinline__

#define NT_IN 1
#define NT_W 1
#define WPOL " nt"

constexpr int S_ = 2048, D_ = 640, F_ = 1536;
constexpr size_t WS_H = 0;
constexpr size_t WS_KV = WS_H + 2621440;
constexpr size_t WS_YA = WS_KV + 2621440;
constexpr size_t WS_U = WS_YA + 2621440;
constexpr size_t WS_ST = WS_U + 1966080;
constexpr size_t WS_DBG = WS_ST + 16384;

#define STAMP(kid, idx) do {} while (0)

DEVI float blo(unsigned u) { return __uint_as_float(u << 16); }
DEVI float bhi(unsigned u) { return __uint_as_float(u & 0xffff0000u); }
DEVI unsigned pk(float a, float b) { f2 v = {a, b}; return __builtin_bit_cast(unsigned, __builtin_convertvector(v, bf2)); }
DEVI float rbf(float a) { return blo(pk(a, a)); }
template <int CTRL, int RM> DEVI float dppf(float x) {
  return __builtin_bit_cast(float, __builtin_amdgcn_update_dpp(0, __builtin_bit_cast(int, x), CTRL, RM, 0xf, true));
}
DEVI float wave_sum(float v) {
  v += dppf<0xB1, 0xf>(v);
  v += dppf<0x4E, 0xf>(v);
  v += dppf<0x141, 0xf>(v);
  v += dppf<0x140, 0xf>(v);
  v += dppf<0x142, 0xa>(v);
  v += dppf<0x143, 0xc>(v);
  return __builtin_bit_cast(float, __builtin_amdgcn_readlane(__builtin_bit_cast(int, v), 63));
}
template <class T> DEVI T ldin(const void* p) {
  return __builtin_nontemporal_load((const T*)p);
}
DEVI u4 make_srd(const void* p, unsigned bytes) {
  uint64_t a = (uint64_t)p;
  u4 r = {(unsigned)a, (unsigned)(a >> 32) & 0xffffu, bytes, 0x00020000u};
  return r;
}
DEVI s4 tr_read(unsigned addr) { return __builtin_amdgcn_ds_read_tr16_b64_v4i16((LAS s4*)(size_t)addr); }
DEVI bf8 cat8(s4 a, s4 b) {
  typedef __attribute__((ext_vector_type(8))) short s8;
  s8 v = __builtin_shufflevector(a, b, 0, 1, 2, 3, 4, 5, 6, 7);
  return __builtin_bit_cast(bf8, v);
}
DEVI f4 mfma16(bf8 a, bf8 b, f4 c) { return __builtin_amdgcn_mfma_f32_16x16x32_bf16(a, b, c, 0, 0, 0); }
template <int N> DEVI void waitv() { asm volatile("s_waitcnt vmcnt(%0)" ::"i"(N) : "memory"); }

constexpr int K1_LN_BLOCKS = 256, K1_KV_BLOCKS = 80;
__global__ __launch_bounds__(512) void k1_prep(const char* __restrict__ x, const char* __restrict__ kk, const char* __restrict__ vv,
                                               const char* __restrict__ shk, char* __restrict__ ws) {
  __shared__ __attribute__((aligned(16))) char sm[8 * 1344];
  const int lane = threadIdx.x & 63, wave = __builtin_amdgcn_readfirstlane(threadIdx.x >> 6);
  const int b = blockIdx.x;
  STAMP(0, 0);
  if (b < K1_LN_BLOCKS) {
    const int s = b * 8 + wave;
    unsigned xr[5];
#pragma unroll
    for (int j = 0; j < 5; ++j) xr[j] = ldin<unsigned>(x + (size_t)s * 1280 + (lane + 64 * j) * 4);
    float e[10];
    float sum = 0.f;
#pragma unroll
    for (int j = 0; j < 5; ++j) { e[2 * j] = blo(xr[j]); e[2 * j + 1] = bhi(xr[j]); sum += e[2 * j] + e[2 * j + 1]; }
    const float mean = wave_sum(sum) * (1.f / 640.f);
    float sq = 0.f;
#pragma unroll
    for (int j = 0; j < 10; ++j) { float t = e[j] - mean; sq += t * t; }
    const float var = wave_sum(sq) * (1.f / 640.f);
    const float m1 = rbf(mean);
    const float inv1 = 1.f / rbf(__builtin_sqrtf(rbf(rbf(var) + 1e-5f)));
    sum = 0.f;
#pragma unroll
    for (int j = 0; j < 5; ++j) {
      unsigned t = pk(e[2 * j] - m1, e[2 * j + 1] - m1);
      t = pk(blo(t) * inv1, bhi(t) * inv1);
      e[2 * j] = blo(t); e[2 * j + 1] = bhi(t); sum += e[2 * j] + e[2 * j + 1];
    }
    const float mean2 = wave_sum(sum) * (1.f / 640.f);
    sq = 0.f;
#pragma unroll
    for (int j = 0; j < 10; ++j) { float t = e[j] - mean2; sq += t * t; }
    const float var2 = wave_sum(sq) * (1.f / 640.f);
    const float m2 = rbf(mean2);
    const float inv2 = 1.f / rbf(__builtin_sqrtf(rbf(rbf(var2) + 1e-5f)));
#pragma unroll
    for (int j = 0; j < 5; ++j) {
      unsigned t = pk(e[2 * j] - m2, e[2 * j + 1] - m2);
      t = pk(blo(t) * inv2, bhi(t) * inv2);
      *(unsigned*)(sm + wave * 1344 + (lane + 64 * j) * 4) = t;
    }
    if (lane == 0) { f2 st = {m1, inv1}; *(f2*)(ws + WS_ST + s * 8) = st; }
    __syncthreads();
    const unsigned base = (unsigned)(size_t)sm + ((lane >> 2) & 3) * 1344 + (lane >> 4) * 32 + (lane & 3) * 8;
    for (int p = wave; p < 10; p += 8) {
      s4 a = tr_read(base + p * 128), c = tr_read(base + p * 128 + 4 * 1344);
      *(bf8*)(ws + WS_H + ((size_t)b * 640 + p * 64 + lane) * 16) = cat8(a, c);
    }
  } else {
    const int id = (b - K1_LN_BLOCKS) * 8 + wave;
    const int cb = id % 20, rb = id / 20;
    const int rg = lane & 7, cq = lane >> 3;
    const int r0 = rb * 64 + rg * 8, c0 = cb * 32 + cq * 4;
    u2 kr[11], vr[8];
#pragma unroll
    for (int i = 0; i < 11; ++i) {
      int r = r0 - 3 + i;
      u2 z = {0u, 0u};
      kr[i] = (r >= 0) ? ldin<u2>(kk + (size_t)r * 1280 + c0 * 2) : z;
    }
#pragma unroll
    for (int i = 0; i < 8; ++i) vr[i] = ldin<u2>(vv + (size_t)(r0 + i) * 1280 + c0 * 2);
    u4 w01 = *(const u4*)(shk + c0 * 8), w23 = *(const u4*)(shk + c0 * 8 + 16);
#pragma unroll
    for (int c = 0; c < 4; ++c) {
      const unsigned wa = (c == 0) ? w01.x : (c == 1) ? w01.z : (c == 2) ? w23.x : w23.z;
      const unsigned wb = (c == 0) ? w01.y : (c == 1) ? w01.w : (c == 2) ? w23.y : w23.w;
      const float w0 = blo(wa), w1 = bhi(wa), w2 = blo(wb), w3 = bhi(wb);
      float kf[11];
#pragma unroll
      for (int i = 0; i < 11; ++i) { unsigned t = (c < 2) ? kr[i].x : kr[i].y; kf[i] = (c & 1) ? bhi(t) : blo(t); }
      float o[8];
#pragma unroll
      for (int i = 0; i < 8; i += 2) {
        unsigned p3 = pk(kf[i] * w3, kf[i + 1] * w3), p2 = pk(kf[i + 1] * w2, kf[i + 2] * w2);
        unsigned p1 = pk(kf[i + 2] * w1, kf[i + 3] * w1), p0 = pk(kf[i + 3] * w0, kf[i + 4] * w0);
        unsigned a = pk(blo(p3) + blo(p2), bhi(p3) + bhi(p2));
        a = pk(blo(a) + blo(p1), bhi(a) + bhi(p1));
        a = pk(blo(a) + blo(p0), bhi(a) + bhi(p0));
        unsigned t0 = (c < 2) ? vr[i].x : vr[i].y, t1 = (c < 2) ? vr[i + 1].x : vr[i + 1].y;
        o[i] = blo(a) * ((c & 1) ? bhi(t0) : blo(t0));
        o[i + 1] = bhi(a) * ((c & 1) ? bhi(t1) : blo(t1));
      }
      u4 out = {pk(o[7], o[6]), pk(o[5], o[4]), pk(o[3], o[2]), pk(o[1], o[0])};
      *(u4*)(ws + WS_KV + (size_t)(c0 + c) * 4096 + (2040 - r0) * 2) = out;
    }
  }
  STAMP(0, 1);
}

constexpr int CV_CP = 4160;
constexpr int CV_X = 4 * CV_CP;
constexpr int CV_BYTES = 21248;
DEVI void conv_channel(int ch, char* lds, const char* __restrict__ ssmk, const char* __restrict__ ssmd, char* __restrict__ ws, int lane) {
  const char* krow = ssmk + (size_t)ch * 4096;
  {
    u4 c[4]; u2 n[4]; u4 xv[4];
#pragma unroll
    for (int j = 0; j < 4; ++j) {
      const int z = lane + 64 * j;
      c[j] = ldin<u4>(krow + z * 16);
      u2 zz = {0u, 0u};
      n[j] = (z < 255) ? ldin<u2>(krow + z * 16 + 16) : zz;
      xv[j] = *(const u4*)(ws + WS_KV + (size_t)ch * 4096 + z * 16);
    }
    const u4 zero = {0u, 0u, 0u, 0u};
    if (lane < 30) *(u4*)(lds + CV_X + 4096 + lane * 16) = zero;
    if (lane < 4) *(u4*)(lds + lane * CV_CP) = zero;
    if (lane == 0) {
      const unsigned d4 = c[0].x, d5 = c[0].y;
      u4 t0 = {0u, 0u, 0u, d4 << 16}, t1 = {0u, 0u, 0u, d4}, t2 = {0u, 0u, d4 << 16, __builtin_amdgcn_alignbit(d5, d4, 16)}, t3 = {0u, 0u, d4, d5};
      *(u4*)(lds + 16) = t0; *(u4*)(lds + CV_CP + 16) = t1; *(u4*)(lds + 2 * CV_CP + 16) = t2; *(u4*)(lds + 3 * CV_CP + 16) = t3;
    }
#pragma unroll
    for (int j = 0; j < 4; ++j) {
      const int z = lane + 64 * j;
      const unsigned a0 = __builtin_amdgcn_alignbit(c[j].y, c[j].x, 16), a1 = __builtin_amdgcn_alignbit(c[j].z, c[j].y, 16),
                     a2 = __builtin_amdgcn_alignbit(c[j].w, c[j].z, 16), a3 = __builtin_amdgcn_alignbit(n[j].x, c[j].w, 16),
                     a4 = __builtin_amdgcn_alignbit(n[j].y, n[j].x, 16);
      u4 t0 = {a0, a1, a2, a3}, t1 = {c[j].y, c[j].z, c[j].w, n[j].x}, t2 = {a1, a2, a3, a4}, t3 = {c[j].z, c[j].w, n[j].x, n[j].y};
      char* p = lds + (z + 2) * 16;
      *(u4*)(p) = t0; *(u4*)(p + CV_CP) = t1; *(u4*)(p + 2 * CV_CP) = t2; *(u4*)(p + 3 * CV_CP) = t3;
      *(u4*)(lds + CV_X + z * 16) = xv[j];
    }
  }
  __builtin_amdgcn_fence(__ATOMIC_RELEASE, "wavefront");
  __builtin_amdgcn_wave_barrier();
  __builtin_amdgcn_fence(__ATOMIC_ACQUIRE, "wavefront");
  const int c16 = lane & 15, kg = lane >> 4;
  const char* ab = lds + (c16 & 3) * CV_CP + (c16 >> 2) * 8 + kg * 16;
  const char* bb = lds + CV_X + 480 - 32 * c16 + 16 * kg;
  f4 acc[8];
#pragma unroll
  for (int n = 0; n < 8; ++n) acc[n] = f4{0.f, 0.f, 0.f, 0.f};
#pragma unroll 2
  for (int q = 0; q < 8; ++q) {
    bf8 A[8], B[8];
#pragma unroll
    for (int p = 0; p < 8; ++p) {
      s4 lo = *(const s4*)(ab + 512 * p + 64 * q), hi = *(const s4*)(ab + 512 * p + 64 * q + 8);
      A[p] = cat8(lo, hi);
      B[p] = *(const bf8*)(bb + 3584 - 512 * p + 64 * q);
    }
#pragma unroll
    for (int p = 0; p < 8; ++p)
#pragma unroll
      for (int m = 0; m + p < 8; ++m) acc[p + m] = mfma16(A[p], B[m], acc[p + m]);
  }
  const float dd = blo(*(const unsigned short*)(ssmd + ch * 2));
#pragma unroll
  for (int n = 0; n < 8; ++n) {
    const int t0 = 256 * n + 16 * c16 + 4 * kg;
    const u2 xr = *(const u2*)(lds + CV_X + (2044 - t0) * 2);
    const unsigned y01 = pk(acc[n][0], acc[n][1]), y23 = pk(acc[n][2], acc[n][3]);
    const unsigned g01 = pk(bhi(xr.y) * dd, blo(xr.y) * dd), g23 = pk(bhi(xr.x) * dd, blo(xr.x) * dd);
    u2 o = {pk(blo(y01) + blo(g01), bhi(y01) + bhi(g01)), pk(blo(y23) + blo(g23), bhi(y23) + bhi(g23))};
    *(u2*)(ws + WS_YA + (size_t)ch * 4096 + t0 * 2) = o;
  }
}

DEVI float gelu_t(float x) {
  const float z = 1.5957691216057308f * (x + 0.044715f * x * x * x);
  return x * __builtin_amdgcn_rcpf(1.f + __expf(-z));
}

constexpr int G1_D = 5;
constexpr int G1_RING = G1_D * 4096;
constexpr int K2_CONV_OFF = 4 * G1_RING;
constexpr int K2_CTR = 7 * CV_BYTES;
constexpr int K2_LDS = K2_CTR + 16;

__global__ __launch_bounds__(448, 2) void k2_gemm1_conv(const char* __restrict__ w1, const char* __restrict__ b1, const char* __restrict__ ssmk,
                                                        const char* __restrict__ ssmd, char* __restrict__ ws) {
  extern __shared__ __attribute__((aligned(16))) char smem[];
  const int lane = threadIdx.x & 63, wave = __builtin_amdgcn_readfirstlane(threadIdx.x >> 6);
  const int b = blockIdx.x;
  if (wave == 0) STAMP(1, 0);
  if (threadIdx.x == 0) *(volatile unsigned*)(smem + K2_CTR) = 0u;
  __syncthreads();
  if (b >= 240) {
    conv_channel(528 + (b - 240) * 7 + wave, smem + wave * CV_BYTES, ssmk, ssmd, ws, lane);
    STAMP(1, 2 + wave);
    return;
  }
  if (wave >= 4) {
    if (wave < 6 || b < 48) conv_channel(wave < 6 ? 2 * b + (wave - 4) : 480 + b, smem + K2_CONV_OFF + (wave - 4) * CV_BYTES, ssmk, ssmd, ws, lane);
    STAMP(1, 2 + wave);
    return;
  }
  __builtin_amdgcn_s_setprio(2);
  const int xg = b & 7, r = b >> 3;
  const int dt = (xg & 1) * 5 + r % 5, ft = (xg >> 1) * 6 + r / 5;
  const u4 srdH = make_srd(ws + WS_H, 2621440), srdW = make_srd(w1, 6291456);
  const unsigned ring = __builtin_amdgcn_readfirstlane((unsigned)(size_t)smem + wave * G1_RING);
  const unsigned voh = ((lane >> 4) * 640 + dt * 64 + (lane & 15)) * 16;
  unsigned vow[4];
#pragma unroll
  for (int t = 0; t < 4; ++t) vow[t] = (8 * t + (lane >> 3)) * 3072 + ft * 128 + (((lane & 7) ^ (2 * ((lane >> 4) & 1) + 4 * (t & 1))) * 16) - 1024 * t;
  unsigned tra[4];
  {
    const int g = lane >> 4, q = (lane >> 2) & 3, p = lane & 3, fsw = 2 * (q >> 1) + 4 * (g & 1);
#pragma unroll
    for (int m = 0; m < 4; ++m) tra[m] = ring + (8 * g + q) * 128 + (((2 * m) ^ fsw) + (p >> 1)) * 16 + (p & 1) * 8;
  }
  u4 hq[G1_D][4];
  f4 acc[4][4];
#pragma unroll
  for (int m = 0; m < 4; ++m)
#pragma unroll
    for (int n = 0; n < 4; ++n) acc[m][n] = f4{0.f, 0.f, 0.f, 0.f};

#define G1_ISSUE(i)                                                                                                          \
  do {                                                                                                                       \
    const unsigned ks_ = 4 * (i) + wave;                                                                                     \
    asm volatile("s_waitcnt lgkmcnt(0)\n\ts_mov_b32 m0, %0\n\ts_nop 0\n\t"                                                   \
                 "buffer_load_dwordx4 %1, %5, %6 offen" WPOL " lds\n\t"                                                      \
                 "buffer_load_dwordx4 %2, %5, %6 offen offset:1024" WPOL " lds\n\t"                                          \
                 "buffer_load_dwordx4 %3, %5, %6 offen offset:2048" WPOL " lds\n\t"                                          \
                 "buffer_load_dwordx4 %4, %5, %6 offen offset:3072" WPOL " lds"                                              \
                 ::"s"(ring + ((i) % G1_D) * 4096), "v"(vow[0]), "v"(vow[1]), "v"(vow[2]), "v"(vow[3]), "s"(srdW), "s"(ks_ * 98304u) \
                 : "memory");                                                                                                \
    asm volatile("buffer_load_dwordx4 %0, %4, %5, %6 offen\n\t"                                                              \
                 "buffer_load_dwordx4 %1, %4, %5, %6 offen offset:256\n\t"                                                   \
                 "buffer_load_dwordx4 %2, %4, %5, %6 offen offset:512\n\t"                                                   \
                 "buffer_load_dwordx4 %3, %4, %5, %6 offen offset:768"                                                       \
                 : "=&v"(hq[(i) % G1_D][0]), "=&v"(hq[(i) % G1_D][1]), "=&v"(hq[(i) % G1_D][2]), "=&v"(hq[(i) % G1_D][3])     \
                 : "v"(voh), "s"(srdH), "s"(ks_ * 40960u)                                                                    \
                 : "memory");                                                                                                \
  } while (0)

#pragma unroll
  for (int i = 0; i < G1_D - 1; ++i) G1_ISSUE(i);
#pragma unroll
  for (int i = 0; i < 16; ++i) {
    constexpr int dummy = 0; (void)dummy;
    const int younger = (15 - i < G1_D - 2) ? 15 - i : G1_D - 2;
    u4* hs = hq[i % G1_D];
#define G1_WAIT(N) asm volatile("s_waitcnt vmcnt(" #N ")" : "+v"(hs[0]), "+v"(hs[1]), "+v"(hs[2]), "+v"(hs[3])::"memory")
    if (younger == 3) G1_WAIT(24); else if (younger == 2) G1_WAIT(16); else if (younger == 1) G1_WAIT(8); else G1_WAIT(0);
    if (i + G1_D - 1 < 16) G1_ISSUE(i + G1_D - 1);
    bf8 wf[4];
#pragma unroll
    for (int m = 0; m < 4; ++m) wf[m] = cat8(tr_read(tra[m] + (i % G1_D) * 4096), tr_read(tra[m] + (i % G1_D) * 4096 + 512));
#pragma unroll
    for (int m = 0; m < 4; ++m)
#pragma unroll
      for (int n = 0; n < 4; ++n) acc[m][n] = mfma16(wf[m], __builtin_bit_cast(bf8, hs[n]), acc[m][n]);
  }
  static_assert(G1_D == 5, "wait ladder above assumes depth 5");
  STAMP(1, 9 + wave);
  u2 bias[4];
#pragma unroll
  for (int m = 0; m < 4; ++m) bias[m] = *(const u2*)(b1 + (ft * 64 + 16 * m + 4 * (lane >> 4)) * 2);
  char* myring = smem + wave * G1_RING;
#pragma unroll
  for (int m = 0; m < 4; ++m)
#pragma unroll
    for (int n = 0; n < 4; ++n) *(f4*)(myring + (m * 4 + n) * 1024 + lane * 16) = acc[m][n];
  {
    LAS unsigned* ctr = (LAS unsigned*)(size_t)(unsigned)(size_t)(smem + K2_CTR);
    __builtin_amdgcn_fence(__ATOMIC_RELEASE, "workgroup");
    if (lane == 0) __hip_atomic_fetch_add(ctr, 1u, __ATOMIC_RELAXED, __HIP_MEMORY_SCOPE_WORKGROUP);
    while (__hip_atomic_load(ctr, __ATOMIC_RELAXED, __HIP_MEMORY_SCOPE_WORKGROUP) < 4u) __builtin_amdgcn_s_sleep(1);
    __builtin_amdgcn_fence(__ATOMIC_ACQUIRE, "workgroup");
  }
  unsigned o[8];
#pragma unroll
  for (int m = 0; m < 4; ++m) {
    f4 t = *(const f4*)(smem + (m * 4 + wave) * 1024 + lane * 16);
#pragma unroll
    for (int w = 1; w < 4; ++w) t += *(const f4*)(smem + w * G1_RING + (m * 4 + wave) * 1024 + lane * 16);
    const unsigned a01 = pk(t[0], t[1]), a23 = pk(t[2], t[3]);
    const unsigned c01 = pk(blo(a01) + blo(bias[m].x), bhi(a01) + bhi(bias[m].x)), c23 = pk(blo(a23) + blo(bias[m].y), bhi(a23) + bhi(bias[m].y));
    o[2 * m] = pk(gelu_t(blo(c01)), gelu_t(bhi(c01)));
    o[2 * m + 1] = pk(gelu_t(blo(c23)), gelu_t(bhi(c23)));
  }
#pragma unroll
  for (int P = 0; P < 2; ++P) {
    u4 v = {o[4 * P], o[4 * P + 1], o[4 * P + 2], o[4 * P + 3]};
    *(u4*)(ws + WS_U + ((size_t)(ft * 2 + P) * 40 + dt * 4 + wave) * 1024 + lane * 16) = v;
  }
  STAMP(1, 2 + wave);
}

constexpr int G2_D = 6;
constexpr int G2_RING = G2_D * 4096;
constexpr int K3_YA = 4 * G2_RING;
constexpr int K3_LDS = K3_YA + 10240;

__global__ __launch_bounds__(256, 1) void k3_gemm2(const char* __restrict__ w2, const char* __restrict__ b2, const char* __restrict__ x,
                                                   char* __restrict__ out, char* __restrict__ ws) {
  extern __shared__ __attribute__((aligned(16))) char smem[];
  const int lane = threadIdx.x & 63, wave = __builtin_amdgcn_readfirstlane(threadIdx.x >> 6);
  const int b = blockIdx.x;
  if (wave == 0) STAMP(2, 0);
  const int xg = b & 7, r = b >> 3;
  const int dt = (xg & 1) * 4 + (r & 3), st = (xg >> 1) * 8 + (r >> 2);
  const u4 srdU = make_srd(ws + WS_U, 1966080), srdW = make_srd(w2, 6291456), srdY = make_srd(ws + WS_YA, 2621440);
  const unsigned sbase = __builtin_amdgcn_readfirstlane((unsigned)(size_t)smem);
  const unsigned ring = sbase + wave * G2_RING;
  const unsigned vou0 = dt * 5120 + lane * 16, vou1 = vou0 + 4096;
  unsigned vow[4];
#pragma unroll
  for (int t = 0; t < 4; ++t) vow[t] = (8 * t + (lane >> 3)) * 4096 + st * 128 + (((lane & 7) ^ ((lane >> 3) & 6)) * 16) - 1024 * t;
  unsigned tra[4];
  const int g = lane >> 4, q = (lane >> 2) & 3, p = lane & 3;
  {
    const int fsw = 4 * (g & 1) + (q & 2);
#pragma unroll
    for (int m = 0; m < 4; ++m) tra[m] = ring + (4 * g + q) * 128 + (((2 * m) ^ fsw) + (p >> 1)) * 16 + (p & 1) * 8;
  }
  {
    const unsigned voy = (dt * 80 + (lane >> 3)) * 4096 + st * 128 + (lane & 7) * 16;
    for (int i = wave; i < 10; i += 4)
      asm volatile("s_mov_b32 m0, %0\n\ts_nop 0\n\tbuffer_load_dwordx4 %1, %2, %3 offen lds" ::"s"(sbase + K3_YA + i * 1024), "v"(voy), "s"(srdY), "s"(i * 32768u) : "memory");
  }
  u4 uq[G2_D][5];
  f4 acc[5][4];
#pragma unroll
  for (int m = 0; m < 5; ++m)
#pragma unroll
    for (int n = 0; n < 4; ++n) acc[m][n] = f4{0.f, 0.f, 0.f, 0.f};

#define G2_ISSUE(i)                                                                                                          \
  do {                                                                                                                       \
    const unsigned ks_ = 4 * (i) + wave;                                                                                     \
    asm volatile("s_waitcnt lgkmcnt(0)\n\ts_mov_b32 m0, %0\n\ts_nop 0\n\t"                                                   \
                 "buffer_load_dwordx4 %1, %5, %6 offen" WPOL " lds\n\t"                                                      \
                 "buffer_load_dwordx4 %2, %5, %6 offen offset:1024" WPOL " lds\n\t"                                          \
                 "buffer_load_dwordx4 %3, %5, %6 offen offset:2048" WPOL " lds\n\t"                                          \
                 "buffer_load_dwordx4 %4, %5, %6 offen offset:3072" WPOL " lds"                                              \
                 ::"s"(ring + ((i) % G2_D) * 4096), "v"(vow[0]), "v"(vow[1]), "v"(vow[2]), "v"(vow[3]), "s"(srdW), "s"(ks_ * 131072u) \
                 : "memory");                                                                                                \
    asm volatile("buffer_load_dwordx4 %0, %5, %7, %8 offen\n\t"                                                              \
                 "buffer_load_dwordx4 %1, %5, %7, %8 offen offset:1024\n\t"                                                  \
                 "buffer_load_dwordx4 %2, %5, %7, %8 offen offset:2048\n\t"                                                  \
                 "buffer_load_dwordx4 %3, %5, %7, %8 offen offset:3072\n\t"                                                  \
                 "buffer_load_dwordx4 %4, %6, %7, %8 offen"                                                                  \
                 : "=&v"(uq[(i) % G2_D][0]), "=&v"(uq[(i) % G2_D][1]), "=&v"(uq[(i) % G2_D][2]), "=&v"(uq[(i) % G2_D][3]),   \
                   "=&v"(uq[(i) % G2_D][4])                                                                                  \
                 : "v"(vou0), "v"(vou1), "s"(srdU), "s"(ks_ * 40960u)                                                        \
                 : "memory");                                                                                                \
  } while (0)

#pragma unroll
  for (int i = 0; i < G2_D - 1; ++i) G2_ISSUE(i);
#pragma unroll
  for (int i = 0; i < 12; ++i) {
    const int younger = (11 - i < G2_D - 2) ? 11 - i : G2_D - 2;
    u4* us = uq[i % G2_D];
#define G2_WAIT(N) asm volatile("s_waitcnt vmcnt(" #N ")" : "+v"(us[0]), "+v"(us[1]), "+v"(us[2]), "+v"(us[3]), "+v"(us[4])::"memory")
    if (younger == 4) G2_WAIT(36); else if (younger == 3) G2_WAIT(27); else if (younger == 2) G2_WAIT(18); else if (younger == 1) G2_WAIT(9); else G2_WAIT(0);
    if (i + G2_D - 1 < 12) G2_ISSUE(i + G2_D - 1);
    bf8 wf[4];
#pragma unroll
    for (int n = 0; n < 4; ++n) wf[n] = cat8(tr_read(tra[n] + (i % G2_D) * 4096), tr_read(tra[n] + (i % G2_D) * 4096 + 2048));
#pragma unroll
    for (int m = 0; m < 5; ++m)
#pragma unroll
      for (int n = 0; n < 4; ++n) acc[m][n] = mfma16(__builtin_bit_cast(bf8, us[m]), wf[n], acc[m][n]);
  }
  static_assert(G2_D == 6, "wait ladder above assumes depth 6");
  STAMP(2, 9 + wave);
  const int s = st * 64 + wave * 16 + (lane & 15);
  u2 xr[5];
#pragma unroll
  for (int m = 0; m < 5; ++m) xr[m] = *(const u2*)(x + (size_t)s * 1280 + (dt * 80 + 16 * m + 4 * g) * 2);
  const f2 stt = *(const f2*)(ws + WS_ST + s * 8);
  const float bb = blo(*(const unsigned short*)(b2 + s * 2));
  char* myring = smem + wave * G2_RING;
#pragma unroll
  for (int m = 0; m < 5; ++m)
#pragma unroll
    for (int n = 0; n < 4; ++n) *(f4*)(myring + (m * 4 + n) * 1024 + lane * 16) = acc[m][n];
  __syncthreads();
  const unsigned yab = sbase + K3_YA + (4 * g + q) * 128 + wave * 32 + p * 8;
#pragma unroll
  for (int m = 0; m < 5; ++m) {
    f4 t = *(const f4*)(smem + (m * 4 + wave) * 1024 + lane * 16);
#pragma unroll
    for (int w = 1; w < 4; ++w) t += *(const f4*)(smem + w * G2_RING + (m * 4 + wave) * 1024 + lane * 16);
    const u2 ya = __builtin_bit_cast(u2, tr_read(yab + m * 2048));
    unsigned res[2];
#pragma unroll
    for (int h = 0; h < 2; ++h) {
      const unsigned xx = h ? xr[m].y : xr[m].x, yy = h ? ya.y : ya.x;
      unsigned bv = pk(t[2 * h], t[2 * h + 1]);
      bv = pk(blo(bv) + bb, bhi(bv) + bb);
      unsigned xn = pk(blo(xx) - stt.x, bhi(xx) - stt.x);
      xn = pk(blo(xn) * stt.y, bhi(xn) * stt.y);
      unsigned a = pk(blo(xn) * blo(yy), bhi(xn) * bhi(yy));
      a = pk(blo(xx) + blo(a), bhi(xx) + bhi(a));
      res[h] = pk(blo(a) + blo(bv), bhi(a) + bhi(bv));
    }
    u2 o = {res[0], res[1]};
    *(u2*)(out + (size_t)s * 1280 + (dt * 80 + 16 * m + 4 * g) * 2) = o;
  }
  STAMP(2, 2 + wave);
}

extern "C" void kernel_launch(void* const* d_in, const int* in_sizes, int n_in, void* d_out, int out_size, void* d_ws, size_t ws_size,
                              hipStream_t stream) {
  const char* q = (const char*)d_in[0];
  const char* k = (const char*)d_in[1];
  const char* v = (const char*)d_in[2];
  const char* shk = (const char*)d_in[3];
  const char* ssmk = (const char*)d_in[4];
  const char* ssmd = (const char*)d_in[5];
  const char* w1 = (const char*)d_in[7];
  const char* b1 = (const char*)d_in[8];
  const char* w2 = (const char*)d_in[9];
  const char* b2 = (const char*)d_in[10];
  char* ws = (char*)d_ws;
  (void)hipFuncSetAttribute((const void*)k2_gemm1_conv, hipFuncAttributeMaxDynamicSharedMemorySize, K2_LDS);
  (void)hipFuncSetAttribute((const void*)k3_gemm2, hipFuncAttributeMaxDynamicSharedMemorySize, K3_LDS);
  k1_prep<<<K1_LN_BLOCKS + K1_KV_BLOCKS, 512, 0, stream>>>(q, k, v, shk, ws);
  k2_gemm1_conv<<<256, 448, K2_LDS, stream>>>(w1, b1, ssmk, ssmd, ws);
  k3_gemm2<<<256, 256, K3_LDS, stream>>>(w2, b2, q, (char*)d_out, ws);
}
```

```cpp
#include <hip/hip_runtime.h>
#include <hip/hip_bf16.h>
#include <stdint.h>

typedef __attribute__((ext_vector_type(8))) __bf16 bf8;
typedef __attribute__((ext_vector_type(2))) __bf16 bf2;
typedef __attribute__((ext_vector_type(4))) short s4;
typedef __attribute__((ext_vector_type(4))) float f4;
typedef __attribute__((ext_vector_type(2))) float f2;
typedef __attribute__((ext_vector_type(4))) unsigned u4;
typedef __attribute__((ext_vector_type(2))) unsigned u2;
#define LAS __attribute__((address_space(3)))
#define DEVI __device__ __forceinline__

#define NT_IN 0
#define NT_W 0
#define WPOL ""

constexpr int S_ = 2048, D_ = 640, F_ = 1536;
constexpr size_t WS_H = 0;
constexpr size_t WS_KV = WS_H + 2621440;
constexpr size_t WS_YA = WS_KV + 2621440;
constexpr size_t WS_U = WS_YA + 2621440;
constexpr size_t WS_ST = WS_U + 1966080;
constexpr size_t WS_DBG = WS_ST + 16384;

#define STAMP(kid, idx) do {} while (0)

DEVI float blo(unsigned u) { return __uint_as_float(u << 16); }
DEVI float bhi(unsigned u) { return __uint_as_float(u & 0xffff0000u); }
DEVI unsigned pk(float a, float b) { f2 v = {a, b}; return __builtin_bit_cast(unsigned, __builtin_convertvector(v, bf2)); }
DEVI float rbf(float a) { return blo(pk(a, a)); }
template <int CTRL, int RM> DEVI float dppf(float x) {
  return __builtin_bit_cast(float, __builtin_amdgcn_update_dpp(0, __builtin_bit_cast(int, x), CTRL, RM, 0xf, true));
}
DEVI float wave_sum(float v) {
  v += dppf<0xB1, 0xf>(v);
  v += dppf<0x4E, 0xf>(v);
  v += dppf<0x141, 0xf>(v);
  v += dppf<0x140, 0xf>(v);
  v += dppf<0x142, 0xa>(v);
  v += dppf<0x143, 0xc>(v);
  return __builtin_bit_cast(float, __builtin_amdgcn_readlane(__builtin_bit_cast(int, v), 63));
}
template <class T> DEVI T ldin(const void* p) {
  return *(const T*)p;
}
DEVI u4 make_srd(const void* p, unsigned bytes) {
  uint64_t a = (uint64_t)p;
  u4 r = {(unsigned)a, (unsigned)(a >> 32) & 0xffffu, bytes, 0x00020000u};
  return r;
}
DEVI s4 tr_read(unsigned addr) { return __builtin_amdgcn_ds_read_tr16_b64_v4i16((LAS s4*)(size_t)addr); }
DEVI bf8 cat8(s4 a, s4 b) {
  typedef __attribute__((ext_vector_type(8))) short s8;
  s8 v = __builtin_shufflevector(a, b, 0, 1, 2, 3, 4, 5, 6, 7);
  return __builtin_bit_cast(bf8, v);
}
DEVI f4 mfma16(bf8 a, bf8 b, f4 c) { return __builtin_amdgcn_mfma_f32_16x16x32_bf16(a, b, c, 0, 0, 0); }
template <int N> DEVI void waitv() { asm volatile("s_waitcnt vmcnt(%0)" ::"i"(N) : "memory"); }

constexpr int K1_LN_BLOCKS = 256, K1_KV_BLOCKS = 80;
__global__ __launch_bounds__(512) void k1_prep(const char* __restrict__ x, const char* __restrict__ kk, const char* __restrict__ vv,
                                               const char* __restrict__ shk, char* __restrict__ ws) {
  __shared__ __attribute__((aligned(16))) char sm[8 * 1344];
  const int lane = threadIdx.x & 63, wave = __builtin_amdgcn_readfirstlane(threadIdx.x >> 6);
  const int b = blockIdx.x;
  STAMP(0, 0);
  if (b < K1_LN_BLOCKS) {
    const int s = b * 8 + wave;
    unsigned xr[5];
#pragma unroll
    for (int j = 0; j < 5; ++j) xr[j] = ldin<unsigned>(x + (size_t)s * 1280 + (lane + 64 * j) * 4);
    float e[10];
    float sum = 0.f;
#pragma unroll
    for (int j = 0; j < 5; ++j) { e[2 * j] = blo(xr[j]); e[2 * j + 1] = bhi(xr[j]); sum += e[2 * j] + e[2 * j + 1]; }
    const float mean = wave_sum(sum) * (1.f / 640.f);
    float sq = 0.f;
#pragma unroll
    for (int j = 0; j < 10; ++j) { float t = e[j] - mean; sq += t * t; }
    const float var = wave_sum(sq) * (1.f / 640.f);
    const float m1 = rbf(mean);
    const float inv1 = 1.f / rbf(__builtin_sqrtf(rbf(rbf(var) + 1e-5f)));
    sum = 0.f;
#pragma unroll
    for (int j = 0; j < 5; ++j) {
      unsigned t = pk(e[2 * j] - m1, e[2 * j + 1] - m1);
      t = pk(blo(t) * inv1, bhi(t) * inv1);
      e[2 * j] = blo(t); e[2 * j + 1] = bhi(t); sum += e[2 * j] + e[2 * j + 1];
    }
    const float mean2 = wave_sum(sum) * (1.f / 640.f);
    sq = 0.f;
#pragma unroll
    for (int j = 0; j < 10; ++j) { float t = e[j] - mean2; sq += t * t; }
    const float var2 = wave_sum(sq) * (1.f / 640.f);
    const float m2 = rbf(mean2);
    const float inv2 = 1.f / rbf(__builtin_sqrtf(rbf(rbf(var2) + 1e-5f)));
#pragma unroll
    for (int j = 0; j < 5; ++j) {
      unsigned t = pk(e[2 * j] - m2, e[2 * j + 1] - m2);
      t = pk(blo(t) * inv2, bhi(t) * inv2);
      *(unsigned*)(sm + wave * 1344 + (lane + 64 * j) * 4) = t;
    }
    if (lane == 0) { f2 st = {m1, inv1}; *(f2*)(ws + WS_ST + s * 8) = st; }
    __syncthreads();
    const unsigned base = (unsigned)(size_t)sm + ((lane >> 2) & 3) * 1344 + (lane >> 4) * 32 + (lane & 3) * 8;
    for (int p = wave; p < 10; p += 8) {
      s4 a = tr_read(base + p * 128), c = tr_read(base + p * 128 + 4 * 1344);
      *(bf8*)(ws + WS_H + ((size_t)b * 640 + p * 64 + lane) * 16) = cat8(a, c);
    }
  } else {
    const int id = (b - K1_LN_BLOCKS) * 8 + wave;
    const int cb = id % 20, rb = id / 20;
    const int rg = lane & 7, cq = lane >> 3;
    const int r0 = rb * 64 + rg * 8, c0 = cb * 32 + cq * 4;
    u2 kr[11], vr[8];
#pragma unroll
    for (int i = 0; i < 11; ++i) {
      int r = r0 - 3 + i;
      u2 z = {0u, 0u};
      kr[i] = (r >= 0) ? ldin<u2>(kk + (size_t)r * 1280 + c0 * 2) : z;
    }
#pragma unroll
    for (int i = 0; i < 8; ++i) vr[i] = ldin<u2>(vv + (size_t)(r0 + i) * 1280 + c0 * 2);
    u4 w01 = *(const u4*)(shk + c0 * 8), w23 = *(const u4*)(shk + c0 * 8 + 16);
#pragma unroll
    for (int c = 0; c < 4; ++c) {
      const unsigned wa = (c == 0) ? w01.x : (c == 1) ? w01.z : (c == 2) ? w23.x : w23.z;
      const unsigned wb = (c == 0) ? w01.y : (c == 1) ? w01.w : (c == 2) ? w23.y : w23.w;
      const float w0 = blo(wa), w1 = bhi(wa), w2 = blo(wb), w3 = bhi(wb);
      float kf[11];
#pragma unroll
      for (int i = 0; i < 11; ++i) { unsigned t = (c < 2) ? kr[i].x : kr[i].y; kf[i] = (c & 1) ? bhi(t) : blo(t); }
      float o[8];
#pragma unroll
      for (int i = 0; i < 8; i += 2) {
        unsigned p3 = pk(kf[i] * w3, kf[i + 1] * w3), p2 = pk(kf[i + 1] * w2, kf[i + 2] * w2);
        unsigned p1 = pk(kf[i + 2] * w1, kf[i + 3] * w1), p0 = pk(kf[i + 3] * w0, kf[i + 4] * w0);
        unsigned a = pk(blo(p3) + blo(p2), bhi(p3) + bhi(p2));
        a = pk(blo(a) + blo(p1), bhi(a) + bhi(p1));
        a = pk(blo(a) + blo(p0), bhi(a) + bhi(p0));
        unsigned t0 = (c < 2) ? vr[i].x : vr[i].y, t1 = (c < 2) ? vr[i + 1].x : vr[i + 1].y;
        o[i] = blo(a) * ((c & 1) ? bhi(t0) : blo(t0));
        o[i + 1] = bhi(a) * ((c & 1) ? bhi(t1) : blo(t1));
      }
      u4 out = {pk(o[7], o[6]), pk(o[5], o[4]), pk(o[3], o[2]), pk(o[1], o[0])};
      *(u4*)(ws + WS_KV + (size_t)(c0 + c) * 4096 + (2040 - r0) * 2) = out;
    }
  }
  STAMP(0, 1);
}

constexpr int CV_CP = 4160;
constexpr int CV_X = 4 * CV_CP;
constexpr int CV_BYTES = 21248;
DEVI void conv_channel(int ch, char* lds, const char* __restrict__ ssmk, const char* __restrict__ ssmd, char* __restrict__ ws, int lane) {
  const char* krow = ssmk + (size_t)ch * 4096;
  {
    u4 c[4]; u2 n[4]; u4 xv[4];
#pragma unroll
    for (int j = 0; j < 4; ++j) {
      const int z = lane + 64 * j;
      c[j] = ldin<u4>(krow + z * 16);
      u2 zz = {0u, 0u};
      n[j] = (z < 255) ? ldin<u2>(krow + z * 16 + 16) : zz;
      xv[j] = *(const u4*)(ws + WS_KV + (size_t)ch * 4096 + z * 16);
    }
    const u4 zero = {0u, 0u, 0u, 0u};
    if (lane < 30) *(u4*)(lds + CV_X + 4096 + lane * 16) = zero;
    if (lane < 4) *(u4*)(lds + lane * CV_CP) = zero;
    if (lane == 0) {
      const unsigned d4 = c[0].x, d5 = c[0].y;
      u4 t0 = {0u, 0u, 0u, d4 << 16}, t1 = {0u, 0u, 0u, d4}, t2 = {0u, 0u, d4 << 16, __builtin_amdgcn_alignbit(d5, d4, 16)}, t3 = {0u, 0u, d4, d5};
      *(u4*)(lds + 16) = t0; *(u4*)(lds + CV_CP + 16) = t1; *(u4*)(lds + 2 * CV_CP + 16) = t2; *(u4*)(lds + 3 * CV_CP + 16) = t3;
    }
#pragma unroll
    for (int j = 0; j < 4; ++j) {
      const int z = lane + 64 * j;
      const unsigned a0 = __builtin_amdgcn_alignbit(c[j].y, c[j].x, 16), a1 = __builtin_amdgcn_alignbit(c[j].z, c[j].y, 16),
                     a2 = __builtin_amdgcn_alignbit(c[j].w, c[j].z, 16), a3 = __builtin_amdgcn_alignbit(n[j].x, c[j].w, 16),
                     a4 = __builtin_amdgcn_alignbit(n[j].y, n[j].x, 16);
      u4 t0 = {a0, a1, a2, a3}, t1 = {c[j].y, c[j].z, c[j].w, n[j].x}, t2 = {a1, a2, a3, a4}, t3 = {c[j].z, c[j].w, n[j].x, n[j].y};
      char* p = lds + (z + 2) * 16;
      *(u4*)(p) = t0; *(u4*)(p + CV_CP) = t1; *(u4*)(p + 2 * CV_CP) = t2; *(u4*)(p + 3 * CV_CP) = t3;
      *(u4*)(lds + CV_X + z * 16) = xv[j];
    }
  }
  __builtin_amdgcn_fence(__ATOMIC_RELEASE, "wavefront");
  __builtin_amdgcn_wave_barrier();
  __builtin_amdgcn_fence(__ATOMIC_ACQUIRE, "wavefront");
  const int c16 = lane & 15, kg = lane >> 4;
  const char* ab = lds + (c16 & 3) * CV_CP + (c16 >> 2) * 8 + kg * 16;
  const char* bb = lds + CV_X + 480 - 32 * c16 + 16 * kg;
  f4 acc[8];
#pragma unroll
  for (int n = 0; n < 8; ++n) acc[n] = f4{0.f, 0.f, 0.f, 0.f};
#pragma unroll 2
  for (int q = 0; q < 8; ++q) {
    bf8 A[8], B[8];
#pragma unroll
    for (int p = 0; p < 8; ++p) {
      s4 lo = *(const s4*)(ab + 512 * p + 64 * q), hi = *(const s4*)(ab + 512 * p + 64 * q + 8);
      A[p] = cat8(lo, hi);
      B[p] = *(const bf8*)(bb + 3584 - 512 * p + 64 * q);
    }
#pragma unroll
    for (int p = 0; p < 8; ++p)
#pragma unroll
      for (int m = 0; m + p < 8; ++m) acc[p + m] = mfma16(A[p], B[m], acc[p + m]);
  }
  const float dd = blo(*(const unsigned short*)(ssmd + ch * 2));
#pragma unroll
  for (int n = 0; n < 8; ++n) {
    const int t0 = 256 * n + 16 * c16 + 4 * kg;
    const u2 xr = *(const u2*)(lds + CV_X + (2044 - t0) * 2);
    const unsigned y01 = pk(acc[n][0], acc[n][1]), y23 = pk(acc[n][2], acc[n][3]);
    const unsigned g01 = pk(bhi(xr.y) * dd, blo(xr.y) * dd), g23 = pk(bhi(xr.x) * dd, blo(xr.x) * dd);
    u2 o = {pk(blo(y01) + blo(g01), bhi(y01) + bhi(g01)), pk(blo(y23) + blo(g23), bhi(y23) + bhi(g23))};
    *(u2*)(ws + WS_YA + (size_t)ch * 4096 + t0 * 2) = o;
  }
}

DEVI float gelu_t(float x) {
  const float z = 1.5957691216057308f * (x + 0.044715f * x * x * x);
  return x * __builtin_amdgcn_rcpf(1.f + __expf(-z));
}

constexpr int G1_D = 5;
constexpr int G1_RING = G1_D * 4096;
constexpr int K2_CONV_OFF = 4 * G1_RING;
constexpr int K2_CTR = 7 * CV_BYTES;
constexpr int K2_LDS = K2_CTR + 16;

__global__ __launch_bounds__(448, 2) void k2_gemm1_conv(const char* __restrict__ w1, const char* __restrict__ b1, const char* __restrict__ ssmk,
                                                        const char* __restrict__ ssmd, char* __restrict__ ws) {
  extern __shared__ __attribute__((aligned(16))) char smem[];
  const int lane = threadIdx.x & 63, wave = __builtin_amdgcn_readfirstlane(threadIdx.x >> 6);
  const int b = blockIdx.x;
  if (wave == 0) STAMP(1, 0);
  if (threadIdx.x == 0) *(volatile unsigned*)(smem + K2_CTR) = 0u;
  __syncthreads();
  if (b >= 240) {
    conv_channel(528 + (b - 240) * 7 + wave, smem + wave * CV_BYTES, ssmk, ssmd, ws, lane);
    STAMP(1, 2 + wave);
    return;
  }
  if (wave >= 4) {
    if (wave < 6 || b < 48) conv_channel(wave < 6 ? 2 * b + (wave - 4) : 480 + b, smem + K2_CONV_OFF + (wave - 4) * CV_BYTES, ssmk, ssmd, ws, lane);
    STAMP(1, 2 + wave);
    return;
  }
  __builtin_amdgcn_s_setprio(2);
  const int xg = b & 7, r = b >> 3;
  const int dt = (xg & 1) * 5 + r % 5, ft = (xg >> 1) * 6 + r / 5;
  const u4 srdH = make_srd(ws + WS_H, 2621440), srdW = make_srd(w1, 6291456);
  const unsigned ring = __builtin_amdgcn_readfirstlane((unsigned)(size_t)smem + wave * G1_RING);
  const unsigned voh = ((lane >> 4) * 640 + dt * 64 + (lane & 15)) * 16;
  unsigned vow[4];
#pragma unroll
  for (int t = 0; t < 4; ++t) vow[t] = (8 * t + (lane >> 3)) * 3072 + ft * 128 + (((lane & 7) ^ (2 * ((lane >> 4) & 1) + 4 * (t & 1))) * 16) - 1024 * t;
  unsigned tra[4];
  {
    const int g = lane >> 4, q = (lane >> 2) & 3, p = lane & 3, fsw = 2 * (q >> 1) + 4 * (g & 1);
#pragma unroll
    for (int m = 0; m < 4; ++m) tra[m] = ring + (8 * g + q) * 128 + (((2 * m) ^ fsw) + (p >> 1)) * 16 + (p & 1) * 8;
  }
  u4 hq[G1_D][4];
  f4 acc[4][4];
#pragma unroll
  for (int m = 0; m < 4; ++m)
#pragma unroll
    for (int n = 0; n < 4; ++n) acc[m][n] = f4{0.f, 0.f, 0.f, 0.f};

#define G1_ISSUE(i)                                                                                                          \
  do {                                                                                                                       \
    const unsigned ks_ = 4 * (i) + wave;                                                                                     \
    asm volatile("s_waitcnt lgkmcnt(0)\n\ts_mov_b32 m0, %0\n\ts_nop 0\n\t"                                                   \
                 "buffer_load_dwordx4 %1, %5, %6 offen" WPOL " lds\n\t"                                                      \
                 "buffer_load_dwordx4 %2, %5, %6 offen offset:1024" WPOL " lds\n\t"                                          \
                 "buffer_load_dwordx4 %3, %5, %6 offen offset:2048" WPOL " lds\n\t"                                          \
                 "buffer_load_dwordx4 %4, %5, %6 offen offset:3072" WPOL " lds"                                              \
                 ::"s"(ring + ((i) % G1_D) * 4096), "v"(vow[0]), "v"(vow[1]), "v"(vow[2]), "v"(vow[3]), "s"(srdW), "s"(ks_ * 98304u) \
                 : "memory");                                                                                                \
    asm volatile("buffer_load_dwordx4 %0, %4, %5, %6 offen\n\t"                                                              \
                 "buffer_load_dwordx4 %1, %4, %5, %6 offen offset:256\n\t"                                                   \
                 "buffer_load_dwordx4 %2, %4, %5, %6 offen offset:512\n\t"                                                   \
                 "buffer_load_dwordx4 %3, %4, %5, %6 offen offset:768"                                                       \
                 : "=&v"(hq[(i) % G1_D][0]), "=&v"(hq[(i) % G1_D][1]), "=&v"(hq[(i) % G1_D][2]), "=&v"(hq[(i) % G1_D][3])     \
                 : "v"(voh), "s"(srdH), "s"(ks_ * 40960u)                                                                    \
                 : "memory");                                                                                                \
  } while (0)

#pragma unroll
  for (int i = 0; i < G1_D - 1; ++i) G1_ISSUE(i);
#pragma unroll
  for (int i = 0; i < 16; ++i) {
    constexpr int dummy = 0; (void)dummy;
    const int younger = (15 - i < G1_D - 2) ? 15 - i : G1_D - 2;
    u4* hs = hq[i % G1_D];
#define G1_WAIT(N) asm volatile("s_waitcnt vmcnt(" #N ")" : "+v"(hs[0]), "+v"(hs[1]), "+v"(hs[2]), "+v"(hs[3])::"memory")
    if (younger == 3) G1_WAIT(24); else if (younger == 2) G1_WAIT(16); else if (younger == 1) G1_WAIT(8); else G1_WAIT(0);
    if (i + G1_D - 1 < 16) G1_ISSUE(i + G1_D - 1);
    bf8 wf[4];
#pragma unroll
    for (int m = 0; m < 4; ++m) wf[m] = cat8(tr_read(tra[m] + (i % G1_D) * 4096), tr_read(tra[m] + (i % G1_D) * 4096 + 512));
#pragma unroll
    for (int m = 0; m < 4; ++m)
#pragma unroll
      for (int n = 0; n < 4; ++n) acc[m][n] = mfma16(wf[m], __builtin_bit_cast(bf8, hs[n]), acc[m][n]);
  }
  static_assert(G1_D == 5, "wait ladder above assumes depth 5");
  STAMP(1, 9 + wave);
  u2 bias[4];
#pragma unroll
  for (int m = 0; m < 4; ++m) bias[m] = *(const u2*)(b1 + (ft * 64 + 16 * m + 4 * (lane >> 4)) * 2);
  char* myring = smem + wave * G1_RING;
#pragma unroll
  for (int m = 0; m < 4; ++m)
#pragma unroll
    for (int n = 0; n < 4; ++n) *(f4*)(myring + (m * 4 + n) * 1024 + lane * 16) = acc[m][n];
  {
    LAS unsigned* ctr = (LAS unsigned*)(size_t)(unsigned)(size_t)(smem + K2_CTR);
    __builtin_amdgcn_fence(__ATOMIC_RELEASE, "workgroup");
    if (lane == 0) __hip_atomic_fetch_add(ctr, 1u, __ATOMIC_RELAXED, __HIP_MEMORY_SCOPE_WORKGROUP);
    while (__hip_atomic_load(ctr, __ATOMIC_RELAXED, __HIP_MEMORY_SCOPE_WORKGROUP) < 4u) __builtin_amdgcn_s_sleep(1);
    __builtin_amdgcn_fence(__ATOMIC_ACQUIRE, "workgroup");
  }
  unsigned o[8];
#pragma unroll
  for (int m = 0; m < 4; ++m) {
    f4 t = *(const f4*)(smem + (m * 4 + wave) * 1024 + lane * 16);
#pragma unroll
    for (int w = 1; w < 4; ++w) t += *(const f4*)(smem + w * G1_RING + (m * 4 + wave) * 1024 + lane * 16);
    const unsigned a01 = pk(t[0], t[1]), a23 = pk(t[2], t[3]);
    const unsigned c01 = pk(blo(a01) + blo(bias[m].x), bhi(a01) + bhi(bias[m].x)), c23 = pk(blo(a23) + blo(bias[m].y), bhi(a23) + bhi(bias[m].y));
    o[2 * m] = pk(gelu_t(blo(c01)), gelu_t(bhi(c01)));
    o[2 * m + 1] = pk(gelu_t(blo(c23)), gelu_t(bhi(c23)));
  }
#pragma unroll
  for (int P = 0; P < 2; ++P) {
    u4 v = {o[4 * P], o[4 * P + 1], o[4 * P + 2], o[4 * P + 3]};
    *(u4*)(ws + WS_U + ((size_t)(ft * 2 + P) * 40 + dt * 4 + wave) * 1024 + lane * 16) = v;
  }
  STAMP(1, 2 + wave);
}

constexpr int G2_D = 6;
constexpr int G2_RING = G2_D * 4096;
constexpr int K3_YA = 4 * G2_RING;
constexpr int K3_LDS = K3_YA + 10240;

__global__ __launch_bounds__(256, 1) void k3_gemm2(const char* __restrict__ w2, const char* __restrict__ b2, const char* __restrict__ x,
                                                   char* __restrict__ out, char* __restrict__ ws) {
  extern __shared__ __attribute__((aligned(16))) char smem[];
  const int lane = threadIdx.x & 63, wave = __builtin_amdgcn_readfirstlane(threadIdx.x >> 6);
  const int b = blockIdx.x;
  if (wave == 0) STAMP(2, 0);
  const int xg = b & 7, r = b >> 3;
  const int dt = (xg & 1) * 4 + (r & 3), st = (xg >> 1) * 8 + (r >> 2);
  const u4 srdU = make_srd(ws + WS_U, 1966080), srdW = make_srd(w2, 6291456), srdY = make_srd(ws + WS_YA, 2621440);
  const unsigned sbase = __builtin_amdgcn_readfirstlane((unsigned)(size_t)smem);
  const unsigned ring = sbase + wave * G2_RING;
  const unsigned vou0 = dt * 5120 + lane * 16, vou1 = vou0 + 4096;
  unsigned vow[4];
#pragma unroll
  for (int t = 0; t < 4; ++t) vow[t] = (8 * t + (lane >> 3)) * 4096 + st * 128 + (((lane & 7) ^ ((lane >> 3) & 6)) * 16) - 1024 * t;
  unsigned tra[4];
  const int g = lane >> 4, q = (lane >> 2) & 3, p = lane & 3;
  {
    const int fsw = 4 * (g & 1) + (q & 2);
#pragma unroll
    for (int m = 0; m < 4; ++m) tra[m] = ring + (4 * g + q) * 128 + (((2 * m) ^ fsw) + (p >> 1)) * 16 + (p & 1) * 8;
  }
  {
    const unsigned voy = (dt * 80 + (lane >> 3)) * 4096 + st * 128 + (lane & 7) * 16;
    for (int i = wave; i < 10; i += 4)
      asm volatile("s_mov_b32 m0, %0\n\ts_nop 0\n\tbuffer_load_dwordx4 %1, %2, %3 offen lds" ::"s"(sbase + K3_YA + i * 1024), "v"(voy), "s"(srdY), "s"(i * 32768u) : "memory");
  }
  u4 uq[G2_D][5];
  f4 acc[5][4];
#pragma unroll
  for (int m = 0; m < 5; ++m)
#pragma unroll
    for (int n = 0; n < 4; ++n) acc[m][n] = f4{0.f, 0.f, 0.f, 0.f};

#define G2_ISSUE(i)                                                                                                          \
  do {                                                                                                                       \
    const unsigned ks_ = 4 * (i) + wave;                                                                                     \
    asm volatile("s_waitcnt lgkmcnt(0)\n\ts_mov_b32 m0, %0\n\ts_nop 0\n\t"                                                   \
                 "buffer_load_dwordx4 %1, %5, %6 offen" WPOL " lds\n\t"                                                      \
                 "buffer_load_dwordx4 %2, %5, %6 offen offset:1024" WPOL " lds\n\t"                                          \
                 "buffer_load_dwordx4 %3, %5, %6 offen offset:2048" WPOL " lds\n\t"                                          \
                 "buffer_load_dwordx4 %4, %5, %6 offen offset:3072" WPOL " lds"                                              \
                 ::"s"(ring + ((i) % G2_D) * 4096), "v"(vow[0]), "v"(vow[1]), "v"(vow[2]), "v"(vow[3]), "s"(srdW), "s"(ks_ * 131072u) \
                 : "memory");                                                                                                \
    asm volatile("buffer_load_dwordx4 %0, %5, %7, %8 offen\n\t"                                                              \
                 "buffer_load_dwordx4 %1, %5, %7, %8 offen offset:1024\n\t"                                                  \
                 "buffer_load_dwordx4 %2, %5, %7, %8 offen offset:2048\n\t"                                                  \
                 "buffer_load_dwordx4 %3, %5, %7, %8 offen offset:3072\n\t"                                                  \
                 "buffer_load_dwordx4 %4, %6, %7, %8 offen"                                                                  \
                 : "=&v"(uq[(i) % G2_D][0]), "=&v"(uq[(i) % G2_D][1]), "=&v"(uq[(i) % G2_D][2]), "=&v"(uq[(i) % G2_D][3]),   \
                   "=&v"(uq[(i) % G2_D][4])                                                                                  \
                 : "v"(vou0), "v"(vou1), "s"(srdU), "s"(ks_ * 40960u)                                                        \
                 : "memory");                                                                                                \
  } while (0)

#pragma unroll
  for (int i = 0; i < G2_D - 1; ++i) G2_ISSUE(i);
#pragma unroll
  for (int i = 0; i < 12; ++i) {
    const int younger = (11 - i < G2_D - 2) ? 11 - i : G2_D - 2;
    u4* us = uq[i % G2_D];
#define G2_WAIT(N) asm volatile("s_waitcnt vmcnt(" #N ")" : "+v"(us[0]), "+v"(us[1]), "+v"(us[2]), "+v"(us[3]), "+v"(us[4])::"memory")
    if (younger == 4) G2_WAIT(36); else if (younger == 3) G2_WAIT(27); else if (younger == 2) G2_WAIT(18); else if (younger == 1) G2_WAIT(9); else G2_WAIT(0);
    if (i + G2_D - 1 < 12) G2_ISSUE(i + G2_D - 1);
    bf8 wf[4];
#pragma unroll
    for (int n = 0; n < 4; ++n) wf[n] = cat8(tr_read(tra[n] + (i % G2_D) * 4096), tr_read(tra[n] + (i % G2_D) * 4096 + 2048));
#pragma unroll
    for (int m = 0; m < 5; ++m)
#pragma unroll
      for (int n = 0; n < 4; ++n) acc[m][n] = mfma16(__builtin_bit_cast(bf8, us[m]), wf[n], acc[m][n]);
  }
  static_assert(G2_D == 6, "wait ladder above assumes depth 6");
  STAMP(2, 9 + wave);
  const int s = st * 64 + wave * 16 + (lane & 15);
  u2 xr[5];
#pragma unroll
  for (int m = 0; m < 5; ++m) xr[m] = *(const u2*)(x + (size_t)s * 1280 + (dt * 80 + 16 * m + 4 * g) * 2);
  const f2 stt = *(const f2*)(ws + WS_ST + s * 8);
  const float bb = blo(*(const unsigned short*)(b2 + s * 2));
  char* myring = smem + wave * G2_RING;
#pragma unroll
  for (int m = 0; m < 5; ++m)
#pragma unroll
    for (int n = 0; n < 4; ++n) *(f4*)(myring + (m * 4 + n) * 1024 + lane * 16) = acc[m][n];
  __syncthreads();
  const unsigned yab = sbase + K3_YA + (4 * g + q) * 128 + wave * 32 + p * 8;
#pragma unroll
  for (int m = 0; m < 5; ++m) {
    f4 t = *(const f4*)(smem + (m * 4 + wave) * 1024 + lane * 16);
#pragma unroll
    for (int w = 1; w < 4; ++w) t += *(const f4*)(smem + w * G2_RING + (m * 4 + wave) * 1024 + lane * 16);
    const u2 ya = __builtin_bit_cast(u2, tr_read(yab + m * 2048));
    unsigned res[2];
#pragma unroll
    for (int h = 0; h < 2; ++h) {
      const unsigned xx = h ? xr[m].y : xr[m].x, yy = h ? ya.y : ya.x;
      unsigned bv = pk(t[2 * h], t[2 * h + 1]);
      bv = pk(blo(bv) + bb, bhi(bv) + bb);
      unsigned xn = pk(blo(xx) - stt.x, bhi(xx) - stt.x);
      xn = pk(blo(xn) * stt.y, bhi(xn) * stt.y);
      unsigned a = pk(blo(xn) * blo(yy), bhi(xn) * bhi(yy));
      a = pk(blo(xx) + blo(a), bhi(xx) + bhi(a));
      res[h] = pk(blo(a) + blo(bv), bhi(a) + bhi(bv));
    }
    u2 o = {res[0], res[1]};
    *(u2*)(out + (size_t)s * 1280 + (dt * 80 + 16 * m + 4 * g) * 2) = o;
  }
  STAMP(2, 2 + wave);
}

extern "C" void kernel_launch(void* const* d_in, const int* in_sizes, int n_in, void* d_out, int out_size, void* d_ws, size_t ws_size,
                              hipStream_t stream) {
  const char* q = (const char*)d_in[0];
  const char* k = (const char*)d_in[1];
  const char* v = (const char*)d_in[2];
  const char* shk = (const char*)d_in[3];
  const char* ssmk = (const char*)d_in[4];
  const char* ssmd = (const char*)d_in[5];
  const char* w1 = (const char*)d_in[7];
  const char* b1 = (const char*)d_in[8];
  const char* w2 = (const char*)d_in[9];
  const char* b2 = (const char*)d_in[10];
  char* ws = (char*)d_ws;
  (void)hipFuncSetAttribute((const void*)k2_gemm1_conv, hipFuncAttributeMaxDynamicSharedMemorySize, K2_LDS);
  (void)hipFuncSetAttribute((const void*)k3_gemm2, hipFuncAttributeMaxDynamicSharedMemorySize, K3_LDS);
  k1_prep<<<K1_LN_BLOCKS + K1_KV_BLOCKS, 512, 0, stream>>>(q, k, v, shk, ws);
  k2_gemm1_conv<<<256, 448, K2_LDS, stream>>>(w1, b1, ssmk, ssmd, ws);
  k3_gemm2<<<256, 256, K3_LDS, stream>>>(w2, b2, q, (char*)d_out, ws);
}
```

```cpp
#include <hip/hip_runtime.h>
#include <hip/hip_bf16.h>
#include <stdint.h>

typedef __attribute__((ext_vector_type(8))) __bf16 bf8;
typedef __attribute__((ext_vector_type(2))) __bf16 bf2;
typedef __attribute__((ext_vector_type(4))) short s4;
typedef __attribute__((ext_vector_type(4))) float f4;
typedef __attribute__((ext_vector_type(2))) float f2;
typedef __attribute__((ext_vector_type(4))) unsigned u4;
typedef __attribute__((ext_vector_type(2))) unsigned u2;
#define LAS __attribute__((address_space(3)))
#define DEVI __device__ __forceinline__
#define SCHED_FENCE() __builtin_amdgcn_sched_barrier(0)

#define PF_W2 1
#define NT_FIRST 2

constexpr unsigned WS_EPOCH = 0;
constexpr unsigned WS_F1 = 4096;
constexpr unsigned WS_F2 = WS_F1 + 32768;
constexpr unsigned WS_F3 = WS_F2 + 32768;
constexpr unsigned WS_ST = WS_F3 + 8192;
constexpr unsigned WS_H = WS_ST + 16384;
constexpr unsigned WS_KV = WS_H + 2621440;
constexpr unsigned WS_YA = WS_KV + 2621440;
constexpr unsigned WS_U = WS_YA + 2621440;
constexpr unsigned WS_END = WS_U + 1966080;
constexpr size_t WS_DBG = WS_END;

#define STAMP(idx) do {} while (0)

constexpr unsigned L_RING = 28672;
constexpr unsigned CV_CP = 4160;
constexpr unsigned CV_X = 4 * CV_CP;
constexpr unsigned CV_BYTES = 21248;
constexpr unsigned L_CONV = 4 * L_RING;
constexpr unsigned L_HBUF = L_CONV;
constexpr unsigned L_KBUF = L_CONV + 10752;
constexpr unsigned L_VBUF = L_KBUF + 11264;
constexpr unsigned L_YA = L_CONV + CV_BYTES;
constexpr unsigned L_CTL = L_CONV + 2 * CV_BYTES;
constexpr unsigned L_TOTAL = L_CTL + 128;
enum { C_H = 0, C_GO1, C_G1, C_G2, C_G3, C_GO2, C_P0, C_P1, C_TAG0, C_TAG1 };
constexpr unsigned SPIN_CAP = 1u << 16;

DEVI float blo(unsigned u) { return __uint_as_float(u << 16); }
DEVI float bhi(unsigned u) { return __uint_as_float(u & 0xffff0000u); }
DEVI unsigned pk(float a, float b) { f2 v = {a, b}; return __builtin_bit_cast(unsigned, __builtin_convertvector(v, bf2)); }
DEVI float rbf(float a) { return blo(pk(a, a)); }
template <int CTRL, int RM> DEVI float dppf(float x) {
  return __builtin_bit_cast(float, __builtin_amdgcn_update_dpp(0, __builtin_bit_cast(int, x), CTRL, RM, 0xf, true));
}
template <class T> DEVI T ldnt(const void* p) { return __builtin_nontemporal_load((const T*)p); }
DEVI u4 make_srd(const void* p, unsigned bytes) {
  uint64_t a = (uint64_t)p;
  u4 r = {(unsigned)a, (unsigned)(a >> 32) & 0xffffu, bytes, 0x00020000u};
  return r;
}
DEVI s4 tr_read(unsigned addr) { return __builtin_amdgcn_ds_read_tr16_b64_v4i16((LAS s4*)(size_t)addr); }
DEVI bf8 cat8(s4 a, s4 b) {
  typedef __attribute__((ext_vector_type(8))) short s8;
  s8 v = __builtin_shufflevector(a, b, 0, 1, 2, 3, 4, 5, 6, 7);
  return __builtin_bit_cast(bf8, v);
}
DEVI f4 mfma16(bf8 a, bf8 b, f4 c) { return __builtin_amdgcn_mfma_f32_16x16x32_bf16(a, b, c, 0, 0, 0); }
template <int N> DEVI void waitv() { asm volatile("s_waitcnt vmcnt(%0)" ::"i"(N) : "memory"); }
template <class T> DEVI T lds_ld(unsigned a) { return *(const LAS T*)(size_t)a; }
template <class T> DEVI void lds_st(unsigned a, T v) { *(LAS T*)(size_t)a = v; }
DEVI void raw_barrier() { asm volatile("s_waitcnt lgkmcnt(0)\n\ts_barrier" ::: "memory"); }
DEVI unsigned ctr_arrive(unsigned a, int lane) {
  asm volatile("s_waitcnt lgkmcnt(0)" ::: "memory");
  unsigned old = 0;
  if (lane == 0) old = __hip_atomic_fetch_add((LAS unsigned*)(size_t)a, 1u, __ATOMIC_RELAXED, __HIP_MEMORY_SCOPE_WORKGROUP);
  old = __builtin_amdgcn_readfirstlane(old);
  asm volatile("" ::: "memory");
  return old;
}
DEVI void ctr_wait(unsigned a, unsigned n) {
  while (__builtin_amdgcn_readfirstlane(__hip_atomic_load((LAS unsigned*)(size_t)a, __ATOMIC_RELAXED, __HIP_MEMORY_SCOPE_WORKGROUP)) < n)
    __builtin_amdgcn_s_sleep(1);
  asm volatile("" ::: "memory");
}
typedef __amdgpu_buffer_rsrc_t rsrc_t;
DEVI u4 ld16_sc1(rsrc_t r, unsigned off) { return __builtin_amdgcn_raw_buffer_load_b128(r, off, 0, 16); }
DEVI u2 ld8_sc1(rsrc_t r, unsigned off) { return __builtin_amdgcn_raw_buffer_load_b64(r, off, 0, 16); }
DEVI void st16_sc1(rsrc_t r, unsigned off, u4 v) { __builtin_amdgcn_raw_buffer_store_b128(v, r, off, 0, 16); }
DEVI void st8_sc1(rsrc_t r, unsigned off, u2 v) { __builtin_amdgcn_raw_buffer_store_b64(v, r, off, 0, 16); }

DEVI f4 sel4(bool c, f4 a, f4 b) {
  asm("" : "+v"(a)); asm("" : "+v"(b));
  return c ? a : b;
}
DEVI float gelu_t(float x) {
  const float t = x * __builtin_fmaf(x * x, -0.10294324f, -2.3022082f);
  return x * __builtin_amdgcn_rcpf(1.f + __builtin_amdgcn_exp2f(t));
}

DEVI void ln_row(const unsigned (&xr)[5], unsigned (&hp)[5], float& m1, float& inv1) {
  float e[10], s = 0.f, q = 0.f;
  const float piv = blo(__builtin_amdgcn_readfirstlane(xr[0]));
#pragma unroll
  for (int j = 0; j < 5; ++j) { e[2 * j] = blo(xr[j]); e[2 * j + 1] = bhi(xr[j]); }
#pragma unroll
  for (int j = 0; j < 10; ++j) { const float d = e[j] - piv; s += d; q = __builtin_fmaf(d, d, q); }
  s += dppf<0xB1, 0xf>(s);  q += dppf<0xB1, 0xf>(q);
  s += dppf<0x4E, 0xf>(s);  q += dppf<0x4E, 0xf>(q);
  s += dppf<0x141, 0xf>(s); q += dppf<0x141, 0xf>(q);
  s += dppf<0x140, 0xf>(s); q += dppf<0x140, 0xf>(q);
  s += dppf<0x142, 0xa>(s); q += dppf<0x142, 0xa>(q);
  s += dppf<0x143, 0xc>(s); q += dppf<0x143, 0xc>(q);
  const float md = __builtin_bit_cast(float, __builtin_amdgcn_readlane(__builtin_bit_cast(int, s), 63)) * (1.f / 640.f);
  const float qq = __builtin_bit_cast(float, __builtin_amdgcn_readlane(__builtin_bit_cast(int, q), 63)) * (1.f / 640.f);
  const float var = __builtin_fmaxf(qq - md * md, 0.f), mean = piv + md;
  m1 = rbf(mean);
  inv1 = __builtin_amdgcn_rcpf(rbf(__builtin_amdgcn_sqrtf(rbf(rbf(var) + 1e-5f))));
  const float m2 = rbf((mean - m1) * inv1);
  const float inv2 = __builtin_amdgcn_rcpf(rbf(__builtin_amdgcn_sqrtf(rbf(rbf(var * inv1 * inv1) + 1e-5f))));
  const float A = inv1 * inv2, B = -m2 * inv2;
#pragma unroll
  for (int j = 0; j < 5; ++j) hp[j] = pk(__builtin_fmaf(e[2 * j] - m1, A, B), __builtin_fmaf(e[2 * j + 1] - m1, A, B));
}

struct Filt { u4 c[2]; u2 n[2]; };
DEVI void filt_load(Filt& f, const char* krow, int half, int lane) {
#pragma unroll
  for (int j = 0; j < 2; ++j) {
    const int z = lane + 64 * j + 128 * half;
    f.c[j] = ldnt<u4>(krow + z * 16);
    f.n[j] = ldnt<u2>(krow + (z < 255 ? z * 16 + 16 : 0));
  }
}
DEVI void filt_build(Filt& f, unsigned cv, int half, int lane) {
  const u4 zero = {0u, 0u, 0u, 0u};
  if (half == 1) {
    if (lane == 63) f.n[1] = u2{0u, 0u};
    if (lane < 30) lds_st<u4>(cv + CV_X + 4096 + lane * 16, zero);
  } else {
    if (lane < 4) lds_st<u4>(cv + lane * CV_CP, zero);
    if (lane == 0) {
      const unsigned d4 = f.c[0].x, d5 = f.c[0].y;
      lds_st<u4>(cv + 16, u4{0u, 0u, 0u, d4 << 16});
      lds_st<u4>(cv + CV_CP + 16, u4{0u, 0u, 0u, d4});
      lds_st<u4>(cv + 2 * CV_CP + 16, u4{0u, 0u, d4 << 16, __builtin_amdgcn_alignbit(d5, d4, 16)});
      lds_st<u4>(cv + 3 * CV_CP + 16, u4{0u, 0u, d4, d5});
    }
  }
#pragma unroll
  for (int j = 0; j < 2; ++j) {
    const u4 c = f.c[j]; const u2 n = f.n[j];
    const unsigned a0 = __builtin_amdgcn_alignbit(c.y, c.x, 16), a1 = __builtin_amdgcn_alignbit(c.z, c.y, 16), a2 = __builtin_amdgcn_alignbit(c.w, c.z, 16),
                   a3 = __builtin_amdgcn_alignbit(n.x, c.w, 16), a4 = __builtin_amdgcn_alignbit(n.y, n.x, 16);
    const unsigned p = cv + (lane + 64 * j + 128 * half + 2) * 16;
    lds_st<u4>(p, u4{a0, a1, a2, a3});
    lds_st<u4>(p + CV_CP, u4{c.y, c.z, c.w, n.x});
    lds_st<u4>(p + 2 * CV_CP, u4{a1, a2, a3, a4});
    lds_st<u4>(p + 3 * CV_CP, u4{c.z, c.w, n.x, n.y});
  }
}

template <int M0, int NM>
DEVI void final_tiles(f4 (&own)[NM], const u2 (&xr)[NM], u2 stt, unsigned b2r, unsigned yab, char* op) {
  const float bb = blo(b2r), m1 = __uint_as_float(stt.x), inv1 = __uint_as_float(stt.y);
#pragma unroll
  for (int m = 0; m < NM; ++m) {
    const u2 ya = __builtin_bit_cast(u2, tr_read(yab + (M0 + m) * 2048));
    const float x0 = blo(xr[m].x), x1 = bhi(xr[m].x), x2 = blo(xr[m].y), x3 = bhi(xr[m].y);
    const float r0 = __builtin_fmaf((x0 - m1) * inv1, blo(ya.x), x0) + (own[m][0] + bb), r1 = __builtin_fmaf((x1 - m1) * inv1, bhi(ya.x), x1) + (own[m][1] + bb);
    const float r2 = __builtin_fmaf((x2 - m1) * inv1, blo(ya.y), x2) + (own[m][2] + bb), r3 = __builtin_fmaf((x3 - m1) * inv1, bhi(ya.y), x3) + (own[m][3] + bb);
    *(u2*)(op + (M0 + m) * 32) = u2{pk(r0, r1), pk(r2, r3)};
  }
}

__global__ __launch_bounds__(512, 2) __attribute__((amdgpu_waves_per_eu(2, 2))) void fused_kernel(
    const char* __restrict__ x, const char* __restrict__ kk, const char* __restrict__ vv, const char* __restrict__ shk,
    const char* __restrict__ ssmk, const char* __restrict__ ssmd, const char* __restrict__ w1, const char* __restrict__ b1,
    const char* __restrict__ w2, const char* __restrict__ b2, char* __restrict__ out, char* ws) {
  extern __shared__ __attribute__((aligned(16))) char smem[];
  const int lane = threadIdx.x & 63, wave = __builtin_amdgcn_readfirstlane(threadIdx.x >> 6);
  const int b = blockIdx.x, xg = b & 7, r = b >> 3;
  const unsigned sb = __builtin_amdgcn_readfirstlane((unsigned)(size_t)smem);
  const unsigned ctl = sb + L_CTL;
  asm volatile("" ::"s"(x), "s"(kk), "s"(vv), "s"(shk), "s"(ssmk), "s"(ssmd), "s"(w1), "s"(b1), "s"(w2), "s"(b2), "s"(out), "s"(ws));
  const rsrc_t rws = __builtin_amdgcn_make_buffer_rsrc(ws, 0, WS_END, 0x00020000);
  if (wave == 0) STAMP(0);
  const int dt2 = r & 7, st = xg * 4 + (r >> 3);

  const u4 srdS = make_srd(ws, WS_END);
  u2 tag;
  {
    unsigned xr[5];
    asm volatile("buffer_load_dword %0, %5, %6, %7 offen nt\n\tbuffer_load_dword %1, %5, %6, %7 offen offset:256 nt\n\t"
                 "buffer_load_dword %2, %5, %6, %7 offen offset:512 nt\n\tbuffer_load_dword %3, %5, %6, %7 offen offset:768 nt\n\t"
                 "buffer_load_dword %4, %5, %6, %7 offen offset:1024 nt"
                 : "=&v"(xr[0]), "=&v"(xr[1]), "=&v"(xr[2]), "=&v"(xr[3]), "=&v"(xr[4])
                 : "v"(lane * 4), "s"(make_srd(x, 2621440)), "s"((b * 8 + wave) * 1280)
                 : "memory");
    {
      const u4 srdK = make_srd(kk, 2621440), srdV = make_srd(vv, 2621440);
#pragma unroll
      for (int i = 0; i < 3; ++i) {
        const int pc = wave + 8 * i;
        if (pc < 22) {
          const bool isv = pc >= 11;
          const int n = (isv ? pc - 11 : pc) * 64 + lane, g8 = n / 81, rem = n - g8 * 81, rr = rem / 10;
          const int row = g8 * 8 + rr, c = rem - rr * 10;
          const int grow = r * 64 + row - (isv ? 0 : 3);
          const bool ok = rem < 80 && grow >= 0 && row < (isv ? 64 : 67);
          const unsigned vo = ok ? grow * 1280 + xg * 160 + c * 16 : 0x7fffff00u;
          const unsigned dst = sb + (isv ? L_VBUF - 11264 : L_KBUF) + pc * 1024;
          if (isv) asm volatile("s_mov_b32 m0, %0\n\ts_nop 0\n\tbuffer_load_dwordx4 %1, %2, 0 offen nt lds" ::"s"(dst), "v"(vo), "s"(srdV) : "memory");
          else     asm volatile("s_mov_b32 m0, %0\n\ts_nop 0\n\tbuffer_load_dwordx4 %1, %2, 0 offen nt lds" ::"s"(dst), "v"(vo), "s"(srdK) : "memory");
        }
      }
    }
    u2 ep = ld8_sc1(rws, WS_EPOCH);
    u4 shw = *(const u4*)(shk + (xg * 80 + (wave >= 3 ? wave - 3 : 0) * 16 + (lane >> 3) * 2) * 8);
    SCHED_FENCE();
    if (wave == 0 && lane < 8) lds_st<unsigned>(ctl + lane * 4, 0u);
    asm volatile("s_waitcnt vmcnt(4)" : "+v"(xr[0]), "+v"(xr[1]), "+v"(xr[2]), "+v"(xr[3]), "+v"(xr[4])::"memory");
    STAMP(1);
    unsigned hp[5]; float m1, inv1;
    ln_row(xr, hp, m1, inv1);
#pragma unroll
    for (int j = 0; j < 5; ++j) lds_st<unsigned>(sb + L_HBUF + wave * 1344 + (lane + 64 * j) * 4, hp[j]);
    STAMP(2);
    asm volatile("s_waitcnt vmcnt(0)" : "+v"(ep), "+v"(shw)::"memory");
    const uint64_t tg = (((uint64_t)ep.y << 32) | ep.x) * 6364136223846793005ull + 1442695040888963407ull;
    tag = u2{(unsigned)__builtin_amdgcn_readfirstlane((unsigned)tg), (unsigned)__builtin_amdgcn_readfirstlane((unsigned)(tg >> 32))};
    STAMP(3);
    raw_barrier();
    if (lane == 0) st8_sc1(rws, WS_ST + (b * 8 + wave) * 8, u2{__float_as_uint(m1), __float_as_uint(inv1)});
    if (wave < 3) {
      const unsigned tb = sb + L_HBUF + ((lane >> 2) & 3) * 1344 + (lane >> 4) * 32 + (lane & 3) * 8;
      u4 v[4];
#pragma unroll
      for (int i = 0; i < 4; ++i) { const int it = i < 3 ? wave + 3 * i : 9; v[i] = __builtin_bit_cast(u4, cat8(tr_read(tb + it * 128), tr_read(tb + it * 128 + 4 * 1344))); }
#pragma unroll
      for (int i = 0; i < 4; ++i)
        if (i < 3 || wave == 0) st16_sc1(rws, WS_H + (b * 640 + (i < 3 ? wave + 3 * i : 9) * 64 + lane) * 16, v[i]);
    } else {
      const int rg = lane & 7, cp = (wave - 3) * 8 + (lane >> 3);
      const unsigned ka = sb + L_KBUF + rg * 1296 + cp * 4;
      unsigned kr[11], vr[8];
#pragma unroll
      for (int i = 0; i < 11; ++i) kr[i] = lds_ld<unsigned>(ka + i * 160 + (i >> 3) * 16);
#pragma unroll
      for (int i = 0; i < 8; ++i) vr[i] = lds_ld<unsigned>(ka + 11264 + i * 160);
#pragma unroll
      for (int c = 0; c < 2; ++c) {
        const unsigned wa = c ? shw.z : shw.x, wb = c ? shw.w : shw.y;
        const float w0 = blo(wa), w1f = bhi(wa), w2f = blo(wb), w3 = bhi(wb);
        float kf[11], o[8];
#pragma unroll
        for (int i = 0; i < 11; ++i) kf[i] = c ? bhi(kr[i]) : blo(kr[i]);
#pragma unroll
        for (int i = 0; i < 8; ++i) o[i] = (kf[i] * w3 + kf[i + 1] * w2f + kf[i + 2] * w1f + kf[i + 3] * w0) * (c ? bhi(vr[i]) : blo(vr[i]));
        st16_sc1(rws, WS_KV + (xg * 80 + cp * 2 + c) * 4096 + (2040 - 64 * r - 8 * rg) * 2, u4{pk(o[7], o[6]), pk(o[5], o[4]), pk(o[3], o[2]), pk(o[1], o[0])});
      }
    }
    STAMP(4);
  }

  if (wave >= 4) {
    const int hw = wave - 4, pair = hw >> 1, half = hw & 1;
    Filt f;
    filt_load(f, ssmk + (size_t)(2 * b + pair) * 4096, half, lane);
    waitv<4>();
    if (ctr_arrive(ctl + C_H * 4, lane) == 7 && lane < 8) st8_sc1(rws, WS_F1 + lane * 4096 + b * 16, tag);
    STAMP(20 + hw);
    ctr_wait(ctl + C_H * 4, 8);
    const unsigned cv = sb + L_CONV + pair * CV_BYTES;
    filt_build(f, cv, half, lane);
    if (hw == 3) {
#pragma unroll 1
      for (unsigned n = 0; n < SPIN_CAP; ++n) {
        bool ok = true;
#pragma unroll
        for (int i = 0; i < 4; ++i) { const u2 a = ld8_sc1(rws, WS_F1 + xg * 4096 + i * 1024 + lane * 16); ok = ok && a.x == tag.x && a.y == tag.y; }
        if (__all(ok)) break;
        asm volatile("" ::: "memory");
      }
      asm volatile("" ::: "memory");
      if (lane == 0) lds_st<unsigned>(ctl + C_GO1 * 4, 1u);
      if (b == 0 && lane == 0) st8_sc1(rws, WS_EPOCH, tag);
      STAMP(24);
    } else {
      ctr_wait(ctl + C_GO1 * 4, 1);
    }
    const int nrounds = (pair == 0 && b >= 128) ? 2 : 1;
    const int c16 = lane & 15, kg = lane >> 4;
    for (int round = 0; round < nrounds; ++round) {
      const int ch = round == 0 ? 2 * b + pair : 384 + b;
      const unsigned pc = ctl + (C_P0 + pair) * 4, pbase = round * 8;
      if (round) filt_load(f, ssmk + (size_t)ch * 4096, half, lane);
      u4 xv[2];
#pragma unroll
      for (int j = 0; j < 2; ++j) xv[j] = ld16_sc1(rws, WS_KV + ch * 4096 + (lane + 64 * j + 128 * half) * 16);
      const float dd = blo(*(const unsigned short*)(ssmd + ch * 2));
      if (round) filt_build(f, cv, half, lane);
#pragma unroll
      for (int j = 0; j < 2; ++j) lds_st<u4>(cv + CV_X + (lane + 64 * j + 128 * half) * 16, xv[j]);
      ctr_arrive(pc, lane);
      ctr_wait(pc, pbase + 2);
      const unsigned ab = cv + (c16 & 3) * CV_CP + (c16 >> 2) * 8 + kg * 16 + half * 256;
      const unsigned bb = cv + CV_X + 480 - 32 * c16 + 16 * kg + half * 256;
      f4 acc[8];
#pragma unroll
      for (int n = 0; n < 8; ++n) acc[n] = f4{0.f, 0.f, 0.f, 0.f};
      bf8 A0[8], B0[8], A1[8], B1[8];
#define CV_LOAD(A, B, q)                                                                              \
  _Pragma("unroll") for (int p = 0; p < 8; ++p) {                                                      \
    B[p] = *(const volatile LAS bf8*)(size_t)(bb + 3584 - 512 * p + 64 * (q));                          \
    s4 lo = *(const volatile LAS s4*)(size_t)(ab + 512 * p + 64 * (q));                                 \
    s4 hi = *(const volatile LAS s4*)(size_t)(ab + 512 * p + 64 * (q) + 8);                             \
    A[p] = cat8(lo, hi);                                                                               \
  }
#define CV_MMA(A, B)                                                                                   \
  _Pragma("unroll") for (int p = 0; p < 8; ++p)                                                        \
  _Pragma("unroll") for (int m = 0; m + p < 8; ++m) acc[p + m] = mfma16(A[p], B[m], acc[p + m]);
#define CV_SCHED()                                                                                     \
  _Pragma("unroll") for (int z = 0; z < 24; ++z) {                                                     \
    __builtin_amdgcn_sched_group_barrier(0x8, 1, 0);                                                   \
    __builtin_amdgcn_sched_group_barrier(0x100, 1, 0);                                                 \
  }                                                                                                    \
  __builtin_amdgcn_sched_group_barrier(0x8, 12, 0);                                                    \
  SCHED_FENCE();
      CV_LOAD(A0, B0, 0);
      SCHED_FENCE();
#pragma unroll 1
      for (int q = 0; q < 4; q += 2) {
        CV_LOAD(A1, B1, q + 1); CV_MMA(A0, B0); CV_SCHED();
        CV_LOAD(A0, B0, (q + 2) & 3); CV_MMA(A1, B1); CV_SCHED();
      }
      if (round == 0) STAMP(25 + hw);
      ctr_arrive(pc, lane);
      ctr_wait(pc, pbase + 4);
#pragma unroll
      for (int n = 0; n < 4; ++n) lds_st<f4>(cv + half * 4096 + n * 1024 + lane * 16, sel4(half, acc[n], acc[n + 4]));
      ctr_arrive(pc, lane);
      ctr_wait(pc, pbase + 6);
#pragma unroll
      for (int n = 0; n < 4; ++n) {
        const f4 y = sel4(half, acc[n + 4], acc[n]) + lds_ld<f4>(cv + (half ^ 1) * 4096 + n * 1024 + lane * 16);
        const int t0 = 1024 * half + 256 * n + 16 * c16 + 4 * kg;
        const u2 xq = lds_ld<u2>(cv + CV_X + (2044 - t0) * 2);
        lds_st<u2>(cv + 8192 + t0 * 2, u2{pk(__builtin_fmaf(bhi(xq.y), dd, y[0]), __builtin_fmaf(blo(xq.y), dd, y[1])),
                                          pk(__builtin_fmaf(bhi(xq.x), dd, y[2]), __builtin_fmaf(blo(xq.x), dd, y[3]))});
      }
#pragma unroll
      for (int n = 0; n < 2; ++n) {
        const unsigned o = half * 2048 + n * 1024 + lane * 16;
        st16_sc1(rws, WS_YA + ch * 4096 + o, lds_ld<u4>(cv + 8192 + o));
      }
      waitv<0>();
      if (ctr_arrive(pc, lane) == pbase + 7 && lane == 0) st8_sc1(rws, WS_F3 + ch * 8, tag);
      ctr_wait(pc, pbase + 8);
    }
    STAMP(32 + hw);
    const int s = st * 64 + hw * 16 + c16;
    const size_t xo = (size_t)s * 1280 + (dt2 * 80 + 4 * kg) * 2;
    u2 xq[2] = {*(const u2*)(x + xo + 96), *(const u2*)(x + xo + 128)};
    u2 stt = ld8_sc1(rws, WS_ST + s * 8);
    unsigned b2r = *(const unsigned short*)(b2 + s * 2);
    asm volatile("" : "+v"(xq[0]), "+v"(xq[1]), "+v"(stt), "+v"(b2r));
    if (hw == 3) {
      const int dta = (dt2 * 5) >> 2;
#pragma unroll 1
      for (unsigned n = 0; n < SPIN_CAP; ++n) {
        const u2 a = ld8_sc1(rws, WS_F2 + xg * 4096 + (dta * 24 + (lane < 48 ? lane : 0)) * 16);
        if (__all(a.x == tag.x && a.y == tag.y)) break;
        asm volatile("" ::: "memory");
      }
      asm volatile("" ::: "memory");
      if (lane == 0) lds_st<unsigned>(ctl + C_GO2 * 4, 1u);
      STAMP(36);
#pragma unroll 1
      for (unsigned n = 0; n < SPIN_CAP; ++n) {
        const u4 a = ld16_sc1(rws, WS_F3 + dt2 * 640 + (lane < 40 ? lane : 0) * 16);
        if (__all(a.x == tag.x && a.y == tag.y && a.z == tag.x && a.w == tag.y)) break;
        __builtin_amdgcn_s_sleep(2);
        asm volatile("" ::: "memory");
      }
      asm volatile("" ::: "memory");
      STAMP(37);
      u4 y[10];
#pragma unroll
      for (int i = 0; i < 10; ++i) y[i] = ld16_sc1(rws, WS_YA + (dt2 * 80 + i * 8 + (lane >> 3)) * 4096 + st * 128 + (lane & 7) * 16);
      SCHED_FENCE();
#pragma unroll
      for (int i = 0; i < 10; ++i) lds_st<u4>(sb + L_YA + i * 1024 + lane * 16, y[i]);
      STAMP(38);
    }
    raw_barrier();
    f4 own[2];
#pragma unroll
    for (int m = 0; m < 2; ++m) {
      const unsigned a = sb + (hw * 5 + 3 + m) * 1024 + lane * 16;
      own[m] = (lds_ld<f4>(a) + lds_ld<f4>(a + L_RING)) + (lds_ld<f4>(a + 2 * L_RING) + lds_ld<f4>(a + 3 * L_RING));
    }
    final_tiles<3, 2>(own, xq, stt, b2r, sb + L_YA + (4 * kg + ((lane >> 2) & 3)) * 128 + hw * 32 + (lane & 3) * 8, out + xo);
    return;
  }

  const int g = lane >> 4, q = (lane >> 2) & 3, p = lane & 3;
  const unsigned ring = sb + wave * L_RING;
  const bool has1 = r < 30;
  const int dt = r % 10, ft = xg * 3 + r / 10;
  const int rot1 = dt * 16 / 10, rot2 = dt2 * 12 / 8;
  const u4 srdW1 = make_srd(w1, 6291456), srdW2 = make_srd(w2, 6291456);
  unsigned vow[4];
#define DMA4(POL, slot, srd, soff)                                                                                             \
  asm volatile("s_mov_b32 m0, %0\n\ts_nop 0\n\t"                                                                               \
               "buffer_load_dwordx4 %1, %5, %6 offen" POL " lds\n\t"                                                           \
               "buffer_load_dwordx4 %2, %5, %6 offen offset:1024" POL " lds\n\t"                                               \
               "buffer_load_dwordx4 %3, %5, %6 offen offset:2048" POL " lds\n\t"                                               \
               "buffer_load_dwordx4 %4, %5, %6 offen offset:3072" POL " lds" ::"s"(ring + (slot) * 4096),                      \
               "v"(vow[0]), "v"(vow[1]), "v"(vow[2]), "v"(vow[3]), "s"(srd), "s"(soff)                                         \
               : "memory")
#define KS1(i) (4u * (((i) + rot1) & 15) + wave)
#define KS2(j) (4u * ((j) + rot2 >= 12 ? (j) + rot2 - 12 : (j) + rot2) + wave)
#define W1_DMA(i) do { if ((i) < NT_FIRST) DMA4(" nt", (i) % 7, srdW1, KS1(i) * 98304u); else DMA4("", (i) % 7, srdW1, KS1(i) * 98304u); } while (0)
#define W2_DMA(j) do { if (!PF_W2 && (j) < NT_FIRST) DMA4(" nt", ((j) + 2) % 7, srdW2, KS2(j) * 131072u); else DMA4("", ((j) + 2) % 7, srdW2, KS2(j) * 131072u); } while (0)
#pragma unroll
  for (int t = 0; t < 4; ++t) vow[t] = (8 * t + (lane >> 3)) * 3072 + ft * 128 + (((lane & 7) ^ (2 * ((lane >> 4) & 1) + 4 * (t & 1))) * 16) - 1024 * t;
  waitv<0>();
  if (ctr_arrive(ctl + C_H * 4, lane) == 7 && lane < 8) st8_sc1(rws, WS_F1 + lane * 4096 + b * 16, tag);
  STAMP(5 + wave);
  ctr_wait(ctl + C_H * 4, 8);
  if (has1) {
    W1_DMA(0); W1_DMA(1);
    waitv<4>(); W1_DMA(2); waitv<4>(); W1_DMA(3); waitv<4>(); W1_DMA(4); waitv<4>(); W1_DMA(5); waitv<4>(); W1_DMA(6);
  }
  unsigned pfv = 0;
  if (PF_W2 && wave < 3) {
    const int j = r * 192 + wave * 64 + lane;
    pfv = ldnt<unsigned>(w2 + (size_t)(j >> 2) * 4096 + xg * 512 + (j & 3) * 128);
  }
  __builtin_amdgcn_s_setprio(2);
  unsigned tra[4];
  bf8 wf[2][4];

  if (has1) {
    {
      const int fsw = 2 * (q >> 1) + 4 * (g & 1);
#pragma unroll
      for (int m = 0; m < 4; ++m) tra[m] = ring + (8 * g + q) * 128 + (((2 * m) ^ fsw) + (p >> 1)) * 16 + (p & 1) * 8;
    }
    u2 bias[4];
#pragma unroll
    for (int m = 0; m < 4; ++m) bias[m] = *(const u2*)(b1 + (ft * 64 + 16 * m + 4 * g) * 2);
    const unsigned voh = WS_H + (g * 640 + dt * 64 + (lane & 15)) * 16;
    u4 hq[3][4];
    f4 acc[4][4];
#pragma unroll
    for (int m = 0; m < 4; ++m)
#pragma unroll
      for (int n = 0; n < 4; ++n) acc[m][n] = f4{0.f, 0.f, 0.f, 0.f};
#define H_LOAD(i)                                                                                                            \
  asm volatile("buffer_load_dwordx4 %0, %4, %5, %6 offen sc1\n\t"                                                            \
               "buffer_load_dwordx4 %1, %4, %5, %6 offen offset:256 sc1\n\t"                                                 \
               "buffer_load_dwordx4 %2, %4, %5, %6 offen offset:512 sc1\n\t"                                                 \
               "buffer_load_dwordx4 %3, %4, %5, %6 offen offset:768 sc1"                                                     \
               : "=&v"(hq[(i) % 3][0]), "=&v"(hq[(i) % 3][1]), "=&v"(hq[(i) % 3][2]), "=&v"(hq[(i) % 3][3])                   \
               : "v"(voh), "s"(srdS), "s"(KS1(i) * 40960u)                                                                   \
               : "memory")
#define H_WAIT(N, hs) asm volatile("s_waitcnt vmcnt(%4)" : "+v"(hs[0]), "+v"(hs[1]), "+v"(hs[2]), "+v"(hs[3]) : "i"(N) : "memory")
#define G1_READ(i) _Pragma("unroll") for (int m = 0; m < 4; ++m) wf[(i) & 1][m] = cat8(tr_read(tra[m] + ((i) % 7) * 4096), tr_read(tra[m] + ((i) % 7) * 4096 + 512))
    asm volatile("" : "+v"(bias[0]), "+v"(bias[1]), "+v"(bias[2]), "+v"(bias[3]), "+v"(pfv));
    ctr_wait(ctl + C_GO1 * 4, 1);
    STAMP(9 + wave);
    H_LOAD(0); H_LOAD(1); H_LOAD(2);
    H_WAIT(8, hq[0]);
    G1_READ(0);
#define G1_STEP(i, N)                                                                                                        \
  do {                                                                                                                       \
    if ((i) + 1 < 16) { H_WAIT(N, hq[((i) + 1) % 3]); G1_READ((i) + 1); }                                                    \
    SCHED_FENCE();                                                                                                           \
    _Pragma("unroll") for (int m = 0; m < 4; ++m)                                                                            \
    _Pragma("unroll") for (int n = 0; n < 4; ++n) acc[m][n] = mfma16(wf[(i) & 1][m], __builtin_bit_cast(bf8, hq[(i) % 3][n]), acc[m][n]); \
    SCHED_FENCE();                                                                                                           \
    if ((i) <= 8) W1_DMA((i) + 7); else if ((i) <= 12) W2_DMA((i) - 9);                                                      \
    if ((i) <= 12) H_LOAD((i) + 3);                                                                                          \
  } while (0)
    G1_STEP(0, 4); G1_STEP(1, 8); G1_STEP(2, 8); G1_STEP(3, 8); G1_STEP(4, 8); G1_STEP(5, 8); G1_STEP(6, 8); G1_STEP(7, 8);
    G1_STEP(8, 8);
#pragma unroll
    for (int t = 0; t < 4; ++t) vow[t] = (8 * t + (lane >> 3)) * 4096 + st * 128 + (((lane & 7) ^ ((lane >> 3) & 6)) * 16) - 1024 * t;
    G1_STEP(9, 8); G1_STEP(10, 8); G1_STEP(11, 8); G1_STEP(12, 8); G1_STEP(13, 8); G1_STEP(14, 0); G1_STEP(15, 0);
    STAMP(40 + wave);
#pragma unroll
    for (int n = 0; n < 4; ++n)
      if (n != wave) {
        const int k = n < wave ? n : n - 1;
        const unsigned a = ring + (k == 0 ? 6 : k - 1) * 4096 + lane * 16;
#pragma unroll
        for (int m = 0; m < 4; ++m) lds_st<f4>(a + m * 1024, acc[m][n]);
      }
    f4 own[4];
#pragma unroll
    for (int m = 0; m < 4; ++m) own[m] = sel4(wave & 2, sel4(wave & 1, acc[m][3], acc[m][2]), sel4(wave & 1, acc[m][1], acc[m][0]));
    ctr_arrive(ctl + C_G1 * 4, lane);
    ctr_wait(ctl + C_G1 * 4, 4);
#pragma unroll
    for (int w = 1; w < 4; ++w) {
      const int src = (wave + w) & 3, k = wave < src ? wave : wave - 1;
      const unsigned a = sb + src * L_RING + (k == 0 ? 6 : k - 1) * 4096 + lane * 16;
#pragma unroll
      for (int m = 0; m < 4; ++m) own[m] += lds_ld<f4>(a + m * 1024);
    }
    unsigned o[8];
#pragma unroll
    for (int m = 0; m < 4; ++m) {
      o[2 * m] = pk(gelu_t(own[m][0] + blo(bias[m].x)), gelu_t(own[m][1] + bhi(bias[m].x)));
      o[2 * m + 1] = pk(gelu_t(own[m][2] + blo(bias[m].y)), gelu_t(own[m][3] + bhi(bias[m].y)));
    }
#pragma unroll
    for (int P = 0; P < 2; ++P)
      st16_sc1(rws, WS_U + ((ft * 2 + P) * 40 + dt * 4 + wave) * 1024 + lane * 16, u4{o[4 * P], o[4 * P + 1], o[4 * P + 2], o[4 * P + 3]});
    ctr_arrive(ctl + C_G2 * 4, lane);
    ctr_wait(ctl + C_G2 * 4, 4);
    W2_DMA(4);
    waitv<4>();
    if (ctr_arrive(ctl + C_G3 * 4, lane) == 3 && lane < 8) st8_sc1(rws, WS_F2 + lane * 4096 + (dt * 24 + ft) * 16, tag);
    STAMP(44 + wave);
    W2_DMA(5); waitv<4>(); W2_DMA(6);
  } else {
#pragma unroll
    for (int t = 0; t < 4; ++t) vow[t] = (8 * t + (lane >> 3)) * 4096 + st * 128 + (((lane & 7) ^ ((lane >> 3) & 6)) * 16) - 1024 * t;
    asm volatile("" : "+v"(pfv));
    ctr_wait(ctl + C_GO1 * 4, 1);
    W2_DMA(0); W2_DMA(1);
    waitv<4>(); W2_DMA(2); waitv<4>(); W2_DMA(3); waitv<4>(); W2_DMA(4); waitv<4>(); W2_DMA(5); waitv<4>(); W2_DMA(6);
  }

  {
    const int fsw = 4 * (g & 1) + (q & 2);
#pragma unroll
    for (int m = 0; m < 4; ++m) tra[m] = ring + (4 * g + q) * 128 + (((2 * m) ^ fsw) + (p >> 1)) * 16 + (p & 1) * 8;
  }
  const int s = st * 64 + wave * 16 + (lane & 15);
  const size_t xo = (size_t)s * 1280 + (dt2 * 80 + 4 * g) * 2;
  u2 xr[3];
#pragma unroll
  for (int m = 0; m < 3; ++m) xr[m] = *(const u2*)(x + xo + m * 32);
  u2 stt = ld8_sc1(rws, WS_ST + s * 8);
  unsigned b2r = *(const unsigned short*)(b2 + s * 2);
  const unsigned vou0 = WS_U + dt2 * 5120 + lane * 16, vou1 = vou0 + 4096;
  u4 uq[3][5];
  f4 acc[5][4];
#pragma unroll
  for (int m = 0; m < 5; ++m)
#pragma unroll
    for (int n = 0; n < 4; ++n) acc[m][n] = f4{0.f, 0.f, 0.f, 0.f};
#define U_LOAD(j)                                                                                                            \
  asm volatile("buffer_load_dwordx4 %0, %5, %7, %8 offen sc1\n\t"                                                            \
               "buffer_load_dwordx4 %1, %5, %7, %8 offen offset:1024 sc1\n\t"                                                \
               "buffer_load_dwordx4 %2, %5, %7, %8 offen offset:2048 sc1\n\t"                                                \
               "buffer_load_dwordx4 %3, %5, %7, %8 offen offset:3072 sc1\n\t"                                                \
               "buffer_load_dwordx4 %4, %6, %7, %8 offen sc1"                                                                \
               : "=&v"(uq[(j) % 3][0]), "=&v"(uq[(j) % 3][1]), "=&v"(uq[(j) % 3][2]), "=&v"(uq[(j) % 3][3]), "=&v"(uq[(j) % 3][4]) \
               : "v"(vou0), "v"(vou1), "s"(srdS), "s"(KS2(j) * 40960u)                                                       \
               : "memory")
#define U_WAIT(N, us) asm volatile("s_waitcnt vmcnt(%5)" : "+v"(us[0]), "+v"(us[1]), "+v"(us[2]), "+v"(us[3]), "+v"(us[4]) : "i"(N) : "memory")
#define G2_READ(j) _Pragma("unroll") for (int n = 0; n < 4; ++n) wf[(j) & 1][n] = cat8(tr_read(tra[n] + (((j) + 2) % 7) * 4096), tr_read(tra[n] + (((j) + 2) % 7) * 4096 + 2048))
#define G2_STEP(j, N)                                                                                                        \
  do {                                                                                                                       \
    if ((j) + 1 < 12) { U_WAIT(N, uq[((j) + 1) % 3]); G2_READ((j) + 1); }                                                    \
    SCHED_FENCE();                                                                                                           \
    _Pragma("unroll") for (int m = 0; m < 5; ++m)                                                                            \
    _Pragma("unroll") for (int n = 0; n < 4; ++n) acc[m][n] = mfma16(__builtin_bit_cast(bf8, uq[(j) % 3][m]), wf[(j) & 1][n], acc[m][n]); \
    SCHED_FENCE();                                                                                                           \
    if ((j) <= 4) W2_DMA((j) + 7);                                                                                           \
    if ((j) <= 8) U_LOAD((j) + 3);                                                                                           \
  } while (0)
  asm volatile("" : "+v"(xr[0]), "+v"(xr[1]), "+v"(xr[2]), "+v"(stt), "+v"(b2r));
  ctr_wait(ctl + C_GO2 * 4, 1);
  STAMP(48 + wave);
  U_LOAD(0); U_LOAD(1); U_LOAD(2);
  U_WAIT(10, uq[0]);
  G2_READ(0);
  G2_STEP(0, 5); G2_STEP(1, 9); G2_STEP(2, 9); G2_STEP(3, 9); G2_STEP(4, 9); G2_STEP(5, 9); G2_STEP(6, 5); G2_STEP(7, 5);
  G2_STEP(8, 5); G2_STEP(9, 5); G2_STEP(10, 0); G2_STEP(11, 0);
  STAMP(52 + wave);
#pragma unroll
  for (int n = 0; n < 4; ++n) {
    if (n != wave) {
#pragma unroll
      for (int m = 0; m < 3; ++m) lds_st<f4>(ring + (n * 5 + m) * 1024 + lane * 16, acc[m][n]);
    }
#pragma unroll
    for (int m = 3; m < 5; ++m) lds_st<f4>(ring + (n * 5 + m) * 1024 + lane * 16, acc[m][n]);
  }
  f4 own[3];
#pragma unroll
  for (int m = 0; m < 3; ++m) own[m] = sel4(wave & 2, sel4(wave & 1, acc[m][3], acc[m][2]), sel4(wave & 1, acc[m][1], acc[m][0]));
  raw_barrier();
  STAMP(56 + wave);
#pragma unroll
  for (int w = 1; w < 4; ++w) {
    const unsigned a = sb + ((wave + w) & 3) * L_RING + wave * 5120 + lane * 16;
#pragma unroll
    for (int m = 0; m < 3; ++m) own[m] += lds_ld<f4>(a + m * 1024);
  }
  final_tiles<0, 3>(own, xr, stt, b2r, sb + L_YA + (4 * g + q) * 128 + wave * 32 + p * 8, out + xo);
  STAMP(60 + wave);
}

extern "C" void kernel_launch(void* const* d_in, const int* in_sizes, int n_in, void* d_out, int out_size, void* d_ws, size_t ws_size,
                              hipStream_t stream) {
  (void)hipFuncSetAttribute((const void*)fused_kernel, hipFuncAttributeMaxDynamicSharedMemorySize, L_TOTAL);
  fused_kernel<<<256, 512, L_TOTAL, stream>>>((const char*)d_in[0], (const char*)d_in[1], (const char*)d_in[2], (const char*)d_in[3],
                                              (const char*)d_in[4], (const char*)d_in[5], (const char*)d_in[7], (const char*)d_in[8],
                                              (const char*)d_in[9], (const char*)d_in[10], (char*)d_out, (char*)d_ws);
}
```

```cpp
#include <hip/hip_runtime.h>
#include <hip/hip_bf16.h>
#include <stdint.h>

typedef __attribute__((ext_vector_type(8))) __bf16 bf8;
typedef __attribute__((ext_vector_type(2))) __bf16 bf2;
typedef __attribute__((ext_vector_type(4))) short s4;
typedef __attribute__((ext_vector_type(4))) float f4;
typedef __attribute__((ext_vector_type(2))) float f2;
typedef __attribute__((ext_vector_type(4))) unsigned u4;
typedef __attribute__((ext_vector_type(2))) unsigned u2;
#define LAS __attribute__((address_space(3)))
#define DEVI __device__ __forceinline__
#define SCHED_FENCE() __builtin_amdgcn_sched_barrier(0)

#define PF_W2 1
#define NT_FIRST 2

constexpr unsigned WS_EPOCH = 0;
constexpr unsigned WS_F1 = 4096;
constexpr unsigned WS_F2 = WS_F1 + 32768;
constexpr unsigned WS_F3 = WS_F2 + 32768;
constexpr unsigned WS_ST = WS_F3 + 8192;
constexpr unsigned WS_H = WS_ST + 16384;
constexpr unsigned WS_KV = WS_H + 2621440;
constexpr unsigned WS_YA = WS_KV + 2621440;
constexpr unsigned WS_U = WS_YA + 2621440;
constexpr unsigned WS_END = WS_U + 1966080;
constexpr size_t WS_DBG = WS_END;

#define STAMP(idx) do {} while (0)

constexpr unsigned L_RING = 28672;
constexpr unsigned CV_CP = 4160;
constexpr unsigned CV_X = 4 * CV_CP;
constexpr unsigned CV_BYTES = 21248;
constexpr unsigned L_CONV = 4 * L_RING;
constexpr unsigned L_HBUF = L_CONV;
constexpr unsigned L_KBUF = L_CONV + 10752;
constexpr unsigned L_VBUF = L_KBUF + 11264;
constexpr unsigned L_YA = L_CONV + CV_BYTES;
constexpr unsigned L_CTL = L_CONV + 2 * CV_BYTES;
constexpr unsigned L_TOTAL = L_CTL + 128;
enum { C_H = 0, C_GO1, C_G1, C_G2, C_G3, C_GO2, C_P0, C_P1, C_TAG0, C_TAG1 };
constexpr unsigned SPIN_CAP = 1u << 16;

DEVI float blo(unsigned u) { return __uint_as_float(u << 16); }
DEVI float bhi(unsigned u) { return __uint_as_float(u & 0xffff0000u); }
DEVI unsigned pk(float a, float b) { f2 v = {a, b}; return __builtin_bit_cast(unsigned, __builtin_convertvector(v, bf2)); }
DEVI float rbf(float a) { return blo(pk(a, a)); }
template <int CTRL, int RM> DEVI float dppf(float x) {
  return __builtin_bit_cast(float, __builtin_amdgcn_update_dpp(0, __builtin_bit_cast(int, x), CTRL, RM, 0xf, true));
}
template <class T> DEVI T ldnt(const void* p) { return __builtin_nontemporal_load((const T*)p); }
DEVI u4 make_srd(const void* p, unsigned bytes) {
  uint64_t a = (uint64_t)p;
  u4 r = {(unsigned)a, (unsigned)(a >> 32) & 0xffffu, bytes, 0x00020000u};
  return r;
}
DEVI s4 tr_read(unsigned addr) { return __builtin_amdgcn_ds_read_tr16_b64_v4i16((LAS s4*)(size_t)addr); }
DEVI bf8 cat8(s4 a, s4 b) {
  typedef __attribute__((ext_vector_type(8))) short s8;
  s8 v = __builtin_shufflevector(a, b, 0, 1, 2, 3, 4, 5, 6, 7);
  return __builtin_bit_cast(bf8, v);
}
DEVI f4 mfma16(bf8 a, bf8 b, f4 c) { return __builtin_amdgcn_mfma_f32_16x16x32_bf16(a, b, c, 0, 0, 0); }
template <int N> DEVI void waitv() { asm volatile("s_waitcnt vmcnt(%0)" ::"i"(N) : "memory"); }
template <class T> DEVI T lds_ld(unsigned a) { return *(const LAS T*)(size_t)a; }
template <class T> DEVI void lds_st(unsigned a, T v) { *(LAS T*)(size_t)a = v; }
DEVI void raw_barrier() { asm volatile("s_waitcnt lgkmcnt(0)\n\ts_barrier" ::: "memory"); }
DEVI unsigned ctr_arrive(unsigned a, int lane) {
  asm volatile("s_waitcnt lgkmcnt(0)" ::: "memory");
  unsigned old = 0;
  if (lane == 0) old = __hip_atomic_fetch_add((LAS unsigned*)(size_t)a, 1u, __ATOMIC_RELAXED, __HIP_MEMORY_SCOPE_WORKGROUP);
  old = __builtin_amdgcn_readfirstlane(old);
  asm volatile("" ::: "memory");
  return old;
}
DEVI void ctr_wait(unsigned a, unsigned n) {
  while (__builtin_amdgcn_readfirstlane(__hip_atomic_load((LAS unsigned*)(size_t)a, __ATOMIC_RELAXED, __HIP_MEMORY_SCOPE_WORKGROUP)) < n)
    __builtin_amdgcn_s_sleep(1);
  asm volatile("" ::: "memory");
}
typedef __amdgpu_buffer_rsrc_t rsrc_t;
DEVI u4 ld16_sc1(rsrc_t r, unsigned off) { return __builtin_amdgcn_raw_buffer_load_b128(r, off, 0, 16); }
DEVI u2 ld8_sc1(rsrc_t r, unsigned off) { return __builtin_amdgcn_raw_buffer_load_b64(r, off, 0, 16); }
DEVI void st16_sc1(rsrc_t r, unsigned off, u4 v) { __builtin_amdgcn_raw_buffer_store_b128(v, r, off, 0, 16); }
DEVI void st8_sc1(rsrc_t r, unsigned off, u2 v) { __builtin_amdgcn_raw_buffer_store_b64(v, r, off, 0, 16); }

DEVI f4 sel4(bool c, f4 a, f4 b) {
  asm("" : "+v"(a)); asm("" : "+v"(b));
  return c ? a : b;
}
DEVI float gelu_t(float x) {
  const float t = x * __builtin_fmaf(x * x, -0.10294324f, -2.3022082f);
  return x * __builtin_amdgcn_rcpf(1.f + __builtin_amdgcn_exp2f(t));
}

DEVI void ln_row(const unsigned (&xr)[5], unsigned (&hp)[5], float& m1, float& inv1) {
  float e[10], s = 0.f, q = 0.f;
  const float piv = blo(__builtin_amdgcn_readfirstlane(xr[0]));
#pragma unroll
  for (int j = 0; j < 5; ++j) { e[2 * j] = blo(xr[j]); e[2 * j + 1] = bhi(xr[j]); }
#pragma unroll
  for (int j = 0; j < 10; ++j) { const float d = e[j] - piv; s += d; q = __builtin_fmaf(d, d, q); }
  s += dppf<0xB1, 0xf>(s);  q += dppf<0xB1, 0xf>(q);
  s += dppf<0x4E, 0xf>(s);  q += dppf<0x4E, 0xf>(q);
  s += dppf<0x141, 0xf>(s); q += dppf<0x141, 0xf>(q);
  s += dppf<0x140, 0xf>(s); q += dppf<0x140, 0xf>(q);
  s += dppf<0x142, 0xa>(s); q += dppf<0x142, 0xa>(q);
  s += dppf<0x143, 0xc>(s); q += dppf<0x143, 0xc>(q);
  const float md = __builtin_bit_cast(float, __builtin_amdgcn_readlane(__builtin_bit_cast(int, s), 63)) * (1.f / 640.f);
  const float qq = __builtin_bit_cast(float, __builtin_amdgcn_readlane(__builtin_bit_cast(int, q), 63)) * (1.f / 640.f);
  const float var = __builtin_fmaxf(qq - md * md, 0.f), mean = piv + md;
  m1 = rbf(mean);
  inv1 = __builtin_amdgcn_rcpf(rbf(__builtin_amdgcn_sqrtf(rbf(rbf(var) + 1e-5f))));
  const float m2 = rbf((mean - m1) * inv1);
  const float inv2 = __builtin_amdgcn_rcpf(rbf(__builtin_amdgcn_sqrtf(rbf(rbf(var * inv1 * inv1) + 1e-5f))));
  const float A = inv1 * inv2, B = -m2 * inv2;
#pragma unroll
  for (int j = 0; j < 5; ++j) hp[j] = pk(__builtin_fmaf(e[2 * j] - m1, A, B), __builtin_fmaf(e[2 * j + 1] - m1, A, B));
}

struct Filt { u4 c[2]; u2 n[2]; };
DEVI void filt_load(Filt& f, const char* krow, int half, int lane) {
#pragma unroll
  for (int j = 0; j < 2; ++j) {
    const int z = lane + 64 * j + 128 * half;
    f.c[j] = ldnt<u4>(krow + z * 16);
    f.n[j] = ldnt<u2>(krow + (z < 255 ? z * 16 + 16 : 0));
  }
}
DEVI void filt_build(Filt& f, unsigned cv, int half, int lane) {
  const u4 zero = {0u, 0u, 0u, 0u};
  if (half == 1) {
    if (lane == 63) f.n[1] = u2{0u, 0u};
    if (lane < 30) lds_st<u4>(cv + CV_X + 4096 + lane * 16, zero);
  } else {
    if (lane < 4) lds_st<u4>(cv + lane * CV_CP, zero);
    if (lane == 0) {
      const unsigned d4 = f.c[0].x, d5 = f.c[0].y;
      lds_st<u4>(cv + 16, u4{0u, 0u, 0u, d4 << 16});
      lds_st<u4>(cv + CV_CP + 16, u4{0u, 0u, 0u, d4});
      lds_st<u4>(cv + 2 * CV_CP + 16, u4{0u, 0u, d4 << 16, __builtin_amdgcn_alignbit(d5, d4, 16)});
      lds_st<u4>(cv + 3 * CV_CP + 16, u4{0u, 0u, d4, d5});
    }
  }
#pragma unroll
  for (int j = 0; j < 2; ++j) {
    const u4 c = f.c[j]; const u2 n = f.n[j];
    const unsigned a0 = __builtin_amdgcn_alignbit(c.y, c.x, 16), a1 = __builtin_amdgcn_alignbit(c.z, c.y, 16), a2 = __builtin_amdgcn_alignbit(c.w, c.z, 16),
                   a3 = __builtin_amdgcn_alignbit(n.x, c.w, 16), a4 = __builtin_amdgcn_alignbit(n.y, n.x, 16);
    const unsigned p = cv + (lane + 64 * j + 128 * half + 2) * 16;
    lds_st<u4>(p, u4{a0, a1, a2, a3});
    lds_st<u4>(p + CV_CP, u4{c.y, c.z, c.w, n.x});
    lds_st<u4>(p + 2 * CV_CP, u4{a1, a2, a3, a4});
    lds_st<u4>(p + 3 * CV_CP, u4{c.z, c.w, n.x, n.y});
  }
}

template <int M0, int NM>
DEVI void final_tiles(f4 (&own)[NM], const u2 (&xr)[NM], u2 stt, unsigned b2r, unsigned yab, char* op) {
  const float bb = blo(b2r), m1 = __uint_as_float(stt.x), inv1 = __uint_as_float(stt.y);
#pragma unroll
  for (int m = 0; m < NM; ++m) {
    const u2 ya = __builtin_bit_cast(u2, tr_read(yab + (M0 + m) * 2048));
    const float x0 = blo(xr[m].x), x1 = bhi(xr[m].x), x2 = blo(xr[m].y), x3 = bhi(xr[m].y);
    const float r0 = __builtin_fmaf((x0 - m1) * inv1, blo(ya.x), x0) + (own[m][0] + bb), r1 = __builtin_fmaf((x1 - m1) * inv1, bhi(ya.x), x1) + (own[m][1] + bb);
    const float r2 = __builtin_fmaf((x2 - m1) * inv1, blo(ya.y), x2) + (own[m][2] + bb), r3 = __builtin_fmaf((x3 - m1) * inv1, bhi(ya.y), x3) + (own[m][3] + bb);
    { u2 ov = u2{pk(r0, r1), pk(r2, r3)}; asm volatile("global_store_dwordx2 %0, %1, off offset:%2 sc1" :: "v"(op), "v"(ov), "i"((M0 + m) * 32) : "memory"); }
  }
}

__global__ __launch_bounds__(512, 2) __attribute__((amdgpu_waves_per_eu(2, 2))) void fused_kernel(
    const char* __restrict__ x, const char* __restrict__ kk, const char* __restrict__ vv, const char* __restrict__ shk,
    const char* __restrict__ ssmk, const char* __restrict__ ssmd, const char* __restrict__ w1, const char* __restrict__ b1,
    const char* __restrict__ w2, const char* __restrict__ b2, char* __restrict__ out, char* ws) {
  extern __shared__ __attribute__((aligned(16))) char smem[];
  const int lane = threadIdx.x & 63, wave = __builtin_amdgcn_readfirstlane(threadIdx.x >> 6);
  const int b = blockIdx.x, xg = b & 7, r = b >> 3;
  const unsigned sb = __builtin_amdgcn_readfirstlane((unsigned)(size_t)smem);
  const unsigned ctl = sb + L_CTL;
  asm volatile("" ::"s"(x), "s"(kk), "s"(vv), "s"(shk), "s"(ssmk), "s"(ssmd), "s"(w1), "s"(b1), "s"(w2), "s"(b2), "s"(out), "s"(ws));
  const rsrc_t rws = __builtin_amdgcn_make_buffer_rsrc(ws, 0, WS_END, 0x00020000);
  const unsigned long long pad_t0 = __builtin_amdgcn_s_memrealtime();
  const int dt2 = r & 7, st = xg * 4 + (r >> 3);

  const u4 srdS = make_srd(ws, WS_END);
  u2 tag;
  {
    unsigned xr[5];
    asm volatile("buffer_load_dword %0, %5, %6, %7 offen nt\n\tbuffer_load_dword %1, %5, %6, %7 offen offset:256 nt\n\t"
                 "buffer_load_dword %2, %5, %6, %7 offen offset:512 nt\n\tbuffer_load_dword %3, %5, %6, %7 offen offset:768 nt\n\t"
                 "buffer_load_dword %4, %5, %6, %7 offen offset:1024 nt"
                 : "=&v"(xr[0]), "=&v"(xr[1]), "=&v"(xr[2]), "=&v"(xr[3]), "=&v"(xr[4])
                 : "v"(lane * 4), "s"(make_srd(x, 2621440)), "s"((b * 8 + wave) * 1280)
                 : "memory");
    {
      const u4 srdK = make_srd(kk, 2621440), srdV = make_srd(vv, 2621440);
#pragma unroll
      for (int i = 0; i < 3; ++i) {
        const int pc = wave + 8 * i;
        if (pc < 22) {
          const bool isv = pc >= 11;
          const int n = (isv ? pc - 11 : pc) * 64 + lane, g8 = n / 81, rem = n - g8 * 81, rr = rem / 10;
          const int row = g8 * 8 + rr, c = rem - rr * 10;
          const int grow = r * 64 + row - (isv ? 0 : 3);
          const bool ok = rem < 80 && grow >= 0 && row < (isv ? 64 : 67);
          const unsigned vo = ok ? grow * 1280 + xg * 160 + c * 16 : 0x7fffff00u;
          const unsigned dst = sb + (isv ? L_VBUF - 11264 : L_KBUF) + pc * 1024;
          if (isv) asm volatile("s_mov_b32 m0, %0\n\ts_nop 0\n\tbuffer_load_dwordx4 %1, %2, 0 offen nt lds" ::"s"(dst), "v"(vo), "s"(srdV) : "memory");
          else     asm volatile("s_mov_b32 m0, %0\n\ts_nop 0\n\tbuffer_load_dwordx4 %1, %2, 0 offen nt lds" ::"s"(dst), "v"(vo), "s"(srdK) : "memory");
        }
      }
    }
    u2 ep = ld8_sc1(rws, WS_EPOCH);
    u4 shw = *(const u4*)(shk + (xg * 80 + (wave >= 3 ? wave - 3 : 0) * 16 + (lane >> 3) * 2) * 8);
    SCHED_FENCE();
    if (wave == 0 && lane < 8) lds_st<unsigned>(ctl + lane * 4, 0u);
    asm volatile("s_waitcnt vmcnt(4)" : "+v"(xr[0]), "+v"(xr[1]), "+v"(xr[2]), "+v"(xr[3]), "+v"(xr[4])::"memory");
    STAMP(1);
    unsigned hp[5]; float m1, inv1;
    ln_row(xr, hp, m1, inv1);
#pragma unroll
    for (int j = 0; j < 5; ++j) lds_st<unsigned>(sb + L_HBUF + wave * 1344 + (lane + 64 * j) * 4, hp[j]);
    STAMP(2);
    asm volatile("s_waitcnt vmcnt(0)" : "+v"(ep), "+v"(shw)::"memory");
    const uint64_t tg = (((uint64_t)ep.y << 32) | ep.x) * 6364136223846793005ull + 1442695040888963407ull;
    tag = u2{(unsigned)__builtin_amdgcn_readfirstlane((unsigned)tg), (unsigned)__builtin_amdgcn_readfirstlane((unsigned)(tg >> 32))};
    STAMP(3);
    raw_barrier();
    if (threadIdx.x == 0) st8_sc1(rws, 1024 + b * 8, u2{(unsigned)pad_t0, (unsigned)(pad_t0 >> 32)});
    if (lane == 0) st8_sc1(rws, WS_ST + (b * 8 + wave) * 8, u2{__float_as_uint(m1), __float_as_uint(inv1)});
    if (wave < 3) {
      const unsigned tb = sb + L_HBUF + ((lane >> 2) & 3) * 1344 + (lane >> 4) * 32 + (lane & 3) * 8;
      u4 v[4];
#pragma unroll
      for (int i = 0; i < 4; ++i) { const int it = i < 3 ? wave + 3 * i : 9; v[i] = __builtin_bit_cast(u4, cat8(tr_read(tb + it * 128), tr_read(tb + it * 128 + 4 * 1344))); }
#pragma unroll
      for (int i = 0; i < 4; ++i)
        if (i < 3 || wave == 0) st16_sc1(rws, WS_H + (b * 640 + (i < 3 ? wave + 3 * i : 9) * 64 + lane) * 16, v[i]);
    } else {
      const int rg = lane & 7, cp = (wave - 3) * 8 + (lane >> 3);
      const unsigned ka = sb + L_KBUF + rg * 1296 + cp * 4;
      unsigned kr[11], vr[8];
#pragma unroll
      for (int i = 0; i < 11; ++i) kr[i] = lds_ld<unsigned>(ka + i * 160 + (i >> 3) * 16);
#pragma unroll
      for (int i = 0; i < 8; ++i) vr[i] = lds_ld<unsigned>(ka + 11264 + i * 160);
#pragma unroll
      for (int c = 0; c < 2; ++c) {
        const unsigned wa = c ? shw.z : shw.x, wb = c ? shw.w : shw.y;
        const float w0 = blo(wa), w1f = bhi(wa), w2f = blo(wb), w3 = bhi(wb);
        float kf[11], o[8];
#pragma unroll
        for (int i = 0; i < 11; ++i) kf[i] = c ? bhi(kr[i]) : blo(kr[i]);
#pragma unroll
        for (int i = 0; i < 8; ++i) o[i] = (kf[i] * w3 + kf[i + 1] * w2f + kf[i + 2] * w1f + kf[i + 3] * w0) * (c ? bhi(vr[i]) : blo(vr[i]));
        st16_sc1(rws, WS_KV + (xg * 80 + cp * 2 + c) * 4096 + (2040 - 64 * r - 8 * rg) * 2, u4{pk(o[7], o[6]), pk(o[5], o[4]), pk(o[3], o[2]), pk(o[1], o[0])});
      }
    }
    STAMP(4);
  }

  if (wave >= 4) {
    const int hw = wave - 4, pair = hw >> 1, half = hw & 1;
    Filt f;
    filt_load(f, ssmk + (size_t)(2 * b + pair) * 4096, half, lane);
    waitv<4>();
    if (ctr_arrive(ctl + C_H * 4, lane) == 7 && lane < 8) st8_sc1(rws, WS_F1 + lane * 4096 + b * 16, tag);
    STAMP(20 + hw);
    ctr_wait(ctl + C_H * 4, 8);
    const unsigned cv = sb + L_CONV + pair * CV_BYTES;
    filt_build(f, cv, half, lane);
    if (hw == 3) {
#pragma unroll 1
      for (unsigned n = 0; n < SPIN_CAP; ++n) {
        bool ok = true;
#pragma unroll
        for (int i = 0; i < 4; ++i) { const u2 a = ld8_sc1(rws, WS_F1 + xg * 4096 + i * 1024 + lane * 16); ok = ok && a.x == tag.x && a.y == tag.y; }
        if (__all(ok)) break;
        asm volatile("" ::: "memory");
      }
      asm volatile("" ::: "memory");
      if (lane == 0) lds_st<unsigned>(ctl + C_GO1 * 4, 1u);
      if (b == 0 && lane == 0) st8_sc1(rws, WS_EPOCH, tag);
      STAMP(24);
    } else {
      ctr_wait(ctl + C_GO1 * 4, 1);
    }
    const int nrounds = (pair == 0 && b >= 128) ? 2 : 1;
    const int c16 = lane & 15, kg = lane >> 4;
    for (int round = 0; round < nrounds; ++round) {
      const int ch = round == 0 ? 2 * b + pair : 384 + b;
      const unsigned pc = ctl + (C_P0 + pair) * 4, pbase = round * 8;
      if (round) filt_load(f, ssmk + (size_t)ch * 4096, half, lane);
      u4 xv[2];
#pragma unroll
      for (int j = 0; j < 2; ++j) xv[j] = ld16_sc1(rws, WS_KV + ch * 4096 + (lane + 64 * j + 128 * half) * 16);
      const float dd = blo(*(const unsigned short*)(ssmd + ch * 2));
      if (round) filt_build(f, cv, half, lane);
#pragma unroll
      for (int j = 0; j < 2; ++j) lds_st<u4>(cv + CV_X + (lane + 64 * j + 128 * half) * 16, xv[j]);
      ctr_arrive(pc, lane);
      ctr_wait(pc, pbase + 2);
      const unsigned ab = cv + (c16 & 3) * CV_CP + (c16 >> 2) * 8 + kg * 16 + half * 256;
      const unsigned bb = cv + CV_X + 480 - 32 * c16 + 16 * kg + half * 256;
      f4 acc[8];
#pragma unroll
      for (int n = 0; n < 8; ++n) acc[n] = f4{0.f, 0.f, 0.f, 0.f};
      bf8 A0[8], B0[8], A1[8], B1[8];
#define CV_LOAD(A, B, q)                                                                              \
  _Pragma("unroll") for (int p = 0; p < 8; ++p) {                                                      \
    B[p] = *(const volatile LAS bf8*)(size_t)(bb + 3584 - 512 * p + 64 * (q));                          \
    s4 lo = *(const volatile LAS s4*)(size_t)(ab + 512 * p + 64 * (q));                                 \
    s4 hi = *(const volatile LAS s4*)(size_t)(ab + 512 * p + 64 * (q) + 8);                             \
    A[p] = cat8(lo, hi);                                                                               \
  }
#define CV_MMA(A, B)                                                                                   \
  _Pragma("unroll") for (int p = 0; p < 8; ++p)                                                        \
  _Pragma("unroll") for (int m = 0; m + p < 8; ++m) acc[p + m] = mfma16(A[p], B[m], acc[p + m]);
#define CV_SCHED()                                                                                     \
  _Pragma("unroll") for (int z = 0; z < 24; ++z) {                                                     \
    __builtin_amdgcn_sched_group_barrier(0x8, 1, 0);                                                   \
    __builtin_amdgcn_sched_group_barrier(0x100, 1, 0);                                                 \
  }                                                                                                    \
  __builtin_amdgcn_sched_group_barrier(0x8, 12, 0);                                                    \
  SCHED_FENCE();
      CV_LOAD(A0, B0, 0);
      SCHED_FENCE();
#pragma unroll 1
      for (int q = 0; q < 4; q += 2) {
        CV_LOAD(A1, B1, q + 1); CV_MMA(A0, B0); CV_SCHED();
        CV_LOAD(A0, B0, (q + 2) & 3); CV_MMA(A1, B1); CV_SCHED();
      }
      if (round == 0) STAMP(25 + hw);
      ctr_arrive(pc, lane);
      ctr_wait(pc, pbase + 4);
#pragma unroll
      for (int n = 0; n < 4; ++n) lds_st<f4>(cv + half * 4096 + n * 1024 + lane * 16, sel4(half, acc[n], acc[n + 4]));
      ctr_arrive(pc, lane);
      ctr_wait(pc, pbase + 6);
#pragma unroll
      for (int n = 0; n < 4; ++n) {
        const f4 y = sel4(half, acc[n + 4], acc[n]) + lds_ld<f4>(cv + (half ^ 1) * 4096 + n * 1024 + lane * 16);
        const int t0 = 1024 * half + 256 * n + 16 * c16 + 4 * kg;
        const u2 xq = lds_ld<u2>(cv + CV_X + (2044 - t0) * 2);
        lds_st<u2>(cv + 8192 + t0 * 2, u2{pk(__builtin_fmaf(bhi(xq.y), dd, y[0]), __builtin_fmaf(blo(xq.y), dd, y[1])),
                                          pk(__builtin_fmaf(bhi(xq.x), dd, y[2]), __builtin_fmaf(blo(xq.x), dd, y[3]))});
      }
#pragma unroll
      for (int n = 0; n < 2; ++n) {
        const unsigned o = half * 2048 + n * 1024 + lane * 16;
        st16_sc1(rws, WS_YA + ch * 4096 + o, lds_ld<u4>(cv + 8192 + o));
      }
      waitv<0>();
      if (ctr_arrive(pc, lane) == pbase + 7 && lane == 0) st8_sc1(rws, WS_F3 + ch * 8, tag);
      ctr_wait(pc, pbase + 8);
    }
    STAMP(32 + hw);
    const int s = st * 64 + hw * 16 + c16;
    const size_t xo = (size_t)s * 1280 + (dt2 * 80 + 4 * kg) * 2;
    u2 xq[2] = {*(const u2*)(x + xo + 96), *(const u2*)(x + xo + 128)};
    u2 stt = ld8_sc1(rws, WS_ST + s * 8);
    unsigned b2r = *(const unsigned short*)(b2 + s * 2);
    asm volatile("" : "+v"(xq[0]), "+v"(xq[1]), "+v"(stt), "+v"(b2r));
    if (hw == 3) {
      const int dta = (dt2 * 5) >> 2;
#pragma unroll 1
      for (unsigned n = 0; n < SPIN_CAP; ++n) {
        const u2 a = ld8_sc1(rws, WS_F2 + xg * 4096 + (dta * 24 + (lane < 48 ? lane : 0)) * 16);
        if (__all(a.x == tag.x && a.y == tag.y)) break;
        asm volatile("" ::: "memory");
      }
      asm volatile("" ::: "memory");
      if (lane == 0) lds_st<unsigned>(ctl + C_GO2 * 4, 1u);
      STAMP(36);
#pragma unroll 1
      for (unsigned n = 0; n < SPIN_CAP; ++n) {
        const u4 a = ld16_sc1(rws, WS_F3 + dt2 * 640 + (lane < 40 ? lane : 0) * 16);
        if (__all(a.x == tag.x && a.y == tag.y && a.z == tag.x && a.w == tag.y)) break;
        __builtin_amdgcn_s_sleep(2);
        asm volatile("" ::: "memory");
      }
      asm volatile("" ::: "memory");
      STAMP(37);
      u4 y[10];
#pragma unroll
      for (int i = 0; i < 10; ++i) y[i] = ld16_sc1(rws, WS_YA + (dt2 * 80 + i * 8 + (lane >> 3)) * 4096 + st * 128 + (lane & 7) * 16);
      SCHED_FENCE();
#pragma unroll
      for (int i = 0; i < 10; ++i) lds_st<u4>(sb + L_YA + i * 1024 + lane * 16, y[i]);
      STAMP(38);
    }
    raw_barrier();
    f4 own[2];
#pragma unroll
    for (int m = 0; m < 2; ++m) {
      const unsigned a = sb + (hw * 5 + 3 + m) * 1024 + lane * 16;
      own[m] = (lds_ld<f4>(a) + lds_ld<f4>(a + L_RING)) + (lds_ld<f4>(a + 2 * L_RING) + lds_ld<f4>(a + 3 * L_RING));
    }
    final_tiles<3, 2>(own, xq, stt, b2r, sb + L_YA + (4 * kg + ((lane >> 2) & 3)) * 128 + hw * 32 + (lane & 3) * 8, out + xo);
    return;
  }

  const int g = lane >> 4, q = (lane >> 2) & 3, p = lane & 3;
  const unsigned ring = sb + wave * L_RING;
  const bool has1 = r < 30;
  const int dt = r % 10, ft = xg * 3 + r / 10;
  const int rot1 = dt * 16 / 10, rot2 = dt2 * 12 / 8;
  const u4 srdW1 = make_srd(w1, 6291456), srdW2 = make_srd(w2, 6291456);
  unsigned vow[4];
#define DMA4(POL, slot, srd, soff)                                                                                             \
  asm volatile("s_mov_b32 m0, %0\n\ts_nop 0\n\t"                                                                               \
               "buffer_load_dwordx4 %1, %5, %6 offen" POL " lds\n\t"                                                           \
               "buffer_load_dwordx4 %2, %5, %6 offen offset:1024" POL " lds\n\t"                                               \
               "buffer_load_dwordx4 %3, %5, %6 offen offset:2048" POL " lds\n\t"                                               \
               "buffer_load_dwordx4 %4, %5, %6 offen offset:3072" POL " lds" ::"s"(ring + (slot) * 4096),                      \
               "v"(vow[0]), "v"(vow[1]), "v"(vow[2]), "v"(vow[3]), "s"(srd), "s"(soff)                                         \
               : "memory")
#define KS1(i) (4u * (((i) + rot1) & 15) + wave)
#define KS2(j) (4u * ((j) + rot2 >= 12 ? (j) + rot2 - 12 : (j) + rot2) + wave)
#define W1_DMA(i) do { if ((i) < NT_FIRST) DMA4(" nt", (i) % 7, srdW1, KS1(i) * 98304u); else DMA4("", (i) % 7, srdW1, KS1(i) * 98304u); } while (0)
#define W2_DMA(j) do { if (!PF_W2 && (j) < NT_FIRST) DMA4(" nt", ((j) + 2) % 7, srdW2, KS2(j) * 131072u); else DMA4("", ((j) + 2) % 7, srdW2, KS2(j) * 131072u); } while (0)
#pragma unroll
  for (int t = 0; t < 4; ++t) vow[t] = (8 * t + (lane >> 3)) * 3072 + ft * 128 + (((lane & 7) ^ (2 * ((lane >> 4) & 1) + 4 * (t & 1))) * 16) - 1024 * t;
  waitv<0>();
  if (ctr_arrive(ctl + C_H * 4, lane) == 7 && lane < 8) st8_sc1(rws, WS_F1 + lane * 4096 + b * 16, tag);
  STAMP(5 + wave);
  ctr_wait(ctl + C_H * 4, 8);
  if (has1) {
    W1_DMA(0); W1_DMA(1);
    waitv<4>(); W1_DMA(2); waitv<4>(); W1_DMA(3); waitv<4>(); W1_DMA(4); waitv<4>(); W1_DMA(5); waitv<4>(); W1_DMA(6);
  }
  unsigned pfv = 0;
  if (PF_W2 && wave < 3) {
    const int j = r * 192 + wave * 64 + lane;
    pfv = ldnt<unsigned>(w2 + (size_t)(j >> 2) * 4096 + xg * 512 + (j & 3) * 128);
  }
  __builtin_amdgcn_s_setprio(2);
  unsigned tra[4];
  bf8 wf[2][4];

  if (has1) {
    {
      const int fsw = 2 * (q >> 1) + 4 * (g & 1);
#pragma unroll
      for (int m = 0; m < 4; ++m) tra[m] = ring + (8 * g + q) * 128 + (((2 * m) ^ fsw) + (p >> 1)) * 16 + (p & 1) * 8;
    }
    u2 bias[4];
#pragma unroll
    for (int m = 0; m < 4; ++m) bias[m] = *(const u2*)(b1 + (ft * 64 + 16 * m + 4 * g) * 2);
    const unsigned voh = WS_H + (g * 640 + dt * 64 + (lane & 15)) * 16;
    u4 hq[3][4];
    f4 acc[4][4];
#pragma unroll
    for (int m = 0; m < 4; ++m)
#pragma unroll
      for (int n = 0; n < 4; ++n) acc[m][n] = f4{0.f, 0.f, 0.f, 0.f};
#define H_LOAD(i)                                                                                                            \
  asm volatile("buffer_load_dwordx4 %0, %4, %5, %6 offen sc1\n\t"                                                            \
               "buffer_load_dwordx4 %1, %4, %5, %6 offen offset:256 sc1\n\t"                                                 \
               "buffer_load_dwordx4 %2, %4, %5, %6 offen offset:512 sc1\n\t"                                                 \
               "buffer_load_dwordx4 %3, %4, %5, %6 offen offset:768 sc1"                                                     \
               : "=&v"(hq[(i) % 3][0]), "=&v"(hq[(i) % 3][1]), "=&v"(hq[(i) % 3][2]), "=&v"(hq[(i) % 3][3])                   \
               : "v"(voh), "s"(srdS), "s"(KS1(i) * 40960u)                                                                   \
               : "memory")
#define H_WAIT(N, hs) asm volatile("s_waitcnt vmcnt(%4)" : "+v"(hs[0]), "+v"(hs[1]), "+v"(hs[2]), "+v"(hs[3]) : "i"(N) : "memory")
#define G1_READ(i) _Pragma("unroll") for (int m = 0; m < 4; ++m) wf[(i) & 1][m] = cat8(tr_read(tra[m] + ((i) % 7) * 4096), tr_read(tra[m] + ((i) % 7) * 4096 + 512))
    asm volatile("" : "+v"(bias[0]), "+v"(bias[1]), "+v"(bias[2]), "+v"(bias[3]), "+v"(pfv));
    ctr_wait(ctl + C_GO1 * 4, 1);
    STAMP(9 + wave);
    H_LOAD(0); H_LOAD(1); H_LOAD(2);
    H_WAIT(8, hq[0]);
    G1_READ(0);
#define G1_STEP(i, N)                                                                                                        \
  do {                                                                                                                       \
    if ((i) + 1 < 16) { H_WAIT(N, hq[((i) + 1) % 3]); G1_READ((i) + 1); }                                                    \
    SCHED_FENCE();                                                                                                           \
    _Pragma("unroll") for (int m = 0; m < 4; ++m)                                                                            \
    _Pragma("unroll") for (int n = 0; n < 4; ++n) acc[m][n] = mfma16(wf[(i) & 1][m], __builtin_bit_cast(bf8, hq[(i) % 3][n]), acc[m][n]); \
    SCHED_FENCE();                                                                                                           \
    if ((i) <= 8) W1_DMA((i) + 7); else if ((i) <= 12) W2_DMA((i) - 9);                                                      \
    if ((i) <= 12) H_LOAD((i) + 3);                                                                                          \
  } while (0)
    G1_STEP(0, 4); G1_STEP(1, 8); G1_STEP(2, 8); G1_STEP(3, 8); G1_STEP(4, 8); G1_STEP(5, 8); G1_STEP(6, 8); G1_STEP(7, 8);
    G1_STEP(8, 8);
#pragma unroll
    for (int t = 0; t < 4; ++t) vow[t] = (8 * t + (lane >> 3)) * 4096 + st * 128 + (((lane & 7) ^ ((lane >> 3) & 6)) * 16) - 1024 * t;
    G1_STEP(9, 8); G1_STEP(10, 8); G1_STEP(11, 8); G1_STEP(12, 8); G1_STEP(13, 8); G1_STEP(14, 0); G1_STEP(15, 0);
    STAMP(40 + wave);
#pragma unroll
    for (int n = 0; n < 4; ++n)
      if (n != wave) {
        const int k = n < wave ? n : n - 1;
        const unsigned a = ring + (k == 0 ? 6 : k - 1) * 4096 + lane * 16;
#pragma unroll
        for (int m = 0; m < 4; ++m) lds_st<f4>(a + m * 1024, acc[m][n]);
      }
    f4 own[4];
#pragma unroll
    for (int m = 0; m < 4; ++m) own[m] = sel4(wave & 2, sel4(wave & 1, acc[m][3], acc[m][2]), sel4(wave & 1, acc[m][1], acc[m][0]));
    ctr_arrive(ctl + C_G1 * 4, lane);
    ctr_wait(ctl + C_G1 * 4, 4);
#pragma unroll
    for (int w = 1; w < 4; ++w) {
      const int src = (wave + w) & 3, k = wave < src ? wave : wave - 1;
      const unsigned a = sb + src * L_RING + (k == 0 ? 6 : k - 1) * 4096 + lane * 16;
#pragma unroll
      for (int m = 0; m < 4; ++m) own[m] += lds_ld<f4>(a + m * 1024);
    }
    unsigned o[8];
#pragma unroll
    for (int m = 0; m < 4; ++m) {
      o[2 * m] = pk(gelu_t(own[m][0] + blo(bias[m].x)), gelu_t(own[m][1] + bhi(bias[m].x)));
      o[2 * m + 1] = pk(gelu_t(own[m][2] + blo(bias[m].y)), gelu_t(own[m][3] + bhi(bias[m].y)));
    }
#pragma unroll
    for (int P = 0; P < 2; ++P)
      st16_sc1(rws, WS_U + ((ft * 2 + P) * 40 + dt * 4 + wave) * 1024 + lane * 16, u4{o[4 * P], o[4 * P + 1], o[4 * P + 2], o[4 * P + 3]});
    ctr_arrive(ctl + C_G2 * 4, lane);
    ctr_wait(ctl + C_G2 * 4, 4);
    W2_DMA(4);
    waitv<4>();
    if (ctr_arrive(ctl + C_G3 * 4, lane) == 3 && lane < 8) st8_sc1(rws, WS_F2 + lane * 4096 + (dt * 24 + ft) * 16, tag);
    STAMP(44 + wave);
    W2_DMA(5); waitv<4>(); W2_DMA(6);
  } else {
#pragma unroll
    for (int t = 0; t < 4; ++t) vow[t] = (8 * t + (lane >> 3)) * 4096 + st * 128 + (((lane & 7) ^ ((lane >> 3) & 6)) * 16) - 1024 * t;
    asm volatile("" : "+v"(pfv));
    ctr_wait(ctl + C_GO1 * 4, 1);
    W2_DMA(0); W2_DMA(1);
    waitv<4>(); W2_DMA(2); waitv<4>(); W2_DMA(3); waitv<4>(); W2_DMA(4); waitv<4>(); W2_DMA(5); waitv<4>(); W2_DMA(6);
  }

  {
    const int fsw = 4 * (g & 1) + (q & 2);
#pragma unroll
    for (int m = 0; m < 4; ++m) tra[m] = ring + (4 * g + q) * 128 + (((2 * m) ^ fsw) + (p >> 1)) * 16 + (p & 1) * 8;
  }
  const int s = st * 64 + wave * 16 + (lane & 15);
  const size_t xo = (size_t)s * 1280 + (dt2 * 80 + 4 * g) * 2;
  u2 xr[3];
#pragma unroll
  for (int m = 0; m < 3; ++m) xr[m] = *(const u2*)(x + xo + m * 32);
  u2 stt = ld8_sc1(rws, WS_ST + s * 8);
  unsigned b2r = *(const unsigned short*)(b2 + s * 2);
  const unsigned vou0 = WS_U + dt2 * 5120 + lane * 16, vou1 = vou0 + 4096;
  u4 uq[3][5];
  f4 acc[5][4];
#pragma unroll
  for (int m = 0; m < 5; ++m)
#pragma unroll
    for (int n = 0; n < 4; ++n) acc[m][n] = f4{0.f, 0.f, 0.f, 0.f};
#define U_LOAD(j)                                                                                                            \
  asm volatile("buffer_load_dwordx4 %0, %5, %7, %8 offen sc1\n\t"                                                            \
               "buffer_load_dwordx4 %1, %5, %7, %8 offen offset:1024 sc1\n\t"                                                \
               "buffer_load_dwordx4 %2, %5, %7, %8 offen offset:2048 sc1\n\t"                                                \
               "buffer_load_dwordx4 %3, %5, %7, %8 offen offset:3072 sc1\n\t"                                                \
               "buffer_load_dwordx4 %4, %6, %7, %8 offen sc1"                                                                \
               : "=&v"(uq[(j) % 3][0]), "=&v"(uq[(j) % 3][1]), "=&v"(uq[(j) % 3][2]), "=&v"(uq[(j) % 3][3]), "=&v"(uq[(j) % 3][4]) \
               : "v"(vou0), "v"(vou1), "s"(srdS), "s"(KS2(j) * 40960u)                                                       \
               : "memory")
#define U_WAIT(N, us) asm volatile("s_waitcnt vmcnt(%5)" : "+v"(us[0]), "+v"(us[1]), "+v"(us[2]), "+v"(us[3]), "+v"(us[4]) : "i"(N) : "memory")
#define G2_READ(j) _Pragma("unroll") for (int n = 0; n < 4; ++n) wf[(j) & 1][n] = cat8(tr_read(tra[n] + (((j) + 2) % 7) * 4096), tr_read(tra[n] + (((j) + 2) % 7) * 4096 + 2048))
#define G2_STEP(j, N)                                                                                                        \
  do {                                                                                                                       \
    if ((j) + 1 < 12) { U_WAIT(N, uq[((j) + 1) % 3]); G2_READ((j) + 1); }                                                    \
    SCHED_FENCE();                                                                                                           \
    _Pragma("unroll") for (int m = 0; m < 5; ++m)                                                                            \
    _Pragma("unroll") for (int n = 0; n < 4; ++n) acc[m][n] = mfma16(__builtin_bit_cast(bf8, uq[(j) % 3][m]), wf[(j) & 1][n], acc[m][n]); \
    SCHED_FENCE();                                                                                                           \
    if ((j) <= 4) W2_DMA((j) + 7);                                                                                           \
    if ((j) <= 8) U_LOAD((j) + 3);                                                                                           \
  } while (0)
  asm volatile("" : "+v"(xr[0]), "+v"(xr[1]), "+v"(xr[2]), "+v"(stt), "+v"(b2r));
  ctr_wait(ctl + C_GO2 * 4, 1);
  STAMP(48 + wave);
  U_LOAD(0); U_LOAD(1); U_LOAD(2);
  U_WAIT(10, uq[0]);
  G2_READ(0);
  G2_STEP(0, 5); G2_STEP(1, 9); G2_STEP(2, 9); G2_STEP(3, 9); G2_STEP(4, 9); G2_STEP(5, 9); G2_STEP(6, 5); G2_STEP(7, 5);
  G2_STEP(8, 5); G2_STEP(9, 5); G2_STEP(10, 0); G2_STEP(11, 0);
  STAMP(52 + wave);
#pragma unroll
  for (int n = 0; n < 4; ++n) {
    if (n != wave) {
#pragma unroll
      for (int m = 0; m < 3; ++m) lds_st<f4>(ring + (n * 5 + m) * 1024 + lane * 16, acc[m][n]);
    }
#pragma unroll
    for (int m = 3; m < 5; ++m) lds_st<f4>(ring + (n * 5 + m) * 1024 + lane * 16, acc[m][n]);
  }
  f4 own[3];
#pragma unroll
  for (int m = 0; m < 3; ++m) own[m] = sel4(wave & 2, sel4(wave & 1, acc[m][3], acc[m][2]), sel4(wave & 1, acc[m][1], acc[m][0]));
  raw_barrier();
  STAMP(56 + wave);
#pragma unroll
  for (int w = 1; w < 4; ++w) {
    const unsigned a = sb + ((wave + w) & 3) * L_RING + wave * 5120 + lane * 16;
#pragma unroll
    for (int m = 0; m < 3; ++m) own[m] += lds_ld<f4>(a + m * 1024);
  }
  final_tiles<0, 3>(own, xr, stt, b2r, sb + L_YA + (4 * g + q) * 128 + wave * 32 + p * 8, out + xo);
  {
    unsigned mn = 0xffffffffu;
    for (int i = 0; i < 4; ++i) { u2 t = ld8_sc1(rws, 1024 + (lane + 64 * i) * 8); unsigned d = t.x - (unsigned)pad_t0 + 100000u; mn = d < mn ? d : mn; }
    for (int o = 32; o; o >>= 1) { unsigned y = __shfl_xor(mn, o); mn = y < mn ? y : mn; }
    const unsigned long long first = pad_t0 + mn - 100000u;
    while (__builtin_amdgcn_s_memrealtime() - first < 5000) __builtin_amdgcn_s_sleep(1);
  }
}

extern "C" void kernel_launch(void* const* d_in, const int* in_sizes, int n_in, void* d_out, int out_size, void* d_ws, size_t ws_size,
                              hipStream_t stream) {
  (void)hipFuncSetAttribute((const void*)fused_kernel, hipFuncAttributeMaxDynamicSharedMemorySize, L_TOTAL);
  fused_kernel<<<256, 512, L_TOTAL, stream>>>((const char*)d_in[0], (const char*)d_in[1], (const char*)d_in[2], (const char*)d_in[3],
                                              (const char*)d_in[4], (const char*)d_in[5], (const char*)d_in[7], (const char*)d_in[8],
                                              (const char*)d_in[9], (const char*)d_in[10], (char*)d_out, (char*)d_ws);
}
```

```cpp
#include <hip/hip_runtime.h>
#include <hip/hip_bf16.h>
#include <stdint.h>

typedef __attribute__((ext_vector_type(8))) __bf16 bf8;
typedef __attribute__((ext_vector_type(2))) __bf16 bf2;
typedef __attribute__((ext_vector_type(4))) short s4;
typedef __attribute__((ext_vector_type(4))) float f4;
typedef __attribute__((ext_vector_type(2))) float f2;
typedef __attribute__((ext_vector_type(4))) unsigned u4;
typedef __attribute__((ext_vector_type(2))) unsigned u2;
#define LAS __attribute__((address_space(3)))
#define DEVI __device__ __forceinline__
#define SCHED_FENCE() __builtin_amdgcn_sched_barrier(0)

#define PF_W2 1
#define NT_FIRST 2

constexpr unsigned WS_EPOCH = 0;
constexpr unsigned WS_F1 = 4096;
constexpr unsigned WS_F2 = WS_F1 + 32768;
constexpr unsigned WS_F3 = WS_F2 + 32768;
constexpr unsigned WS_ST = WS_F3 + 8192;
constexpr unsigned WS_H = WS_ST + 16384;
constexpr unsigned WS_KV = WS_H + 2621440;
constexpr unsigned WS_YA = WS_KV + 2621440;
constexpr unsigned WS_U = WS_YA + 2621440;
constexpr unsigned WS_END = WS_U + 1966080;
constexpr size_t WS_DBG = WS_END;

#define STAMP(idx) do {} while (0)

constexpr unsigned L_RING = 28672;
constexpr unsigned CV_CP = 4160;
constexpr unsigned CV_X = 4 * CV_CP;
constexpr unsigned CV_BYTES = 21248;
constexpr unsigned L_CONV = 4 * L_RING;
constexpr unsigned L_HBUF = L_CONV;
constexpr unsigned L_KBUF = L_CONV + 10752;
constexpr unsigned L_VBUF = L_KBUF + 11264;
constexpr unsigned L_YA = L_CONV + CV_BYTES;
constexpr unsigned L_CTL = L_CONV + 2 * CV_BYTES;
constexpr unsigned L_TOTAL = L_CTL + 128;
enum { C_H = 0, C_GO1, C_G1, C_G2, C_G3, C_GO2, C_P0, C_P1, C_TAG0, C_TAG1 };
constexpr unsigned SPIN_CAP = 1u << 16;

DEVI float blo(unsigned u) { return __uint_as_float(u << 16); }
DEVI float bhi(unsigned u) { return __uint_as_float(u & 0xffff0000u); }
DEVI unsigned pk(float a, float b) { f2 v = {a, b}; return __builtin_bit_cast(unsigned, __builtin_convertvector(v, bf2)); }
DEVI float rbf(float a) { return blo(pk(a, a)); }
template <int CTRL, int RM> DEVI float dppf(float x) {
  return __builtin_bit_cast(float, __builtin_amdgcn_update_dpp(0, __builtin_bit_cast(int, x), CTRL, RM, 0xf, true));
}
template <class T> DEVI T ldnt(const void* p) { return __builtin_nontemporal_load((const T*)p); }
DEVI u4 make_srd(const void* p, unsigned bytes) {
  uint64_t a = (uint64_t)p;
  u4 r = {(unsigned)a, (unsigned)(a >> 32) & 0xffffu, bytes, 0x00020000u};
  return r;
}
DEVI s4 tr_read(unsigned addr) { return __builtin_amdgcn_ds_read_tr16_b64_v4i16((LAS s4*)(size_t)addr); }
DEVI bf8 cat8(s4 a, s4 b) {
  typedef __attribute__((ext_vector_type(8))) short s8;
  s8 v = __builtin_shufflevector(a, b, 0, 1, 2, 3, 4, 5, 6, 7);
  return __builtin_bit_cast(bf8, v);
}
DEVI f4 mfma16(bf8 a, bf8 b, f4 c) { return __builtin_amdgcn_mfma_f32_16x16x32_bf16(a, b, c, 0, 0, 0); }
template <int N> DEVI void waitv() { asm volatile("s_waitcnt vmcnt(%0)" ::"i"(N) : "memory"); }
template <class T> DEVI T lds_ld(unsigned a) { return *(const LAS T*)(size_t)a; }
template <class T> DEVI void lds_st(unsigned a, T v) { *(LAS T*)(size_t)a = v; }
DEVI void raw_barrier() { asm volatile("s_waitcnt lgkmcnt(0)\n\ts_barrier" ::: "memory"); }
DEVI unsigned ctr_arrive(unsigned a, int lane) {
  asm volatile("s_waitcnt lgkmcnt(0)" ::: "memory");
  unsigned old = 0;
  if (lane == 0) old = __hip_atomic_fetch_add((LAS unsigned*)(size_t)a, 1u, __ATOMIC_RELAXED, __HIP_MEMORY_SCOPE_WORKGROUP);
  old = __builtin_amdgcn_readfirstlane(old);
  asm volatile("" ::: "memory");
  return old;
}
DEVI void ctr_wait(unsigned a, unsigned n) {
  while (__builtin_amdgcn_readfirstlane(__hip_atomic_load((LAS unsigned*)(size_t)a, __ATOMIC_RELAXED, __HIP_MEMORY_SCOPE_WORKGROUP)) < n)
    __builtin_amdgcn_s_sleep(1);
  asm volatile("" ::: "memory");
}
typedef __amdgpu_buffer_rsrc_t rsrc_t;
DEVI u4 ld16_sc1(rsrc_t r, unsigned off) { return __builtin_amdgcn_raw_buffer_load_b128(r, off, 0, 16); }
DEVI u2 ld8_sc1(rsrc_t r, unsigned off) { return __builtin_amdgcn_raw_buffer_load_b64(r, off, 0, 16); }
DEVI void st16_sc1(rsrc_t r, unsigned off, u4 v) { __builtin_amdgcn_raw_buffer_store_b128(v, r, off, 0, 16); }
DEVI void st8_sc1(rsrc_t r, unsigned off, u2 v) { __builtin_amdgcn_raw_buffer_store_b64(v, r, off, 0, 16); }

DEVI f4 sel4(bool c, f4 a, f4 b) {
  asm("" : "+v"(a)); asm("" : "+v"(b));
  return c ? a : b;
}
DEVI float gelu_t(float x) {
  const float t = x * __builtin_fmaf(x * x, -0.10294324f, -2.3022082f);
  return x * __builtin_amdgcn_rcpf(1.f + __builtin_amdgcn_exp2f(t));
}

DEVI void ln_row(const unsigned (&xr)[5], unsigned (&hp)[5], float& m1, float& inv1) {
  float e[10], s = 0.f, q = 0.f;
  const float piv = blo(__builtin_amdgcn_readfirstlane(xr[0]));
#pragma unroll
  for (int j = 0; j < 5; ++j) { e[2 * j] = blo(xr[j]); e[2 * j + 1] = bhi(xr[j]); }
#pragma unroll
  for (int j = 0; j < 10; ++j) { const float d = e[j] - piv; s += d; q = __builtin_fmaf(d, d, q); }
  s += dppf<0xB1, 0xf>(s);  q += dppf<0xB1, 0xf>(q);
  s += dppf<0x4E, 0xf>(s);  q += dppf<0x4E, 0xf>(q);
  s += dppf<0x141, 0xf>(s); q += dppf<0x141, 0xf>(q);
  s += dppf<0x140, 0xf>(s); q += dppf<0x140, 0xf>(q);
  s += dppf<0x142, 0xa>(s); q += dppf<0x142, 0xa>(q);
  s += dppf<0x143, 0xc>(s); q += dppf<0x143, 0xc>(q);
  const float md = __builtin_bit_cast(float, __builtin_amdgcn_readlane(__builtin_bit_cast(int, s), 63)) * (1.f / 640.f);
  const float qq = __builtin_bit_cast(float, __builtin_amdgcn_readlane(__builtin_bit_cast(int, q), 63)) * (1.f / 640.f);
  const float var = __builtin_fmaxf(qq - md * md, 0.f), mean = piv + md;
  m1 = rbf(mean);
  inv1 = __builtin_amdgcn_rcpf(rbf(__builtin_amdgcn_sqrtf(rbf(rbf(var) + 1e-5f))));
  const float m2 = rbf((mean - m1) * inv1);
  const float inv2 = __builtin_amdgcn_rcpf(rbf(__builtin_amdgcn_sqrtf(rbf(rbf(var * inv1 * inv1) + 1e-5f))));
  const float A = inv1 * inv2, B = -m2 * inv2;
#pragma unroll
  for (int j = 0; j < 5; ++j) hp[j] = pk(__builtin_fmaf(e[2 * j] - m1, A, B), __builtin_fmaf(e[2 * j + 1] - m1, A, B));
}

struct Filt { u4 c[2]; u2 n[2]; };
DEVI void filt_load(Filt& f, const char* krow, int half, int lane) {
#pragma unroll
  for (int j = 0; j < 2; ++j) {
    const int z = lane + 64 * j + 128 * half;
    f.c[j] = ldnt<u4>(krow + z * 16);
    f.n[j] = ldnt<u2>(krow + (z < 255 ? z * 16 + 16 : 0));
  }
}
DEVI void filt_build(Filt& f, unsigned cv, int half, int lane) {
  const u4 zero = {0u, 0u, 0u, 0u};
  if (half == 1) {
    if (lane == 63) f.n[1] = u2{0u, 0u};
    if (lane < 30) lds_st<u4>(cv + CV_X + 4096 + lane * 16, zero);
  } else {
    if (lane < 4) lds_st<u4>(cv + lane * CV_CP, zero);
    if (lane == 0) {
      const unsigned d4 = f.c[0].x, d5 = f.c[0].y;
      lds_st<u4>(cv + 16, u4{0u, 0u, 0u, d4 << 16});
      lds_st<u4>(cv + CV_CP + 16, u4{0u, 0u, 0u, d4});
      lds_st<u4>(cv + 2 * CV_CP + 16, u4{0u, 0u, d4 << 16, __builtin_amdgcn_alignbit(d5, d4, 16)});
      lds_st<u4>(cv + 3 * CV_CP + 16, u4{0u, 0u, d4, d5});
    }
  }
#pragma unroll
  for (int j = 0; j < 2; ++j) {
    const u4 c = f.c[j]; const u2 n = f.n[j];
    const unsigned a0 = __builtin_amdgcn_alignbit(c.y, c.x, 16), a1 = __builtin_amdgcn_alignbit(c.z, c.y, 16), a2 = __builtin_amdgcn_alignbit(c.w, c.z, 16),
                   a3 = __builtin_amdgcn_alignbit(n.x, c.w, 16), a4 = __builtin_amdgcn_alignbit(n.y, n.x, 16);
    const unsigned p = cv + (lane + 64 * j + 128 * half + 2) * 16;
    lds_st<u4>(p, u4{a0, a1, a2, a3});
    lds_st<u4>(p + CV_CP, u4{c.y, c.z, c.w, n.x});
    lds_st<u4>(p + 2 * CV_CP, u4{a1, a2, a3, a4});
    lds_st<u4>(p + 3 * CV_CP, u4{c.z, c.w, n.x, n.y});
  }
}

template <int M0, int NM>
DEVI void final_tiles(f4 (&own)[NM], const u2 (&xr)[NM], u2 stt, unsigned b2r, unsigned yab, char* op) {
  const float bb = blo(b2r), m1 = __uint_as_float(stt.x), inv1 = __uint_as_float(stt.y);
#pragma unroll
  for (int m = 0; m < NM; ++m) {
    const u2 ya = __builtin_bit_cast(u2, tr_read(yab + (M0 + m) * 2048));
    const float x0 = blo(xr[m].x), x1 = bhi(xr[m].x), x2 = blo(xr[m].y), x3 = bhi(xr[m].y);
    const float r0 = __builtin_fmaf((x0 - m1) * inv1, blo(ya.x), x0) + (own[m][0] + bb), r1 = __builtin_fmaf((x1 - m1) * inv1, bhi(ya.x), x1) + (own[m][1] + bb);
    const float r2 = __builtin_fmaf((x2 - m1) * inv1, blo(ya.y), x2) + (own[m][2] + bb), r3 = __builtin_fmaf((x3 - m1) * inv1, bhi(ya.y), x3) + (own[m][3] + bb);
    { u2 ov = u2{pk(r0, r1), pk(r2, r3)}; asm volatile("global_store_dwordx2 %0, %1, off offset:%2 sc1" :: "v"(op), "v"(ov), "i"((M0 + m) * 32) : "memory"); }
  }
}

__global__ __launch_bounds__(512, 2) __attribute__((amdgpu_waves_per_eu(2, 2))) void fused_kernel(
    const char* __restrict__ x, const char* __restrict__ kk, const char* __restrict__ vv, const char* __restrict__ shk,
    const char* __restrict__ ssmk, const char* __restrict__ ssmd, const char* __restrict__ w1, const char* __restrict__ b1,
    const char* __restrict__ w2, const char* __restrict__ b2, char* __restrict__ out, char* ws) {
  extern __shared__ __attribute__((aligned(16))) char smem[];
  const int lane = threadIdx.x & 63, wave = __builtin_amdgcn_readfirstlane(threadIdx.x >> 6);
  const int b = blockIdx.x, xg = b & 7, r = b >> 3;
  const unsigned sb = __builtin_amdgcn_readfirstlane((unsigned)(size_t)smem);
  const unsigned ctl = sb + L_CTL;
  asm volatile("" ::"s"(x), "s"(kk), "s"(vv), "s"(shk), "s"(ssmk), "s"(ssmd), "s"(w1), "s"(b1), "s"(w2), "s"(b2), "s"(out), "s"(ws));
  const rsrc_t rws = __builtin_amdgcn_make_buffer_rsrc(ws, 0, WS_END, 0x00020000);
  const unsigned long long pad_t0 = __builtin_amdgcn_s_memrealtime();
  const int dt2 = r & 7, st = xg * 4 + (r >> 3);

  const u4 srdS = make_srd(ws, WS_END);
  u2 tag;
  {
    unsigned xr[5];
    asm volatile("buffer_load_dword %0, %5, %6, %7 offen nt\n\tbuffer_load_dword %1, %5, %6, %7 offen offset:256 nt\n\t"
                 "buffer_load_dword %2, %5, %6, %7 offen offset:512 nt\n\tbuffer_load_dword %3, %5, %6, %7 offen offset:768 nt\n\t"
                 "buffer_load_dword %4, %5, %6, %7 offen offset:1024 nt"
                 : "=&v"(xr[0]), "=&v"(xr[1]), "=&v"(xr[2]), "=&v"(xr[3]), "=&v"(xr[4])
                 : "v"(lane * 4), "s"(make_srd(x, 2621440)), "s"((b * 8 + wave) * 1280)
                 : "memory");
    {
      const u4 srdK = make_srd(kk, 2621440), srdV = make_srd(vv, 2621440);
#pragma unroll
      for (int i = 0; i < 3; ++i) {
        const int pc = wave + 8 * i;
        if (pc < 22) {
          const bool isv = pc >= 11;
          const int n = (isv ? pc - 11 : pc) * 64 + lane, g8 = n / 81, rem = n - g8 * 81, rr = rem / 10;
          const int row = g8 * 8 + rr, c = rem - rr * 10;
          const int grow = r * 64 + row - (isv ? 0 : 3);
          const bool ok = rem < 80 && grow >= 0 && row < (isv ? 64 : 67);
          const unsigned vo = ok ? grow * 1280 + xg * 160 + c * 16 : 0x7fffff00u;
          const unsigned dst = sb + (isv ? L_VBUF - 11264 : L_KBUF) + pc * 1024;
          if (isv) asm volatile("s_mov_b32 m0, %0\n\ts_nop 0\n\tbuffer_load_dwordx4 %1, %2, 0 offen nt lds" ::"s"(dst), "v"(vo), "s"(srdV) : "memory");
          else     asm volatile("s_mov_b32 m0, %0\n\ts_nop 0\n\tbuffer_load_dwordx4 %1, %2, 0 offen nt lds" ::"s"(dst), "v"(vo), "s"(srdK) : "memory");
        }
      }
    }
    u2 ep = ld8_sc1(rws, WS_EPOCH);
    u4 shw = *(const u4*)(shk + (xg * 80 + (wave >= 3 ? wave - 3 : 0) * 16 + (lane >> 3) * 2) * 8);
    SCHED_FENCE();
    if (wave == 0 && lane < 8) lds_st<unsigned>(ctl + lane * 4, 0u);
    asm volatile("s_waitcnt vmcnt(4)" : "+v"(xr[0]), "+v"(xr[1]), "+v"(xr[2]), "+v"(xr[3]), "+v"(xr[4])::"memory");
    STAMP(1);
    unsigned hp[5]; float m1, inv1;
    ln_row(xr, hp, m1, inv1);
#pragma unroll
    for (int j = 0; j < 5; ++j) lds_st<unsigned>(sb + L_HBUF + wave * 1344 + (lane + 64 * j) * 4, hp[j]);
    STAMP(2);
    asm volatile("s_waitcnt vmcnt(0)" : "+v"(ep), "+v"(shw)::"memory");
    const uint64_t tg = (((uint64_t)ep.y << 32) | ep.x) * 6364136223846793005ull + 1442695040888963407ull;
    tag = u2{(unsigned)__builtin_amdgcn_readfirstlane((unsigned)tg), (unsigned)__builtin_amdgcn_readfirstlane((unsigned)(tg >> 32))};
    STAMP(3);
    raw_barrier();
    if (lane == 0) st8_sc1(rws, WS_ST + (b * 8 + wave) * 8, u2{__float_as_uint(m1), __float_as_uint(inv1)});
    if (wave < 3) {
      const unsigned tb = sb + L_HBUF + ((lane >> 2) & 3) * 1344 + (lane >> 4) * 32 + (lane & 3) * 8;
      u4 v[4];
#pragma unroll
      for (int i = 0; i < 4; ++i) { const int it = i < 3 ? wave + 3 * i : 9; v[i] = __builtin_bit_cast(u4, cat8(tr_read(tb + it * 128), tr_read(tb + it * 128 + 4 * 1344))); }
#pragma unroll
      for (int i = 0; i < 4; ++i)
        if (i < 3 || wave == 0) st16_sc1(rws, WS_H + (b * 640 + (i < 3 ? wave + 3 * i : 9) * 64 + lane) * 16, v[i]);
    } else {
      const int rg = lane & 7, cp = (wave - 3) * 8 + (lane >> 3);
      const unsigned ka = sb + L_KBUF + rg * 1296 + cp * 4;
      unsigned kr[11], vr[8];
#pragma unroll
      for (int i = 0; i < 11; ++i) kr[i] = lds_ld<unsigned>(ka + i * 160 + (i >> 3) * 16);
#pragma unroll
      for (int i = 0; i < 8; ++i) vr[i] = lds_ld<unsigned>(ka + 11264 + i * 160);
#pragma unroll
      for (int c = 0; c < 2; ++c) {
        const unsigned wa = c ? shw.z : shw.x, wb = c ? shw.w : shw.y;
        const float w0 = blo(wa), w1f = bhi(wa), w2f = blo(wb), w3 = bhi(wb);
        float kf[11], o[8];
#pragma unroll
        for (int i = 0; i < 11; ++i) kf[i] = c ? bhi(kr[i]) : blo(kr[i]);
#pragma unroll
        for (int i = 0; i < 8; ++i) o[i] = (kf[i] * w3 + kf[i + 1] * w2f + kf[i + 2] * w1f + kf[i + 3] * w0) * (c ? bhi(vr[i]) : blo(vr[i]));
        st16_sc1(rws, WS_KV + (xg * 80 + cp * 2 + c) * 4096 + (2040 - 64 * r - 8 * rg) * 2, u4{pk(o[7], o[6]), pk(o[5], o[4]), pk(o[3], o[2]), pk(o[1], o[0])});
      }
    }
    STAMP(4);
  }

  if (wave >= 4) {
    const int hw = wave - 4, pair = hw >> 1, half = hw & 1;
    Filt f;
    filt_load(f, ssmk + (size_t)(2 * b + pair) * 4096, half, lane);
    waitv<4>();
    if (ctr_arrive(ctl + C_H * 4, lane) == 7 && lane < 8) st8_sc1(rws, WS_F1 + lane * 4096 + b * 16, tag);
    STAMP(20 + hw);
    ctr_wait(ctl + C_H * 4, 8);
    const unsigned cv = sb + L_CONV + pair * CV_BYTES;
    filt_build(f, cv, half, lane);
    if (hw == 3) {
#pragma unroll 1
      for (unsigned n = 0; n < SPIN_CAP; ++n) {
        bool ok = true;
#pragma unroll
        for (int i = 0; i < 4; ++i) { const u2 a = ld8_sc1(rws, WS_F1 + xg * 4096 + i * 1024 + lane * 16); ok = ok && a.x == tag.x && a.y == tag.y; }
        if (__all(ok)) break;
        asm volatile("" ::: "memory");
      }
      asm volatile("" ::: "memory");
      if (lane == 0) lds_st<unsigned>(ctl + C_GO1 * 4, 1u);
      if (b == 0 && lane == 0) st8_sc1(rws, WS_EPOCH, tag);
      STAMP(24);
    } else {
      ctr_wait(ctl + C_GO1 * 4, 1);
    }
    const int nrounds = (pair == 0 && b >= 128) ? 2 : 1;
    const int c16 = lane & 15, kg = lane >> 4;
    for (int round = 0; round < nrounds; ++round) {
      const int ch = round == 0 ? 2 * b + pair : 384 + b;
      const unsigned pc = ctl + (C_P0 + pair) * 4, pbase = round * 8;
      if (round) filt_load(f, ssmk + (size_t)ch * 4096, half, lane);
      u4 xv[2];
#pragma unroll
      for (int j = 0; j < 2; ++j) xv[j] = ld16_sc1(rws, WS_KV + ch * 4096 + (lane + 64 * j + 128 * half) * 16);
      const float dd = blo(*(const unsigned short*)(ssmd + ch * 2));
      if (round) filt_build(f, cv, half, lane);
#pragma unroll
      for (int j = 0; j < 2; ++j) lds_st<u4>(cv + CV_X + (lane + 64 * j + 128 * half) * 16, xv[j]);
      ctr_arrive(pc, lane);
      ctr_wait(pc, pbase + 2);
      const unsigned ab = cv + (c16 & 3) * CV_CP + (c16 >> 2) * 8 + kg * 16 + half * 256;
      const unsigned bb = cv + CV_X + 480 - 32 * c16 + 16 * kg + half * 256;
      f4 acc[8];
#pragma unroll
      for (int n = 0; n < 8; ++n) acc[n] = f4{0.f, 0.f, 0.f, 0.f};
      bf8 A0[8], B0[8], A1[8], B1[8];
#define CV_LOAD(A, B, q)                                                                              \
  _Pragma("unroll") for (int p = 0; p < 8; ++p) {                                                      \
    B[p] = *(const volatile LAS bf8*)(size_t)(bb + 3584 - 512 * p + 64 * (q));                          \
    s4 lo = *(const volatile LAS s4*)(size_t)(ab + 512 * p + 64 * (q));                                 \
    s4 hi = *(const volatile LAS s4*)(size_t)(ab + 512 * p + 64 * (q) + 8);                             \
    A[p] = cat8(lo, hi);                                                                               \
  }
#define CV_MMA(A, B)                                                                                   \
  _Pragma("unroll") for (int p = 0; p < 8; ++p)                                                        \
  _Pragma("unroll") for (int m = 0; m + p < 8; ++m) acc[p + m] = mfma16(A[p], B[m], acc[p + m]);
#define CV_SCHED()                                                                                     \
  _Pragma("unroll") for (int z = 0; z < 24; ++z) {                                                     \
    __builtin_amdgcn_sched_group_barrier(0x8, 1, 0);                                                   \
    __builtin_amdgcn_sched_group_barrier(0x100, 1, 0);                                                 \
  }                                                                                                    \
  __builtin_amdgcn_sched_group_barrier(0x8, 12, 0);                                                    \
  SCHED_FENCE();
      CV_LOAD(A0, B0, 0);
      SCHED_FENCE();
#pragma unroll 1
      for (int q = 0; q < 4; q += 2) {
        CV_LOAD(A1, B1, q + 1); CV_MMA(A0, B0); CV_SCHED();
        CV_LOAD(A0, B0, (q + 2) & 3); CV_MMA(A1, B1); CV_SCHED();
      }
      if (round == 0) STAMP(25 + hw);
      ctr_arrive(pc, lane);
      ctr_wait(pc, pbase + 4);
#pragma unroll
      for (int n = 0; n < 4; ++n) lds_st<f4>(cv + half * 4096 + n * 1024 + lane * 16, sel4(half, acc[n], acc[n + 4]));
      ctr_arrive(pc, lane);
      ctr_wait(pc, pbase + 6);
#pragma unroll
      for (int n = 0; n < 4; ++n) {
        const f4 y = sel4(half, acc[n + 4], acc[n]) + lds_ld<f4>(cv + (half ^ 1) * 4096 + n * 1024 + lane * 16);
        const int t0 = 1024 * half + 256 * n + 16 * c16 + 4 * kg;
        const u2 xq = lds_ld<u2>(cv + CV_X + (2044 - t0) * 2);
        lds_st<u2>(cv + 8192 + t0 * 2, u2{pk(__builtin_fmaf(bhi(xq.y), dd, y[0]), __builtin_fmaf(blo(xq.y), dd, y[1])),
                                          pk(__builtin_fmaf(bhi(xq.x), dd, y[2]), __builtin_fmaf(blo(xq.x), dd, y[3]))});
      }
#pragma unroll
      for (int n = 0; n < 2; ++n) {
        const unsigned o = half * 2048 + n * 1024 + lane * 16;
        st16_sc1(rws, WS_YA + ch * 4096 + o, lds_ld<u4>(cv + 8192 + o));
      }
      waitv<0>();
      if (ctr_arrive(pc, lane) == pbase + 7 && lane == 0) st8_sc1(rws, WS_F3 + ch * 8, tag);
      ctr_wait(pc, pbase + 8);
    }
    STAMP(32 + hw);
    const int s = st * 64 + hw * 16 + c16;
    const size_t xo = (size_t)s * 1280 + (dt2 * 80 + 4 * kg) * 2;
    u2 xq[2] = {*(const u2*)(x + xo + 96), *(const u2*)(x + xo + 128)};
    u2 stt = ld8_sc1(rws, WS_ST + s * 8);
    unsigned b2r = *(const unsigned short*)(b2 + s * 2);
    asm volatile("" : "+v"(xq[0]), "+v"(xq[1]), "+v"(stt), "+v"(b2r));
    if (hw == 3) {
      const int dta = (dt2 * 5) >> 2;
#pragma unroll 1
      for (unsigned n = 0; n < SPIN_CAP; ++n) {
        const u2 a = ld8_sc1(rws, WS_F2 + xg * 4096 + (dta * 24 + (lane < 48 ? lane : 0)) * 16);
        if (__all(a.x == tag.x && a.y == tag.y)) break;
        asm volatile("" ::: "memory");
      }
      asm volatile("" ::: "memory");
      if (lane == 0) lds_st<unsigned>(ctl + C_GO2 * 4, 1u);
      STAMP(36);
#pragma unroll 1
      for (unsigned n = 0; n < SPIN_CAP; ++n) {
        const u4 a = ld16_sc1(rws, WS_F3 + dt2 * 640 + (lane < 40 ? lane : 0) * 16);
        if (__all(a.x == tag.x && a.y == tag.y && a.z == tag.x && a.w == tag.y)) break;
        __builtin_amdgcn_s_sleep(2);
        asm volatile("" ::: "memory");
      }
      asm volatile("" ::: "memory");
      STAMP(37);
      u4 y[10];
#pragma unroll
      for (int i = 0; i < 10; ++i) y[i] = ld16_sc1(rws, WS_YA + (dt2 * 80 + i * 8 + (lane >> 3)) * 4096 + st * 128 + (lane & 7) * 16);
      SCHED_FENCE();
#pragma unroll
      for (int i = 0; i < 10; ++i) lds_st<u4>(sb + L_YA + i * 1024 + lane * 16, y[i]);
      STAMP(38);
    }
    raw_barrier();
    f4 own[2];
#pragma unroll
    for (int m = 0; m < 2; ++m) {
      const unsigned a = sb + (hw * 5 + 3 + m) * 1024 + lane * 16;
      own[m] = (lds_ld<f4>(a) + lds_ld<f4>(a + L_RING)) + (lds_ld<f4>(a + 2 * L_RING) + lds_ld<f4>(a + 3 * L_RING));
    }
    final_tiles<3, 2>(own, xq, stt, b2r, sb + L_YA + (4 * kg + ((lane >> 2) & 3)) * 128 + hw * 32 + (lane & 3) * 8, out + xo);
    return;
  }

  const int g = lane >> 4, q = (lane >> 2) & 3, p = lane & 3;
  const unsigned ring = sb + wave * L_RING;
  const bool has1 = r < 30;
  const int dt = r % 10, ft = xg * 3 + r / 10;
  const int rot1 = dt * 16 / 10, rot2 = dt2 * 12 / 8;
  const u4 srdW1 = make_srd(w1, 6291456), srdW2 = make_srd(w2, 6291456);
  unsigned vow[4];
#define DMA4(POL, slot, srd, soff)                                                                                             \
  asm volatile("s_mov_b32 m0, %0\n\ts_nop 0\n\t"                                                                               \
               "buffer_load_dwordx4 %1, %5, %6 offen" POL " lds\n\t"                                                           \
               "buffer_load_dwordx4 %2, %5, %6 offen offset:1024" POL " lds\n\t"                                               \
               "buffer_load_dwordx4 %3, %5, %6 offen offset:2048" POL " lds\n\t"                                               \
               "buffer_load_dwordx4 %4, %5, %6 offen offset:3072" POL " lds" ::"s"(ring + (slot) * 4096),                      \
               "v"(vow[0]), "v"(vow[1]), "v"(vow[2]), "v"(vow[3]), "s"(srd), "s"(soff)                                         \
               : "memory")
#define KS1(i) (4u * (((i) + rot1) & 15) + wave)
#define KS2(j) (4u * ((j) + rot2 >= 12 ? (j) + rot2 - 12 : (j) + rot2) + wave)
#define W1_DMA(i) do { if ((i) < NT_FIRST) DMA4(" nt", (i) % 7, srdW1, KS1(i) * 98304u); else DMA4("", (i) % 7, srdW1, KS1(i) * 98304u); } while (0)
#define W2_DMA(j) do { if (!PF_W2 && (j) < NT_FIRST) DMA4(" nt", ((j) + 2) % 7, srdW2, KS2(j) * 131072u); else DMA4("", ((j) + 2) % 7, srdW2, KS2(j) * 131072u); } while (0)
#pragma unroll
  for (int t = 0; t < 4; ++t) vow[t] = (8 * t + (lane >> 3)) * 3072 + ft * 128 + (((lane & 7) ^ (2 * ((lane >> 4) & 1) + 4 * (t & 1))) * 16) - 1024 * t;
  waitv<0>();
  if (ctr_arrive(ctl + C_H * 4, lane) == 7 && lane < 8) st8_sc1(rws, WS_F1 + lane * 4096 + b * 16, tag);
  STAMP(5 + wave);
  ctr_wait(ctl + C_H * 4, 8);
  if (has1) {
    W1_DMA(0); W1_DMA(1);
    waitv<4>(); W1_DMA(2); waitv<4>(); W1_DMA(3); waitv<4>(); W1_DMA(4); waitv<4>(); W1_DMA(5); waitv<4>(); W1_DMA(6);
  }
  unsigned pfv = 0;
  if (PF_W2 && wave < 3) {
    const int j = r * 192 + wave * 64 + lane;
    pfv = ldnt<unsigned>(w2 + (size_t)(j >> 2) * 4096 + xg * 512 + (j & 3) * 128);
  }
  __builtin_amdgcn_s_setprio(2);
  unsigned tra[4];
  bf8 wf[2][4];

  if (has1) {
    {
      const int fsw = 2 * (q >> 1) + 4 * (g & 1);
#pragma unroll
      for (int m = 0; m < 4; ++m) tra[m] = ring + (8 * g + q) * 128 + (((2 * m) ^ fsw) + (p >> 1)) * 16 + (p & 1) * 8;
    }
    u2 bias[4];
#pragma unroll
    for (int m = 0; m < 4; ++m) bias[m] = *(const u2*)(b1 + (ft * 64 + 16 * m + 4 * g) * 2);
    const unsigned voh = WS_H + (g * 640 + dt * 64 + (lane & 15)) * 16;
    u4 hq[3][4];
    f4 acc[4][4];
#pragma unroll
    for (int m = 0; m < 4; ++m)
#pragma unroll
      for (int n = 0; n < 4; ++n) acc[m][n] = f4{0.f, 0.f, 0.f, 0.f};
#define H_LOAD(i)                                                                                                            \
  asm volatile("buffer_load_dwordx4 %0, %4, %5, %6 offen sc1\n\t"                                                            \
               "buffer_load_dwordx4 %1, %4, %5, %6 offen offset:256 sc1\n\t"                                                 \
               "buffer_load_dwordx4 %2, %4, %5, %6 offen offset:512 sc1\n\t"                                                 \
               "buffer_load_dwordx4 %3, %4, %5, %6 offen offset:768 sc1"                                                     \
               : "=&v"(hq[(i) % 3][0]), "=&v"(hq[(i) % 3][1]), "=&v"(hq[(i) % 3][2]), "=&v"(hq[(i) % 3][3])                   \
               : "v"(voh), "s"(srdS), "s"(KS1(i) * 40960u)                                                                   \
               : "memory")
#define H_WAIT(N, hs) asm volatile("s_waitcnt vmcnt(%4)" : "+v"(hs[0]), "+v"(hs[1]), "+v"(hs[2]), "+v"(hs[3]) : "i"(N) : "memory")
#define G1_READ(i) _Pragma("unroll") for (int m = 0; m < 4; ++m) wf[(i) & 1][m] = cat8(tr_read(tra[m] + ((i) % 7) * 4096), tr_read(tra[m] + ((i) % 7) * 4096 + 512))
    asm volatile("" : "+v"(bias[0]), "+v"(bias[1]), "+v"(bias[2]), "+v"(bias[3]), "+v"(pfv));
    ctr_wait(ctl + C_GO1 * 4, 1);
    STAMP(9 + wave);
    H_LOAD(0); H_LOAD(1); H_LOAD(2);
    H_WAIT(8, hq[0]);
    G1_READ(0);
#define G1_STEP(i, N)                                                                                                        \
  do {                                                                                                                       \
    if ((i) + 1 < 16) { H_WAIT(N, hq[((i) + 1) % 3]); G1_READ((i) + 1); }                                                    \
    SCHED_FENCE();                                                                                                           \
    _Pragma("unroll") for (int m = 0; m < 4; ++m)                                                                            \
    _Pragma("unroll") for (int n = 0; n < 4; ++n) acc[m][n] = mfma16(wf[(i) & 1][m], __builtin_bit_cast(bf8, hq[(i) % 3][n]), acc[m][n]); \
    SCHED_FENCE();                                                                                                           \
    if ((i) <= 8) W1_DMA((i) + 7); else if ((i) <= 12) W2_DMA((i) - 9);                                                      \
    if ((i) <= 12) H_LOAD((i) + 3);                                                                                          \
  } while (0)
    G1_STEP(0, 4); G1_STEP(1, 8); G1_STEP(2, 8); G1_STEP(3, 8); G1_STEP(4, 8); G1_STEP(5, 8); G1_STEP(6, 8); G1_STEP(7, 8);
    G1_STEP(8, 8);
#pragma unroll
    for (int t = 0; t < 4; ++t) vow[t] = (8 * t + (lane >> 3)) * 4096 + st * 128 + (((lane & 7) ^ ((lane >> 3) & 6)) * 16) - 1024 * t;
    G1_STEP(9, 8); G1_STEP(10, 8); G1_STEP(11, 8); G1_STEP(12, 8); G1_STEP(13, 8); G1_STEP(14, 0); G1_STEP(15, 0);
    STAMP(40 + wave);
#pragma unroll
    for (int n = 0; n < 4; ++n)
      if (n != wave) {
        const int k = n < wave ? n : n - 1;
        const unsigned a = ring + (k == 0 ? 6 : k - 1) * 4096 + lane * 16;
#pragma unroll
        for (int m = 0; m < 4; ++m) lds_st<f4>(a + m * 1024, acc[m][n]);
      }
    f4 own[4];
#pragma unroll
    for (int m = 0; m < 4; ++m) own[m] = sel4(wave & 2, sel4(wave & 1, acc[m][3], acc[m][2]), sel4(wave & 1, acc[m][1], acc[m][0]));
    ctr_arrive(ctl + C_G1 * 4, lane);
    ctr_wait(ctl + C_G1 * 4, 4);
#pragma unroll
    for (int w = 1; w < 4; ++w) {
      const int src = (wave + w) & 3, k = wave < src ? wave : wave - 1;
      const unsigned a = sb + src * L_RING + (k == 0 ? 6 : k - 1) * 4096 + lane * 16;
#pragma unroll
      for (int m = 0; m < 4; ++m) own[m] += lds_ld<f4>(a + m * 1024);
    }
    unsigned o[8];
#pragma unroll
    for (int m = 0; m < 4; ++m) {
      o[2 * m] = pk(gelu_t(own[m][0] + blo(bias[m].x)), gelu_t(own[m][1] + bhi(bias[m].x)));
      o[2 * m + 1] = pk(gelu_t(own[m][2] + blo(bias[m].y)), gelu_t(own[m][3] + bhi(bias[m].y)));
    }
#pragma unroll
    for (int P = 0; P < 2; ++P)
      st16_sc1(rws, WS_U + ((ft * 2 + P) * 40 + dt * 4 + wave) * 1024 + lane * 16, u4{o[4 * P], o[4 * P + 1], o[4 * P + 2], o[4 * P + 3]});
    ctr_arrive(ctl + C_G2 * 4, lane);
    ctr_wait(ctl + C_G2 * 4, 4);
    W2_DMA(4);
    waitv<4>();
    if (ctr_arrive(ctl + C_G3 * 4, lane) == 3 && lane < 8) st8_sc1(rws, WS_F2 + lane * 4096 + (dt * 24 + ft) * 16, tag);
    STAMP(44 + wave);
    W2_DMA(5); waitv<4>(); W2_DMA(6);
  } else {
#pragma unroll
    for (int t = 0; t < 4; ++t) vow[t] = (8 * t + (lane >> 3)) * 4096 + st * 128 + (((lane & 7) ^ ((lane >> 3) & 6)) * 16) - 1024 * t;
    asm volatile("" : "+v"(pfv));
    ctr_wait(ctl + C_GO1 * 4, 1);
    W2_DMA(0); W2_DMA(1);
    waitv<4>(); W2_DMA(2); waitv<4>(); W2_DMA(3); waitv<4>(); W2_DMA(4); waitv<4>(); W2_DMA(5); waitv<4>(); W2_DMA(6);
  }

  {
    const int fsw = 4 * (g & 1) + (q & 2);
#pragma unroll
    for (int m = 0; m < 4; ++m) tra[m] = ring + (4 * g + q) * 128 + (((2 * m) ^ fsw) + (p >> 1)) * 16 + (p & 1) * 8;
  }
  const int s = st * 64 + wave * 16 + (lane & 15);
  const size_t xo = (size_t)s * 1280 + (dt2 * 80 + 4 * g) * 2;
  u2 xr[3];
#pragma unroll
  for (int m = 0; m < 3; ++m) xr[m] = *(const u2*)(x + xo + m * 32);
  u2 stt = ld8_sc1(rws, WS_ST + s * 8);
  unsigned b2r = *(const unsigned short*)(b2 + s * 2);
  const unsigned vou0 = WS_U + dt2 * 5120 + lane * 16, vou1 = vou0 + 4096;
  u4 uq[3][5];
  f4 acc[5][4];
#pragma unroll
  for (int m = 0; m < 5; ++m)
#pragma unroll
    for (int n = 0; n < 4; ++n) acc[m][n] = f4{0.f, 0.f, 0.f, 0.f};
#define U_LOAD(j)                                                                                                            \
  asm volatile("buffer_load_dwordx4 %0, %5, %7, %8 offen sc1\n\t"                                                            \
               "buffer_load_dwordx4 %1, %5, %7, %8 offen offset:1024 sc1\n\t"                                                \
               "buffer_load_dwordx4 %2, %5, %7, %8 offen offset:2048 sc1\n\t"                                                \
               "buffer_load_dwordx4 %3, %5, %7, %8 offen offset:3072 sc1\n\t"                                                \
               "buffer_load_dwordx4 %4, %6, %7, %8 offen sc1"                                                                \
               : "=&v"(uq[(j) % 3][0]), "=&v"(uq[(j) % 3][1]), "=&v"(uq[(j) % 3][2]), "=&v"(uq[(j) % 3][3]), "=&v"(uq[(j) % 3][4]) \
               : "v"(vou0), "v"(vou1), "s"(srdS), "s"(KS2(j) * 40960u)                                                       \
               : "memory")
#define U_WAIT(N, us) asm volatile("s_waitcnt vmcnt(%5)" : "+v"(us[0]), "+v"(us[1]), "+v"(us[2]), "+v"(us[3]), "+v"(us[4]) : "i"(N) : "memory")
#define G2_READ(j) _Pragma("unroll") for (int n = 0; n < 4; ++n) wf[(j) & 1][n] = cat8(tr_read(tra[n] + (((j) + 2) % 7) * 4096), tr_read(tra[n] + (((j) + 2) % 7) * 4096 + 2048))
#define G2_STEP(j, N)                                                                                                        \
  do {                                                                                                                       \
    if ((j) + 1 < 12) { U_WAIT(N, uq[((j) + 1) % 3]); G2_READ((j) + 1); }                                                    \
    SCHED_FENCE();                                                                                                           \
    _Pragma("unroll") for (int m = 0; m < 5; ++m)                                                                            \
    _Pragma("unroll") for (int n = 0; n < 4; ++n) acc[m][n] = mfma16(__builtin_bit_cast(bf8, uq[(j) % 3][m]), wf[(j) & 1][n], acc[m][n]); \
    SCHED_FENCE();                                                                                                           \
    if ((j) <= 4) W2_DMA((j) + 7);                                                                                           \
    if ((j) <= 8) U_LOAD((j) + 3);                                                                                           \
  } while (0)
  asm volatile("" : "+v"(xr[0]), "+v"(xr[1]), "+v"(xr[2]), "+v"(stt), "+v"(b2r));
  ctr_wait(ctl + C_GO2 * 4, 1);
  STAMP(48 + wave);
  U_LOAD(0); U_LOAD(1); U_LOAD(2);
  U_WAIT(10, uq[0]);
  G2_READ(0);
  G2_STEP(0, 5); G2_STEP(1, 9); G2_STEP(2, 9); G2_STEP(3, 9); G2_STEP(4, 9); G2_STEP(5, 9); G2_STEP(6, 5); G2_STEP(7, 5);
  G2_STEP(8, 5); G2_STEP(9, 5); G2_STEP(10, 0); G2_STEP(11, 0);
  STAMP(52 + wave);
#pragma unroll
  for (int n = 0; n < 4; ++n) {
    if (n != wave) {
#pragma unroll
      for (int m = 0; m < 3; ++m) lds_st<f4>(ring + (n * 5 + m) * 1024 + lane * 16, acc[m][n]);
    }
#pragma unroll
    for (int m = 3; m < 5; ++m) lds_st<f4>(ring + (n * 5 + m) * 1024 + lane * 16, acc[m][n]);
  }
  f4 own[3];
#pragma unroll
  for (int m = 0; m < 3; ++m) own[m] = sel4(wave & 2, sel4(wave & 1, acc[m][3], acc[m][2]), sel4(wave & 1, acc[m][1], acc[m][0]));
  raw_barrier();
  STAMP(56 + wave);
#pragma unroll
  for (int w = 1; w < 4; ++w) {
    const unsigned a = sb + ((wave + w) & 3) * L_RING + wave * 5120 + lane * 16;
#pragma unroll
    for (int m = 0; m < 3; ++m) own[m] += lds_ld<f4>(a + m * 1024);
  }
  final_tiles<0, 3>(own, xr, stt, b2r, sb + L_YA + (4 * g + q) * 128 + wave * 32 + p * 8, out + xo);
  if (b == 0 && threadIdx.x == 0) st8_sc1(rws, 1024, u2{(unsigned)pad_t0, (unsigned)(pad_t0 >> 32)});
}

__global__ void tiny1(char* ws) { unsigned long long f = *(volatile unsigned long long*)(ws + 1024); while (__builtin_amdgcn_s_memrealtime() - f < 5000) __builtin_amdgcn_s_sleep(1); }
extern "C" void kernel_launch(void* const* d_in, const int* in_sizes, int n_in, void* d_out, int out_size, void* d_ws, size_t ws_size,
                              hipStream_t stream) {
  (void)hipFuncSetAttribute((const void*)fused_kernel, hipFuncAttributeMaxDynamicSharedMemorySize, L_TOTAL);
  fused_kernel<<<256, 512, L_TOTAL, stream>>>((const char*)d_in[0], (const char*)d_in[1], (const char*)d_in[2], (const char*)d_in[3],
                                              (const char*)d_in[4], (const char*)d_in[5], (const char*)d_in[7], (const char*)d_in[8],
                                              (const char*)d_in[9], (const char*)d_in[10], (char*)d_out, (char*)d_ws);
  tiny1<<<1, 64, 0, stream>>>((char*)d_ws);
}
```

```cpp
#include <hip/hip_runtime.h>
#include <hip/hip_bf16.h>
#include <stdint.h>

typedef __attribute__((ext_vector_type(8))) __bf16 bf8;
typedef __attribute__((ext_vector_type(2))) __bf16 bf2;
typedef __attribute__((ext_vector_type(4))) short s4;
typedef __attribute__((ext_vector_type(4))) float f4;
typedef __attribute__((ext_vector_type(2))) float f2;
typedef __attribute__((ext_vector_type(4))) unsigned u4;
typedef __attribute__((ext_vector_type(2))) unsigned u2;
#define LAS __attribute__((address_space(3)))
#define DEVI __device__ __forceinline__
#define SCHED_FENCE() __builtin_amdgcn_sched_barrier(0)

#define PF_W2 0
#define CONVQ 4
#define HPOL " sc1"
#define MAP1 0
#define R1 3
#define R2 3
#define THR 5
#define GPRIO 1
#define HPRIO 0
#define NT_FIRST 0

constexpr unsigned WS_EPOCH = 0;
constexpr unsigned WS_F1 = 4096;
constexpr unsigned WS_F2 = WS_F1 + 32768;
constexpr unsigned WS_F4 = WS_F2 + 32768;
constexpr unsigned WS_F3 = WS_F4 + 32768;
constexpr unsigned WS_ST = WS_F3 + 8192;
constexpr unsigned WS_H = WS_ST + 16384;
constexpr unsigned WS_KV = WS_H + 2621440;
constexpr unsigned WS_YA = WS_KV + 2621440;
constexpr unsigned WS_U = WS_YA + 2621440;
constexpr unsigned WS_END = WS_U + 1966080;
constexpr size_t WS_DBG = WS_END;

#define STAMP(idx) do {} while (0)

constexpr unsigned L_RING = 28672;
constexpr unsigned CV_CP = 4160;
constexpr unsigned CV_X = 4 * CV_CP;
constexpr unsigned CV_BYTES = 21248;
constexpr unsigned L_CONV = 4 * L_RING;
constexpr unsigned L_HBUF = L_CONV;
constexpr unsigned L_KBUF = L_CONV + 10752;
constexpr unsigned L_VBUF = L_KBUF + 11264;
constexpr unsigned L_YA = L_CONV + CV_BYTES;
constexpr unsigned L_CTL = L_CONV + 2 * CV_BYTES;
constexpr unsigned L_TOTAL = L_CTL + 128;
enum { C_H = 0, C_GO1, C_G1, C_G2, C_G3, C_GO2, C_P0, C_P1, C_Q0, C_Q1, C_KV, C_K2 };
constexpr unsigned SPIN_CAP = 1u << 16;

DEVI float blo(unsigned u) { return __uint_as_float(u << 16); }
DEVI float bhi(unsigned u) { return __uint_as_float(u & 0xffff0000u); }
DEVI unsigned pk(float a, float b) { f2 v = {a, b}; return __builtin_bit_cast(unsigned, __builtin_convertvector(v, bf2)); }
DEVI float rbf(float a) { return blo(pk(a, a)); }
template <int CTRL, int RM> DEVI float dppf(float x) {
  return __builtin_bit_cast(float, __builtin_amdgcn_update_dpp(0, __builtin_bit_cast(int, x), CTRL, RM, 0xf, true));
}
template <class T> DEVI T ldnt(const void* p) { return __builtin_nontemporal_load((const T*)p); }
DEVI u4 make_srd(const void* p, unsigned bytes) {
  uint64_t a = (uint64_t)p;
  u4 r = {(unsigned)a, (unsigned)(a >> 32) & 0xffffu, bytes, 0x00020000u};
  return r;
}
DEVI s4 tr_read(unsigned addr) { return __builtin_amdgcn_ds_read_tr16_b64_v4i16((LAS s4*)(size_t)addr); }
DEVI bf8 cat8(s4 a, s4 b) {
  typedef __attribute__((ext_vector_type(8))) short s8;
  s8 v = __builtin_shufflevector(a, b, 0, 1, 2, 3, 4, 5, 6, 7);
  return __builtin_bit_cast(bf8, v);
}
DEVI f4 mfma16(bf8 a, bf8 b, f4 c) { return __builtin_amdgcn_mfma_f32_16x16x32_bf16(a, b, c, 0, 0, 0); }
template <int N> DEVI void waitv() { asm volatile("s_waitcnt vmcnt(%0)" ::"i"(N) : "memory"); }
template <class T> DEVI T lds_ld(unsigned a) { return *(const LAS T*)(size_t)a; }
template <class T> DEVI void lds_st(unsigned a, T v) { *(LAS T*)(size_t)a = v; }
DEVI void raw_barrier() { asm volatile("s_waitcnt lgkmcnt(0)\n\ts_barrier" ::: "memory"); }
DEVI unsigned ctr_arrive(unsigned a, int lane) {
  asm volatile("s_waitcnt lgkmcnt(0)" ::: "memory");
  unsigned old = 0;
  if (lane == 0) old = __hip_atomic_fetch_add((LAS unsigned*)(size_t)a, 1u, __ATOMIC_RELAXED, __HIP_MEMORY_SCOPE_WORKGROUP);
  old = __builtin_amdgcn_readfirstlane(old);
  asm volatile("" ::: "memory");
  return old;
}
DEVI void ctr_wait(unsigned a, unsigned n) {
  while (__builtin_amdgcn_readfirstlane(__hip_atomic_load((LAS unsigned*)(size_t)a, __ATOMIC_RELAXED, __HIP_MEMORY_SCOPE_WORKGROUP)) < n)
    __builtin_amdgcn_s_sleep(1);
  asm volatile("" ::: "memory");
}
typedef __amdgpu_buffer_rsrc_t rsrc_t;
DEVI u4 ld16_sc1(rsrc_t r, unsigned off) { return __builtin_amdgcn_raw_buffer_load_b128(r, off, 0, 16); }
DEVI u2 ld8_sc1(rsrc_t r, unsigned off) { return __builtin_amdgcn_raw_buffer_load_b64(r, off, 0, 16); }
DEVI void st16_sc1(rsrc_t r, unsigned off, u4 v) { __builtin_amdgcn_raw_buffer_store_b128(v, r, off, 0, 16); }
DEVI void st8_sc1(rsrc_t r, unsigned off, u2 v) { __builtin_amdgcn_raw_buffer_store_b64(v, r, off, 0, 16); }

DEVI f4 sel4(bool c, f4 a, f4 b) {
  asm("" : "+v"(a)); asm("" : "+v"(b));
  return c ? a : b;
}
DEVI float gelu_t(float x) {
  const float t = x * __builtin_fmaf(x * x, -0.10294324f, -2.3022082f);
  return x * __builtin_amdgcn_rcpf(1.f + __builtin_amdgcn_exp2f(t));
}

DEVI void ln_row(const unsigned (&xr)[5], unsigned (&hp)[5], float& m1, float& inv1) {
  float e[10], s = 0.f, q = 0.f;
  const float piv = blo(__builtin_amdgcn_readfirstlane(xr[0]));
#pragma unroll
  for (int j = 0; j < 5; ++j) { e[2 * j] = blo(xr[j]); e[2 * j + 1] = bhi(xr[j]); }
#pragma unroll
  for (int j = 0; j < 10; ++j) { const float d = e[j] - piv; s += d; q = __builtin_fmaf(d, d, q); }
  s += dppf<0xB1, 0xf>(s);  q += dppf<0xB1, 0xf>(q);
  s += dppf<0x4E, 0xf>(s);  q += dppf<0x4E, 0xf>(q);
  s += dppf<0x141, 0xf>(s); q += dppf<0x141, 0xf>(q);
  s += dppf<0x140, 0xf>(s); q += dppf<0x140, 0xf>(q);
  s += dppf<0x142, 0xa>(s); q += dppf<0x142, 0xa>(q);
  s += dppf<0x143, 0xc>(s); q += dppf<0x143, 0xc>(q);
  const float md = __builtin_bit_cast(float, __builtin_amdgcn_readlane(__builtin_bit_cast(int, s), 63)) * (1.f / 640.f);
  const float qq = __builtin_bit_cast(float, __builtin_amdgcn_readlane(__builtin_bit_cast(int, q), 63)) * (1.f / 640.f);
  const float var = __builtin_fmaxf(qq - md * md, 0.f), mean = piv + md;
  m1 = rbf(mean);
  inv1 = __builtin_amdgcn_rcpf(rbf(__builtin_amdgcn_sqrtf(rbf(rbf(var) + 1e-5f))));
  const float m2 = rbf((mean - m1) * inv1);
  const float inv2 = __builtin_amdgcn_rcpf(rbf(__builtin_amdgcn_sqrtf(rbf(rbf(var * inv1 * inv1) + 1e-5f))));
  const float A = inv1 * inv2, B = -m2 * inv2;
#pragma unroll
  for (int j = 0; j < 5; ++j) hp[j] = pk(__builtin_fmaf(e[2 * j] - m1, A, B), __builtin_fmaf(e[2 * j + 1] - m1, A, B));
}

struct Filt { u4 c[2]; u2 n[2]; };
DEVI void filt_load(Filt& f, const char* krow, int half, int lane) {
#pragma unroll
  for (int j = 0; j < 2; ++j) {
    const int z = lane + 64 * j + 128 * half;
    f.c[j] = ldnt<u4>(krow + z * 16);
    f.n[j] = ldnt<u2>(krow + (z < 255 ? z * 16 + 16 : 0));
  }
}
DEVI void filt_build(Filt& f, unsigned cv, int half, int lane) {
  const u4 zero = {0u, 0u, 0u, 0u};
  if (half == 1) {
    if (lane == 63) f.n[1] = u2{0u, 0u};
    if (lane < 30) lds_st<u4>(cv + CV_X + 4096 + lane * 16, zero);
  } else {
    if (lane < 4) lds_st<u4>(cv + lane * CV_CP, zero);
    if (lane == 0) {
      const unsigned d4 = f.c[0].x, d5 = f.c[0].y;
      lds_st<u4>(cv + 16, u4{0u, 0u, 0u, d4 << 16});
      lds_st<u4>(cv + CV_CP + 16, u4{0u, 0u, 0u, d4});
      lds_st<u4>(cv + 2 * CV_CP + 16, u4{0u, 0u, d4 << 16, __builtin_amdgcn_alignbit(d5, d4, 16)});
      lds_st<u4>(cv + 3 * CV_CP + 16, u4{0u, 0u, d4, d5});
    }
  }
#pragma unroll
  for (int j = 0; j < 2; ++j) {
    const u4 c = f.c[j]; const u2 n = f.n[j];
    const unsigned a0 = __builtin_amdgcn_alignbit(c.y, c.x, 16), a1 = __builtin_amdgcn_alignbit(c.z, c.y, 16), a2 = __builtin_amdgcn_alignbit(c.w, c.z, 16),
                   a3 = __builtin_amdgcn_alignbit(n.x, c.w, 16), a4 = __builtin_amdgcn_alignbit(n.y, n.x, 16);
    const unsigned p = cv + (lane + 64 * j + 128 * half + 2) * 16;
    lds_st<u4>(p, u4{a0, a1, a2, a3});
    lds_st<u4>(p + CV_CP, u4{c.y, c.z, c.w, n.x});
    lds_st<u4>(p + 2 * CV_CP, u4{a1, a2, a3, a4});
    lds_st<u4>(p + 3 * CV_CP, u4{c.z, c.w, n.x, n.y});
  }
}

DEVI void conv_phases(f4 (&acc)[8], unsigned ab, unsigned bb) {
  bf8 A0[8], B0[8], A1[8], B1[8];
#pragma unroll
  for (int n = 0; n < 8; ++n) acc[n] = f4{0.f, 0.f, 0.f, 0.f};
#define CV_LOAD(A, B, q)                                                                              \
  _Pragma("unroll") for (int p = 0; p < 8; ++p) {                                                      \
    B[p] = *(const volatile LAS bf8*)(size_t)(bb + 3584 - 512 * p + 64 * (q));                          \
    s4 lo = *(const volatile LAS s4*)(size_t)(ab + 512 * p + 64 * (q));                                 \
    s4 hi = *(const volatile LAS s4*)(size_t)(ab + 512 * p + 64 * (q) + 8);                             \
    A[p] = cat8(lo, hi);                                                                               \
  }
#define CV_MMA(A, B)                                                                                   \
  _Pragma("unroll") for (int p = 0; p < 8; ++p)                                                        \
  _Pragma("unroll") for (int m = 0; m + p < 8; ++m) acc[p + m] = mfma16(A[p], B[m], acc[p + m]);
#define CV_SCHED()                                                                                     \
  _Pragma("unroll") for (int z = 0; z < 24; ++z) {                                                     \
    __builtin_amdgcn_sched_group_barrier(0x8, 1, 0);                                                   \
    __builtin_amdgcn_sched_group_barrier(0x100, 1, 0);                                                 \
  }                                                                                                    \
  __builtin_amdgcn_sched_group_barrier(0x8, 12, 0);                                                    \
  SCHED_FENCE();
  CV_LOAD(A0, B0, 0);
  SCHED_FENCE();
#pragma unroll 1
  for (int q = 0; q < CONVQ; q += 2) {
    CV_LOAD(A1, B1, q + 1); CV_MMA(A0, B0); CV_SCHED();
    CV_LOAD(A0, B0, (q + 2) & 3); CV_MMA(A1, B1); CV_SCHED();
  }
}

constexpr int g1_h(int i) { return i <= 12 ? 4 : 0; }
constexpr int g1_x(int i) { return ((i > R1 && i <= 9) || i == 13 || i == 14) ? 4 : 0; }
constexpr int g1_n(int i) { return i == 0 ? 8 : i == 1 ? 4 + g1_h(0) + g1_x(0) : (i >= 3 ? g1_x(i - 3) : 0) + g1_h(i - 2) + g1_x(i - 2) + g1_h(i - 1) + g1_x(i - 1); }
constexpr int g2_u(int j) { return j <= 8 ? 5 : 0; }
constexpr int g2_x(int j) { return (j > R2 && j <= 5) ? 4 : 0; }
constexpr int g2_n(int j) { return j == 0 ? 10 : j == 1 ? 5 + g2_u(0) + g2_x(0) : (j >= 3 ? g2_x(j - 3) : 0) + g2_u(j - 2) + g2_x(j - 2) + g2_u(j - 1) + g2_x(j - 1); }

template <int M0, int NM>
DEVI void final_tiles(f4 (&own)[NM], const u2 (&xr)[NM], u2 stt, unsigned b2r, unsigned yab, char* op) {
  const float bb = blo(b2r), m1 = __uint_as_float(stt.x), inv1 = __uint_as_float(stt.y);
#pragma unroll
  for (int m = 0; m < NM; ++m) {
    const u2 ya = __builtin_bit_cast(u2, tr_read(yab + (M0 + m) * 2048));
    const float x0 = blo(xr[m].x), x1 = bhi(xr[m].x), x2 = blo(xr[m].y), x3 = bhi(xr[m].y);
    const float r0 = __builtin_fmaf((x0 - m1) * inv1, blo(ya.x), x0) + (own[m][0] + bb), r1 = __builtin_fmaf((x1 - m1) * inv1, bhi(ya.x), x1) + (own[m][1] + bb);
    const float r2 = __builtin_fmaf((x2 - m1) * inv1, blo(ya.y), x2) + (own[m][2] + bb), r3 = __builtin_fmaf((x3 - m1) * inv1, bhi(ya.y), x3) + (own[m][3] + bb);
    *(u2*)(op + (M0 + m) * 32) = u2{pk(r0, r1), pk(r2, r3)};
  }
}

__global__ __launch_bounds__(512, 2) __attribute__((amdgpu_waves_per_eu(2, 2))) void fused_kernel(
    const char* __restrict__ x, const char* __restrict__ kk, const char* __restrict__ vv, const char* __restrict__ shk,
    const char* __restrict__ ssmk, const char* __restrict__ ssmd, const char* __restrict__ w1, const char* __restrict__ b1,
    const char* __restrict__ w2, const char* __restrict__ b2, char* __restrict__ out, char* ws) {
  extern __shared__ __attribute__((aligned(16))) char smem[];
  const int lane = threadIdx.x & 63, wave = __builtin_amdgcn_readfirstlane(threadIdx.x >> 6);
  const int b = blockIdx.x, xg = b & 7, r = b >> 3;
  const unsigned sb = __builtin_amdgcn_readfirstlane((unsigned)(size_t)smem);
  const unsigned ctl = sb + L_CTL;
  asm volatile("" ::"s"(x), "s"(kk), "s"(vv), "s"(shk), "s"(ssmk), "s"(ssmd), "s"(w1), "s"(b1), "s"(w2), "s"(b2), "s"(out), "s"(ws));
  const rsrc_t rws = __builtin_amdgcn_make_buffer_rsrc(ws, 0, WS_END, 0x00020000);
  if (wave == 0) STAMP(0);
  const int dt2 = r & 7, st = xg * 4 + (r >> 3);

  const u4 srdS = make_srd(ws, WS_END);
  const int g = lane >> 4, q = (lane >> 2) & 3, p = lane & 3;
  unsigned ring = sb + wave * L_RING;
  const bool has1 = r < 30;
  const int dt = MAP1 ? (xg & 1) * 5 + r % 5 : r % 10, ft = MAP1 ? (xg >> 1) * 6 + r / 5 : xg * 3 + r / 10;
  const int rot1 = dt * 16 / 10;
  int rot2 = dt2 * 12 / 8;
  const u4 srdW1 = make_srd(w1, 6291456), srdW2 = make_srd(w2, 6291456);
  unsigned vow[4];
#define DMA1(POL, slot, t, srd, soff)                                                                                          \
  asm volatile("s_mov_b32 m0, %0\n\ts_nop 0\n\tbuffer_load_dwordx4 %1, %2, %3 offen offset:%4" POL " lds" ::"s"(ring + (slot) * 4096), \
               "v"(vow[t]), "s"(srd), "s"(soff), "i"((t) * 1024)                                                               \
               : "memory")
#define KS1(i) (4u * (((i) + rot1) & 15) + wave)
#define KS2(j) (4u * ((j) + rot2 >= 12 ? (j) + rot2 - 12 : (j) + rot2) + wave)
#define W1_PIECE(i, t) do { if ((i) < NT_FIRST) DMA1(" nt", (i) % 7, t, srdW1, KS1(i) * 98304u); else DMA1("", (i) % 7, t, srdW1, KS1(i) * 98304u); } while (0)
#define W2_PIECE(j, t) do { if ((j) < NT_FIRST) DMA1(" nt", ((j) + 2) % 7, t, srdW2, KS2(j) * 131072u); else DMA1("", ((j) + 2) % 7, t, srdW2, KS2(j) * 131072u); } while (0)
#define W1_DMA(i) do { W1_PIECE(i, 0); W1_PIECE(i, 1); W1_PIECE(i, 2); W1_PIECE(i, 3); } while (0)
#define W2_DMA(j) do { W2_PIECE(j, 0); W2_PIECE(j, 1); W2_PIECE(j, 2); W2_PIECE(j, 3); } while (0)
#define W1_THR(i) do { W1_PIECE(i, 0); waitv<THR>(); W1_PIECE(i, 1); waitv<THR>(); W1_PIECE(i, 2); waitv<THR>(); W1_PIECE(i, 3); waitv<THR>(); } while (0)
#define W2_THR(j) do { W2_PIECE(j, 0); waitv<THR>(); W2_PIECE(j, 1); waitv<THR>(); W2_PIECE(j, 2); waitv<THR>(); W2_PIECE(j, 3); waitv<THR>(); } while (0)
#define LD_SC1(dst, voff, soff, imm) \
  asm volatile("buffer_load_dwordx4 %0, %1, %2, %3 offen offset:%4" HPOL : "=&v"(dst) : "v"(voff), "s"(srdS), "s"(soff), "i"(imm) : "memory")
#pragma unroll
  for (int t = 0; t < 4; ++t) vow[t] = (8 * t + (lane >> 3)) * 3072 + ft * 128 + (((lane & 7) ^ (2 * ((lane >> 4) & 1) + 4 * (t & 1))) * 16) - 1024 * t;
  u2 tag;
  {
    unsigned xr[5];
    asm volatile("buffer_load_dword %0, %5, %6, %7 offen nt\n\tbuffer_load_dword %1, %5, %6, %7 offen offset:256 nt\n\t"
                 "buffer_load_dword %2, %5, %6, %7 offen offset:512 nt\n\tbuffer_load_dword %3, %5, %6, %7 offen offset:768 nt\n\t"
                 "buffer_load_dword %4, %5, %6, %7 offen offset:1024 nt"
                 : "=&v"(xr[0]), "=&v"(xr[1]), "=&v"(xr[2]), "=&v"(xr[3]), "=&v"(xr[4])
                 : "v"(lane * 4), "s"(make_srd(x, 2621440)), "s"((b * 8 + wave) * 1280)
                 : "memory");
    {
      const u4 srdK = make_srd(kk, 2621440), srdV = make_srd(vv, 2621440);
#pragma unroll
      for (int i = 0; i < 3; ++i) {
        const int pc = wave + 8 * i;
        const bool isv = pc >= 11;
        const int n = (isv ? pc - 11 : pc) * 64 + lane, g8 = (n * 6473) >> 19, rem = n - g8 * 81, rr = (rem * 205) >> 11;
        const int row = g8 * 8 + rr, c = rem - rr * 10;
        const int grow = st * 64 + row - (isv ? 0 : 3);
        const bool ok = rem < 80 && grow >= 0 && row < (isv ? 64 : 67);
        const unsigned vo = ok ? grow * 1280 + dt2 * 160 + c * 16 : 0x7fffff00u;
        const unsigned dst = sb + L_KBUF + pc * 1024;
        if (isv) asm volatile("s_mov_b32 m0, %0\n\ts_nop 0\n\tbuffer_load_dwordx4 %1, %2, 0 offen nt lds" ::"s"(dst), "v"(vo), "s"(srdV) : "memory");
        else     asm volatile("s_mov_b32 m0, %0\n\ts_nop 0\n\tbuffer_load_dwordx4 %1, %2, 0 offen nt lds" ::"s"(dst), "v"(vo), "s"(srdK) : "memory");
      }
    }
    u2 ep; u4 shw;
    asm volatile("buffer_load_dwordx2 %0, off, %2, 0 sc1\n\tbuffer_load_dwordx4 %1, %3, %4, 0 offen"
                 : "=&v"(ep), "=&v"(shw)
                 : "s"(srdS), "v"((dt2 * 80 + (wave >= 3 ? wave - 3 : 0) * 16 + (lane >> 3) * 2) * 8), "s"(make_srd(shk, 5120))
                 : "memory");
    static_assert(WS_EPOCH == 0, "epoch load above");
    if (wave == 0 && lane < 16) lds_st<unsigned>(ctl + lane * 4, 0u);
    asm volatile("s_waitcnt vmcnt(5)" : "+v"(xr[0]), "+v"(xr[1]), "+v"(xr[2]), "+v"(xr[3]), "+v"(xr[4])::"memory");
    STAMP(1);
    unsigned hp[5]; float m1, inv1;
    ln_row(xr, hp, m1, inv1);
#pragma unroll
    for (int j = 0; j < 5; ++j) lds_st<unsigned>(sb + L_HBUF + wave * 1344 + (lane + 64 * j) * 4, hp[j]);
    if (lane == 0) lds_st<u2>(ctl + 64 + wave * 8, u2{__float_as_uint(m1), __float_as_uint(inv1)});
    STAMP(2);
    asm volatile("s_waitcnt vmcnt(0)" : "+v"(ep), "+v"(shw)::"memory");
    const uint64_t tg = (((uint64_t)ep.y << 32) | ep.x) * 6364136223846793005ull + 1442695040888963407ull;
    tag = u2{(unsigned)__builtin_amdgcn_readfirstlane((unsigned)tg), (unsigned)__builtin_amdgcn_readfirstlane((unsigned)(tg >> 32))};
    STAMP(3);
    raw_barrier();
    if (wave < 3) {
      const unsigned tb = sb + L_HBUF + ((lane >> 2) & 3) * 1344 + (lane >> 4) * 32 + (lane & 3) * 8;
      u4 v[4];
#pragma unroll
      for (int i = 0; i < 4; ++i) { const int it = i < 3 ? wave + 3 * i : 9; v[i] = __builtin_bit_cast(u4, cat8(tr_read(tb + it * 128), tr_read(tb + it * 128 + 4 * 1344))); }
      const u4 sv = lds_ld<u4>(ctl + 64 + (lane & 3) * 16);
#pragma unroll
      for (int i = 0; i < 4; ++i)
        if (i < 3 || wave == 0) st16_sc1(rws, WS_H + (b * 640 + (i < 3 ? wave + 3 * i : 9) * 64 + lane) * 16, v[i]);
      if (wave == 1 && lane < 4) st16_sc1(rws, WS_ST + b * 64 + lane * 16, sv);
    } else {
      const int rg = lane & 7, cp = (wave - 3) * 8 + (lane >> 3);
      const unsigned ka = sb + L_KBUF + rg * 1296 + cp * 4;
      unsigned kr[11], vr[8];
#pragma unroll
      for (int i = 0; i < 11; ++i) kr[i] = lds_ld<unsigned>(ka + i * 160 + (i >> 3) * 16);
#pragma unroll
      for (int i = 0; i < 8; ++i) vr[i] = lds_ld<unsigned>(ka + 11264 + i * 160);
      SCHED_FENCE();
#pragma unroll
      for (int c = 0; c < 2; ++c) {
        const unsigned wa = c ? shw.z : shw.x, wb = c ? shw.w : shw.y;
        const float w0 = blo(wa), w1f = bhi(wa), w2f = blo(wb), w3 = bhi(wb);
        float kf[11], o[8];
#pragma unroll
        for (int i = 0; i < 11; ++i) kf[i] = c ? bhi(kr[i]) : blo(kr[i]);
#pragma unroll
        for (int i = 0; i < 8; ++i) o[i] = (kf[i] * w3 + kf[i + 1] * w2f + kf[i + 2] * w1f + kf[i + 3] * w0) * (c ? bhi(vr[i]) : blo(vr[i]));
        st16_sc1(rws, WS_KV + (dt2 * 80 + cp * 2 + c) * 4096 + (2040 - 64 * st - 8 * rg) * 2, u4{pk(o[7], o[6]), pk(o[5], o[4]), pk(o[3], o[2]), pk(o[1], o[0])});
      }
    }
    STAMP(4);
  }

  if (wave >= 4) {
    const int hw = wave - 4, pair = hw >> 1, half = hw & 1;
    const bool two = pair == 0 && b >= 128;
    const int ch0 = 2 * b + pair, ch1 = two ? 384 + b : ch0;
    Filt f0, f1;
    filt_load(f0, ssmk + (size_t)ch0 * 4096, half, lane);
    filt_load(f1, ssmk + (size_t)ch1 * 4096, half, lane);
    waitv<8>();
    if (ctr_arrive(ctl + C_H * 4, lane) == 7 && lane < 8) st8_sc1(rws, WS_F1 + lane * 4096 + b * 16, tag);
    STAMP(20 + hw);
    unsigned dd0 = *(const unsigned short*)(ssmd + ch0 * 2), dd1 = *(const unsigned short*)(ssmd + ch1 * 2);
    __builtin_amdgcn_s_setprio(HPRIO);
    ctr_wait(ctl + C_H * 4, 8);
    const unsigned cv = sb + L_CONV + pair * CV_BYTES;
    filt_build(f0, cv, half, lane);
    asm volatile("" : "+v"(f1.c[0]), "+v"(f1.c[1]), "+v"(f1.n[0]), "+v"(f1.n[1]), "+v"(dd0), "+v"(dd1));
    ctr_wait(ctl + C_GO1 * 4, 1);
    u4 xv0[2], xv1[2];
#pragma unroll
    for (int j = 0; j < 2; ++j) {
      xv0[j] = ld16_sc1(rws, WS_KV + ch0 * 4096 + (lane + 64 * j + 128 * half) * 16);
      xv1[j] = ld16_sc1(rws, WS_KV + ch1 * 4096 + (lane + 64 * j + 128 * half) * 16);
    }
    if (b == 0 && threadIdx.x == 448) st8_sc1(rws, WS_EPOCH, tag);
    const int c16 = lane & 15, kg = lane >> 4;
    const unsigned pc = ctl + (C_P0 + pair) * 4;
    const unsigned ab = cv + (c16 & 3) * CV_CP + (c16 >> 2) * 8 + kg * 16 + half * 256;
    const unsigned bb = cv + CV_X + 480 - 32 * c16 + 16 * kg + half * 256;
#pragma unroll 1
    for (int round = 0; round < (two ? 2 : 1); ++round) {
      if (round) {
        if (has1) ctr_wait(ctl + C_G3 * 4, 4);
        filt_build(f1, cv, half, lane);
        xv0[0] = xv1[0]; xv0[1] = xv1[1]; dd0 = dd1;
      }
#pragma unroll
      for (int j = 0; j < 2; ++j) lds_st<u4>(cv + CV_X + (lane + 64 * j + 128 * half) * 16, xv0[j]);
      ctr_arrive(pc, lane);
      ctr_wait(pc, round * 8 + 2);
      f4 acc[8];
      conv_phases(acc, ab, bb);
      if (round == 0) STAMP(25 + hw);
      ctr_arrive(pc, lane);
      ctr_wait(pc, round * 8 + 4);
#pragma unroll
      for (int n = 0; n < 4; ++n) lds_st<f4>(cv + half * 4096 + n * 1024 + lane * 16, sel4(half, acc[n], acc[n + 4]));
      ctr_arrive(pc, lane);
      ctr_wait(pc, round * 8 + 6);
      const float dd = blo(dd0);
      u2 y[4];
#pragma unroll
      for (int n = 0; n < 4; ++n) {
        const f4 t = sel4(half, acc[n + 4], acc[n]) + lds_ld<f4>(cv + (half ^ 1) * 4096 + n * 1024 + lane * 16);
        const int t0 = 1024 * half + 256 * n + 16 * c16 + 4 * kg;
        const u2 xq = lds_ld<u2>(cv + CV_X + (2044 - t0) * 2);
        y[n] = u2{pk(__builtin_fmaf(bhi(xq.y), dd, t[0]), __builtin_fmaf(blo(xq.y), dd, t[1])),
                  pk(__builtin_fmaf(bhi(xq.x), dd, t[2]), __builtin_fmaf(blo(xq.x), dd, t[3]))};
      }
      ctr_arrive(pc, lane);
#pragma unroll
      for (int n = 0; n < 4; n += 2) {
        const auto sx = __builtin_amdgcn_permlane16_swap(y[n].x, y[n + 1].x, false, false);
        const auto sy = __builtin_amdgcn_permlane16_swap(y[n].y, y[n + 1].y, false, false);
        st16_sc1(rws, WS_YA + (round ? ch1 : ch0) * 4096 + (1024 * half + 256 * (n + (kg & 1)) + 16 * c16 + 8 * (kg >> 1)) * 2, u4{sx[0], sy[0], sx[1], sy[1]});
      }
      ctr_wait(pc, round * 8 + 8);
    }
    waitv<0>();
    if (ctr_arrive(ctl + (C_Q0 + pair) * 4, lane) == 1 && lane < (two ? 2 : 1)) st8_sc1(rws, WS_F3 + (lane ? ch1 : ch0) * 8, tag);
    STAMP(32 + hw);
    const int s = st * 64 + hw * 16 + c16;
    const size_t xo = (size_t)s * 1280 + (dt2 * 80 + 4 * kg) * 2;
    u2 xq[2] = {*(const u2*)(x + xo + 96), *(const u2*)(x + xo + 128)};
    u2 stt = ld8_sc1(rws, WS_ST + s * 8);
    unsigned b2r = *(const unsigned short*)(b2 + s * 2);
    asm volatile("" : "+v"(xq[0]), "+v"(xq[1]), "+v"(stt), "+v"(b2r));
    if (hw == 3) {
#pragma unroll 1
      for (unsigned n = 0; n < SPIN_CAP; ++n) {
        const u4 a = ld16_sc1(rws, WS_F3 + dt2 * 640 + (lane < 40 ? lane : 0) * 16);
        if (__all(a.x == tag.x && a.y == tag.y && a.z == tag.x && a.w == tag.y)) break;
        __builtin_amdgcn_s_sleep(2);
        asm volatile("" ::: "memory");
      }
      asm volatile("" ::: "memory");
      STAMP(37);
      u4 y[10];
#pragma unroll
      for (int i = 0; i < 10; ++i) y[i] = ld16_sc1(rws, WS_YA + (dt2 * 80 + i * 8 + (lane >> 3)) * 4096 + st * 128 + (lane & 7) * 16);
      SCHED_FENCE();
#pragma unroll
      for (int i = 0; i < 10; ++i) lds_st<u4>(sb + L_YA + i * 1024 + lane * 16, y[i]);
      STAMP(38);
    }
    raw_barrier();
    f4 own[2];
#pragma unroll
    for (int m = 0; m < 2; ++m) {
      const unsigned a = sb + (3 + m) * 1024 + lane * 16;
      own[m] = (lds_ld<f4>(a + hw * L_RING) + lds_ld<f4>(a + ((hw - 1) & 3) * L_RING + 5120)) +
               (lds_ld<f4>(a + ((hw - 2) & 3) * L_RING + 10240) + lds_ld<f4>(a + ((hw - 3) & 3) * L_RING + 15360));
    }
    final_tiles<3, 2>(own, xq, stt, b2r, sb + L_YA + (4 * kg + ((lane >> 2) & 3)) * 128 + hw * 32 + (lane & 3) * 8, out + xo);
    return;
  }

  waitv<0>();
  if (ctr_arrive(ctl + C_H * 4, lane) == 7 && lane < 8) st8_sc1(rws, WS_F1 + lane * 4096 + b * 16, tag);
  STAMP(16 + wave);
  __builtin_amdgcn_s_setprio(GPRIO);
#define POLL_F1()                                                                                                            \
  do {                                                                                                                       \
    if (wave == 0) {                                                                                                         \
      _Pragma("unroll 1") for (unsigned n = 0; n < SPIN_CAP; ++n) {                                                          \
        bool ok = true;                                                                                                      \
        _Pragma("unroll") for (int i = 0; i < 4; ++i) {                                                                      \
          const u2 a = ld8_sc1(rws, WS_F1 + xg * 4096 + i * 1024 + lane * 16);                                               \
          ok = ok && a.x == tag.x && a.y == tag.y;                                                                           \
        }                                                                                                                    \
        if (__all(ok)) break;                                                                                                \
        asm volatile("" ::: "memory");                                                                                       \
      }                                                                                                                      \
      asm volatile("" ::: "memory");                                                                                         \
      if (lane == 0) lds_st<unsigned>(ctl + C_GO1 * 4, 1u);                                                                  \
    } else {                                                                                                                 \
      ctr_wait(ctl + C_GO1 * 4, 1);                                                                                          \
    }                                                                                                                        \
  } while (0)
  unsigned tra[4];
  s4 wf[2][8];

  if (has1) {
    {
      const int fsw = 2 * (q >> 1) + 4 * (g & 1);
#pragma unroll
      for (int m = 0; m < 4; ++m) tra[m] = ring + (8 * g + q) * 128 + (((2 * m) ^ fsw) + (p >> 1)) * 16 + (p & 1) * 8;
    }
    u2 bias[4];
#pragma unroll
    for (int m = 0; m < 4; ++m) bias[m] = *(const u2*)(b1 + (ft * 64 + 16 * m + 4 * g) * 2);
    unsigned voh[4];
#pragma unroll
    for (int n = 0; n < 4; ++n) voh[n] = WS_H + (g * 640 + dt * 64 + ((n + wave) & 3) * 16 + (lane & 15)) * 16;
    u4 hq[4][4];
    f4 acc[4][4];
#pragma unroll
    for (int m = 0; m < 4; ++m)
#pragma unroll
      for (int n = 0; n < 4; ++n) acc[m][n] = f4{0.f, 0.f, 0.f, 0.f};
#define H_PIECE(i, n) LD_SC1(hq[(i) & 3][n], voh[n], KS1(i) * 40960u, 0)
#define H_LOAD(i) do { H_PIECE(i, 0); H_PIECE(i, 1); H_PIECE(i, 2); H_PIECE(i, 3); } while (0)
#define H_WAIT(N, hs) asm volatile("s_waitcnt vmcnt(%4)" : "+v"(hs[0]), "+v"(hs[1]), "+v"(hs[2]), "+v"(hs[3]) : "i"(N) : "memory")
#define G1_RD(i, k) wf[(i) & 1][k] = tr_read(tra[(k) >> 1] + ((i) % 7) * 4096 + ((k) & 1) * 512)
    s4 res[R1][8];
    ctr_wait(ctl + C_H * 4, 8);
    W1_DMA(0); W1_DMA(1); W1_DMA(2); W1_DMA(3); W1_DMA(4); W1_DMA(5); W1_DMA(6);
#pragma unroll
    for (int i = 0; i < R1; ++i) {
      waitv<24>();
#pragma unroll
      for (int k = 0; k < 8; ++k) res[i][k] = tr_read(tra[k >> 1] + i * 4096 + (k & 1) * 512);
      asm volatile("s_waitcnt lgkmcnt(0)" ::: "memory");
      W1_DMA(7 + i);
    }
    asm volatile("" : "+v"(bias[0]), "+v"(bias[1]), "+v"(bias[2]), "+v"(bias[3]));
    STAMP(5 + wave);
    POLL_F1();
    STAMP(9 + wave);
    H_LOAD(0); H_LOAD(1); H_LOAD(2);
#define G1_A(i, k) ((i) < R1 ? cat8(res[(i) < R1 ? (i) : 0][(k) & 6], res[(i) < R1 ? (i) : 0][(k) | 1]) : cat8(wf[(i) & 1][(k) & 6], wf[(i) & 1][(k) | 1]))
#define G1_GROUP(i, k)                                                                                                       \
  do {                                                                                                                       \
    acc[(k) >> 1][((k) & 1) * 2] = mfma16(G1_A(i, k), __builtin_bit_cast(bf8, hq[(i) & 3][((k) & 1) * 2]), acc[(k) >> 1][((k) & 1) * 2]); \
    acc[(k) >> 1][((k) & 1) * 2 + 1] = mfma16(G1_A(i, k), __builtin_bit_cast(bf8, hq[(i) & 3][((k) & 1) * 2 + 1]), acc[(k) >> 1][((k) & 1) * 2 + 1]); \
    SCHED_FENCE();                                                                                                           \
    if ((i) + 1 < 16 && (i) + 1 >= R1) G1_RD((i) + 1, k);                                                                    \
    if ((k) < 4) { if (g1_h(i)) H_PIECE((i) + 3, (k) & 3); }                                                                 \
    else if (g1_x(i) && (i) <= 9) W1_PIECE((i) + 6, (k) & 3);                                                                \
    else if (g1_x(i)) W2_PIECE((i) - 13, (k) & 3);                                                                           \
    SCHED_FENCE();                                                                                                           \
  } while (0)
#define G1_STEP(i)                                                                                                           \
  do {                                                                                                                       \
    H_WAIT(g1_n(i), hq[(i) & 3]);                                                                                            \
    G1_GROUP(i, 0); G1_GROUP(i, 1); G1_GROUP(i, 2); G1_GROUP(i, 3); G1_GROUP(i, 4); G1_GROUP(i, 5); G1_GROUP(i, 6); G1_GROUP(i, 7); \
  } while (0)
    G1_STEP(0); G1_STEP(1); G1_STEP(2); G1_STEP(3); G1_STEP(4); G1_STEP(5); G1_STEP(6); G1_STEP(7); G1_STEP(8); G1_STEP(9);
#pragma unroll
    for (int t = 0; t < 4; ++t) vow[t] = (8 * t + (lane >> 3)) * 4096 + st * 128 + (((lane & 7) ^ ((lane >> 3) & 6)) * 16) - 1024 * t;
    G1_STEP(10); G1_STEP(11); G1_STEP(12); G1_STEP(13); G1_STEP(14); G1_STEP(15);
    STAMP(40 + wave);
#pragma unroll
    for (int n = 1; n < 4; ++n)
#pragma unroll
      for (int m = 0; m < 4; ++m) lds_st<f4>(ring + (n == 1 ? 6 : n - 2) * 4096 + m * 1024 + lane * 16, acc[m][n]);
    ctr_arrive(ctl + C_G1 * 4, lane);
    STAMP(14);
    ctr_wait(ctl + C_G1 * 4, 4);
    STAMP(29);
#pragma unroll
    for (int n = 1; n < 4; ++n) {
      const unsigned a = sb + ((wave - n) & 3) * L_RING + (n == 1 ? 6 : n - 2) * 4096 + lane * 16;
#pragma unroll
      for (int m = 0; m < 4; ++m) acc[m][n] = lds_ld<f4>(a + m * 1024);
    }
    SCHED_FENCE();
#pragma unroll
    for (int m = 0; m < 4; ++m) acc[m][0] = (acc[m][0] + acc[m][1]) + (acc[m][2] + acc[m][3]);
    unsigned o[8];
#pragma unroll
    for (int m = 0; m < 4; ++m) {
      o[2 * m] = pk(gelu_t(acc[m][0][0] + blo(bias[m].x)), gelu_t(acc[m][0][1] + bhi(bias[m].x)));
      o[2 * m + 1] = pk(gelu_t(acc[m][0][2] + blo(bias[m].y)), gelu_t(acc[m][0][3] + bhi(bias[m].y)));
    }
#pragma unroll
    for (int P = 0; P < 2; ++P)
      st16_sc1(rws, WS_U + ((ft * 2 + P) * 40 + dt * 4 + wave) * 1024 + lane * 16, u4{o[4 * P], o[4 * P + 1], o[4 * P + 2], o[4 * P + 3]});
    STAMP(13);
    W2_DMA(2);
    waitv<4>();
    if (ctr_arrive(ctl + C_G3 * 4, lane) == 3 && lane < 8) st8_sc1(rws, WS_F2 + lane * 4096 + (dt * 24 + ft) * 16, tag);
    STAMP(44 + wave);
    ctr_wait(ctl + C_G3 * 4, 4);
    W2_DMA(3); W2_DMA(4); W2_DMA(5); W2_DMA(6);
  } else {
#pragma unroll
    for (int t = 0; t < 4; ++t) vow[t] = (8 * t + (lane >> 3)) * 4096 + st * 128 + (((lane & 7) ^ ((lane >> 3) & 6)) * 16) - 1024 * t;
    POLL_F1();
    W2_DMA(0); W2_DMA(1); W2_DMA(2); W2_DMA(3); W2_DMA(4); W2_DMA(5); W2_DMA(6);
  }

  asm volatile("" : "+s"(ring), "+s"(rot2));
  {
    const int fsw = 4 * (g & 1) + (q & 2);
#pragma unroll
    for (int n = 0; n < 4; ++n) tra[n] = ring + (4 * g + q) * 128 + (((2 * ((n + wave) & 3)) ^ fsw) + (p >> 1)) * 16 + (p & 1) * 8;
  }
  const unsigned vou0 = WS_U + dt2 * 5120 + lane * 16, vou1 = vou0 + 4096;
  u4 uq[4][5];
  f4 acc[5][4];
#pragma unroll
  for (int m = 0; m < 5; ++m)
#pragma unroll
    for (int n = 0; n < 4; ++n) acc[m][n] = f4{0.f, 0.f, 0.f, 0.f};
#define U_PIECE(j, m) LD_SC1(uq[(j) & 3][m], (m) < 4 ? vou0 : vou1, KS2(j) * 40960u, ((m) & 3) * 1024)
#define U_LOAD(j) do { U_PIECE(j, 0); U_PIECE(j, 1); U_PIECE(j, 2); U_PIECE(j, 3); U_PIECE(j, 4); } while (0)
#define U_WAIT(N, us) asm volatile("s_waitcnt vmcnt(%5)" : "+v"(us[0]), "+v"(us[1]), "+v"(us[2]), "+v"(us[3]), "+v"(us[4]) : "i"(N) : "memory")
#define G2_RD(j, k) wf[(j) & 1][k] = tr_read(tra[(k) >> 1] + (((j) + 2) % 7) * 4096 + ((k) & 1) * 2048)
#define G2_B(j, n) ((j) < R2 ? cat8(res2[(j) < R2 ? (j) : 0][2 * (n)], res2[(j) < R2 ? (j) : 0][2 * (n) + 1]) : cat8(wf[(j) & 1][2 * (n)], wf[(j) & 1][2 * (n) + 1]))
#define G2_MM(j, m, n) acc[m][n] = mfma16(__builtin_bit_cast(bf8, uq[(j) & 3][m]), G2_B(j, n), acc[m][n])
#define G2_GROUP(j, k)                                                                                                       \
  do {                                                                                                                       \
    G2_MM(j, (2 * (k)) % 5, (2 * (k)) / 5); G2_MM(j, (2 * (k) + 1) % 5, (2 * (k) + 1) / 5);                                  \
    SCHED_FENCE();                                                                                                           \
    if ((k) < 8 && (j) + 1 < 12 && (j) + 1 >= R2) G2_RD((j) + 1, (k) & 7);                                                   \
    if ((k) < 5) { if (g2_u(j)) U_PIECE((j) + 3, (k) % 5); }                                                                 \
    else if ((k) < 9 && g2_x(j)) W2_PIECE((j) + 6, ((k) - 5) & 3);                                                           \
    SCHED_FENCE();                                                                                                           \
  } while (0)
#define G2_STEP(j)                                                                                                           \
  do {                                                                                                                       \
    U_WAIT(g2_n(j), uq[(j) & 3]);                                                                                                  \
    G2_GROUP(j, 0); G2_GROUP(j, 1); G2_GROUP(j, 2); G2_GROUP(j, 3); G2_GROUP(j, 4);                                          \
    G2_GROUP(j, 5); G2_GROUP(j, 6); G2_GROUP(j, 7); G2_GROUP(j, 8); G2_GROUP(j, 9);                                          \
  } while (0)
  s4 res2[R2][8];
  static_assert(R2 <= 3, "wait below");
  waitv<16>();
#pragma unroll
  for (int j = 0; j < R2; ++j) {
#pragma unroll
    for (int k = 0; k < 8; ++k) res2[j][k] = tr_read(tra[k >> 1] + ((j + 2) % 7) * 4096 + (k & 1) * 2048);
    asm volatile("s_waitcnt lgkmcnt(0)" ::: "memory");
    W2_DMA(7 + j);
  }
  const int s = st * 64 + wave * 16 + (lane & 15);
  const size_t xo = (size_t)s * 1280 + (dt2 * 80 + 4 * g) * 2;
  u2 xr[3];
#pragma unroll
  for (int m = 0; m < 3; ++m) xr[m] = *(const u2*)(x + xo + m * 32);
  u2 stt = ld8_sc1(rws, WS_ST + s * 8);
  unsigned b2r = *(const unsigned short*)(b2 + s * 2);
  asm volatile("" : "+v"(xr[0]), "+v"(xr[1]), "+v"(xr[2]), "+v"(stt), "+v"(b2r));
  waitv<0>();
  STAMP(39);
  if (wave == 0) {
    const int dta = (dt2 * 5) >> 2;
#pragma unroll 1
    for (unsigned n = 0; n < SPIN_CAP; ++n) {
      const u2 a = ld8_sc1(rws, WS_F2 + xg * 4096 + (dta * 24 + (lane < 48 ? lane : 0)) * 16);
      if (__all(a.x == tag.x && a.y == tag.y)) break;
      asm volatile("" ::: "memory");
    }
    asm volatile("" ::: "memory");
    if (lane == 0) lds_st<unsigned>(ctl + C_GO2 * 4, 1u);
  } else {
    ctr_wait(ctl + C_GO2 * 4, 1);
  }
  STAMP(48 + wave);
  U_LOAD(0); U_LOAD(1); U_LOAD(2);
  G2_STEP(0); G2_STEP(1); G2_STEP(2); G2_STEP(3); G2_STEP(4); G2_STEP(5); G2_STEP(6); G2_STEP(7); G2_STEP(8); G2_STEP(9); G2_STEP(10); G2_STEP(11);
  STAMP(52 + wave);
#pragma unroll
  for (int n = 0; n < 4; ++n)
#pragma unroll
    for (int m = (n == 0 ? 3 : 0); m < 5; ++m) lds_st<f4>(ring + (n * 5 + m) * 1024 + lane * 16, acc[m][n]);
  f4 own[3] = {acc[0][0], acc[1][0], acc[2][0]};
  raw_barrier();
  STAMP(56 + wave);
#pragma unroll
  for (int w = 1; w < 4; ++w) {
    const unsigned a = sb + ((wave - w) & 3) * L_RING + w * 5120 + lane * 16;
#pragma unroll
    for (int m = 0; m < 3; ++m) own[m] += lds_ld<f4>(a + m * 1024);
  }
  final_tiles<0, 3>(own, xr, stt, b2r, sb + L_YA + (4 * g + q) * 128 + wave * 32 + p * 8, out + xo);
  STAMP(60 + wave);
}

extern "C" void kernel_launch(void* const* d_in, const int* in_sizes, int n_in, void* d_out, int out_size, void* d_ws, size_t ws_size,
                              hipStream_t stream) {
  (void)hipFuncSetAttribute((const void*)fused_kernel, hipFuncAttributeMaxDynamicSharedMemorySize, L_TOTAL);
  fused_kernel<<<256, 512, L_TOTAL, stream>>>((const char*)d_in[0], (const char*)d_in[1], (const char*)d_in[2], (const char*)d_in[3],
                                              (const char*)d_in[4], (const char*)d_in[5], (const char*)d_in[7], (const char*)d_in[8],
                                              (const char*)d_in[9], (const char*)d_in[10], (char*)d_out, (char*)d_ws);
}
```

```cpp
#define STAMPS
#include <stdio.h>
#include <hip/hip_runtime.h>
#include <hip/hip_bf16.h>
#include <stdint.h>

typedef __attribute__((ext_vector_type(8))) __bf16 bf8;
typedef __attribute__((ext_vector_type(2))) __bf16 bf2;
typedef __attribute__((ext_vector_type(4))) short s4;
typedef __attribute__((ext_vector_type(4))) float f4;
typedef __attribute__((ext_vector_type(2))) float f2;
typedef __attribute__((ext_vector_type(4))) unsigned u4;
typedef __attribute__((ext_vector_type(2))) unsigned u2;
#define LAS __attribute__((address_space(3)))
#define DEVI __device__ __forceinline__
#define SCHED_FENCE() __builtin_amdgcn_sched_barrier(0)

#define PF_W2 0
#define CONVQ 4
#define HPOL " sc1"
#define MAP1 0
#define R1 5
#define R2 3
#define THR 5
#define GPRIO 1
#define HPRIO 0
#define NT_FIRST 0

constexpr unsigned WS_EPOCH = 0;
constexpr unsigned WS_F1 = 4096;
constexpr unsigned WS_F2 = WS_F1 + 32768;
constexpr unsigned WS_F4 = WS_F2 + 32768;
constexpr unsigned WS_F3 = WS_F4 + 32768;
constexpr unsigned WS_ST = WS_F3 + 8192;
constexpr unsigned WS_H = WS_ST + 16384;
constexpr unsigned WS_KV = WS_H + 2621440;
constexpr unsigned WS_YA = WS_KV + 2621440;
constexpr unsigned WS_U = WS_YA + 2621440;
constexpr unsigned WS_END = WS_U + 1966080;
constexpr size_t WS_DBG = WS_END;

#define STAMP(idx) do { if ((threadIdx.x & 63) == 0) ((unsigned long long*)(ws + WS_DBG))[(((idx) >> 4) * 512 + blockIdx.x) * 16 + ((idx) & 15)] = __builtin_amdgcn_s_memrealtime(); } while (0)

constexpr unsigned L_RING = 28672;
constexpr unsigned CV_CP = 4160;
constexpr unsigned CV_X = 4 * CV_CP;
constexpr unsigned CV_BYTES = 21248;
constexpr unsigned L_CONV = 4 * L_RING;
constexpr unsigned L_HBUF = L_CONV;
constexpr unsigned L_KBUF = L_CONV + 10752;
constexpr unsigned L_VBUF = L_KBUF + 11264;
constexpr unsigned L_YA = L_CONV + CV_BYTES;
constexpr unsigned L_CTL = L_CONV + 2 * CV_BYTES;
constexpr unsigned L_TOTAL = L_CTL + 128;
enum { C_H = 0, C_GO1, C_G1, C_G2, C_G3, C_GO2, C_P0, C_P1, C_Q0, C_Q1, C_KV, C_K2 };
constexpr unsigned SPIN_CAP = 1u << 16;

DEVI float blo(unsigned u) { return __uint_as_float(u << 16); }
DEVI float bhi(unsigned u) { return __uint_as_float(u & 0xffff0000u); }
DEVI unsigned pk(float a, float b) { f2 v = {a, b}; return __builtin_bit_cast(unsigned, __builtin_convertvector(v, bf2)); }
DEVI float rbf(float a) { return blo(pk(a, a)); }
template <int CTRL, int RM> DEVI float dppf(float x) {
  return __builtin_bit_cast(float, __builtin_amdgcn_update_dpp(0, __builtin_bit_cast(int, x), CTRL, RM, 0xf, true));
}
template <class T> DEVI T ldnt(const void* p) { return __builtin_nontemporal_load((const T*)p); }
DEVI u4 make_srd(const void* p, unsigned bytes) {
  uint64_t a = (uint64_t)p;
  u4 r = {(unsigned)a, (unsigned)(a >> 32) & 0xffffu, bytes, 0x00020000u};
  return r;
}
DEVI s4 tr_read(unsigned addr) { return __builtin_amdgcn_ds_read_tr16_b64_v4i16((LAS s4*)(size_t)addr); }
DEVI bf8 cat8(s4 a, s4 b) {
  typedef __attribute__((ext_vector_type(8))) short s8;
  s8 v = __builtin_shufflevector(a, b, 0, 1, 2, 3, 4, 5, 6, 7);
  return __builtin_bit_cast(bf8, v);
}
DEVI f4 mfma16(bf8 a, bf8 b, f4 c) { return __builtin_amdgcn_mfma_f32_16x16x32_bf16(a, b, c, 0, 0, 0); }
template <int N> DEVI void waitv() { asm volatile("s_waitcnt vmcnt(%0)" ::"i"(N) : "memory"); }
template <class T> DEVI T lds_ld(unsigned a) { return *(const LAS T*)(size_t)a; }
template <class T> DEVI void lds_st(unsigned a, T v) { *(LAS T*)(size_t)a = v; }
DEVI void raw_barrier() { asm volatile("s_waitcnt lgkmcnt(0)\n\ts_barrier" ::: "memory"); }
DEVI unsigned ctr_arrive(unsigned a, int lane) {
  asm volatile("s_waitcnt lgkmcnt(0)" ::: "memory");
  unsigned old = 0;
  if (lane == 0) old = __hip_atomic_fetch_add((LAS unsigned*)(size_t)a, 1u, __ATOMIC_RELAXED, __HIP_MEMORY_SCOPE_WORKGROUP);
  old = __builtin_amdgcn_readfirstlane(old);
  asm volatile("" ::: "memory");
  return old;
}
DEVI void ctr_wait(unsigned a, unsigned n) {
  while (__builtin_amdgcn_readfirstlane(__hip_atomic_load((LAS unsigned*)(size_t)a, __ATOMIC_RELAXED, __HIP_MEMORY_SCOPE_WORKGROUP)) < n)
    __builtin_amdgcn_s_sleep(1);
  asm volatile("" ::: "memory");
}
typedef __amdgpu_buffer_rsrc_t rsrc_t;
DEVI u4 ld16_sc1(rsrc_t r, unsigned off) { return __builtin_amdgcn_raw_buffer_load_b128(r, off, 0, 16); }
DEVI u2 ld8_sc1(rsrc_t r, unsigned off) { return __builtin_amdgcn_raw_buffer_load_b64(r, off, 0, 16); }
DEVI void st16_sc1(rsrc_t r, unsigned off, u4 v) { __builtin_amdgcn_raw_buffer_store_b128(v, r, off, 0, 16); }
DEVI void st8_sc1(rsrc_t r, unsigned off, u2 v) { __builtin_amdgcn_raw_buffer_store_b64(v, r, off, 0, 16); }

DEVI f4 sel4(bool c, f4 a, f4 b) {
  asm("" : "+v"(a)); asm("" : "+v"(b));
  return c ? a : b;
}
DEVI float gelu_t(float x) {
  const float t = x * __builtin_fmaf(x * x, -0.10294324f, -2.3022082f);
  return x * __builtin_amdgcn_rcpf(1.f + __builtin_amdgcn_exp2f(t));
}

DEVI void ln_row(const unsigned (&xr)[5], unsigned (&hp)[5], float& m1, float& inv1) {
  float e[10], s = 0.f, q = 0.f;
  const float piv = blo(__builtin_amdgcn_readfirstlane(xr[0]));
#pragma unroll
  for (int j = 0; j < 5; ++j) { e[2 * j] = blo(xr[j]); e[2 * j + 1] = bhi(xr[j]); }
#pragma unroll
  for (int j = 0; j < 10; ++j) { const float d = e[j] - piv; s += d; q = __builtin_fmaf(d, d, q); }
  s += dppf<0xB1, 0xf>(s);  q += dppf<0xB1, 0xf>(q);
  s += dppf<0x4E, 0xf>(s);  q += dppf<0x4E, 0xf>(q);
  s += dppf<0x141, 0xf>(s); q += dppf<0x141, 0xf>(q);
  s += dppf<0x140, 0xf>(s); q += dppf<0x140, 0xf>(q);
  s += dppf<0x142, 0xa>(s); q += dppf<0x142, 0xa>(q);
  s += dppf<0x143, 0xc>(s); q += dppf<0x143, 0xc>(q);
  const float md = __builtin_bit_cast(float, __builtin_amdgcn_readlane(__builtin_bit_cast(int, s), 63)) * (1.f / 640.f);
  const float qq = __builtin_bit_cast(float, __builtin_amdgcn_readlane(__builtin_bit_cast(int, q), 63)) * (1.f / 640.f);
  const float var = __builtin_fmaxf(qq - md * md, 0.f), mean = piv + md;
  m1 = rbf(mean);
  inv1 = __builtin_amdgcn_rcpf(rbf(__builtin_amdgcn_sqrtf(rbf(rbf(var) + 1e-5f))));
  const float m2 = rbf((mean - m1) * inv1);
  const float inv2 = __builtin_amdgcn_rcpf(rbf(__builtin_amdgcn_sqrtf(rbf(rbf(var * inv1 * inv1) + 1e-5f))));
  const float A = inv1 * inv2, B = -m2 * inv2;
#pragma unroll
  for (int j = 0; j < 5; ++j) hp[j] = pk(__builtin_fmaf(e[2 * j] - m1, A, B), __builtin_fmaf(e[2 * j + 1] - m1, A, B));
}

struct Filt { u4 c[2]; u2 n[2]; };
DEVI void filt_load(Filt& f, const char* krow, int half, int lane) {
#pragma unroll
  for (int j = 0; j < 2; ++j) {
    const int z = lane + 64 * j + 128 * half;
    f.c[j] = ldnt<u4>(krow + z * 16);
    f.n[j] = ldnt<u2>(krow + (z < 255 ? z * 16 + 16 : 0));
  }
}
DEVI void filt_build(Filt& f, unsigned cv, int half, int lane) {
  const u4 zero = {0u, 0u, 0u, 0u};
  if (half == 1) {
    if (lane == 63) f.n[1] = u2{0u, 0u};
    if (lane < 30) lds_st<u4>(cv + CV_X + 4096 + lane * 16, zero);
  } else {
    if (lane < 4) lds_st<u4>(cv + lane * CV_CP, zero);
    if (lane == 0) {
      const unsigned d4 = f.c[0].x, d5 = f.c[0].y;
      lds_st<u4>(cv + 16, u4{0u, 0u, 0u, d4 << 16});
      lds_st<u4>(cv + CV_CP + 16, u4{0u, 0u, 0u, d4});
      lds_st<u4>(cv + 2 * CV_CP + 16, u4{0u, 0u, d4 << 16, __builtin_amdgcn_alignbit(d5, d4, 16)});
      lds_st<u4>(cv + 3 * CV_CP + 16, u4{0u, 0u, d4, d5});
    }
  }
#pragma unroll
  for (int j = 0; j < 2; ++j) {
    const u4 c = f.c[j]; const u2 n = f.n[j];
    const unsigned a0 = __builtin_amdgcn_alignbit(c.y, c.x, 16), a1 = __builtin_amdgcn_alignbit(c.z, c.y, 16), a2 = __builtin_amdgcn_alignbit(c.w, c.z, 16),
                   a3 = __builtin_amdgcn_alignbit(n.x, c.w, 16), a4 = __builtin_amdgcn_alignbit(n.y, n.x, 16);
    const unsigned p = cv + (lane + 64 * j + 128 * half + 2) * 16;
    lds_st<u4>(p, u4{a0, a1, a2, a3});
    lds_st<u4>(p + CV_CP, u4{c.y, c.z, c.w, n.x});
    lds_st<u4>(p + 2 * CV_CP, u4{a1, a2, a3, a4});
    lds_st<u4>(p + 3 * CV_CP, u4{c.z, c.w, n.x, n.y});
  }
}

DEVI void conv_phases(f4 (&acc)[8], unsigned ab, unsigned bb) {
  bf8 A0[8], B0[8], A1[8], B1[8];
#pragma unroll
  for (int n = 0; n < 8; ++n) acc[n] = f4{0.f, 0.f, 0.f, 0.f};
#define CV_LOAD(A, B, q)                                                                              \
  _Pragma("unroll") for (int p = 0; p < 8; ++p) {                                                      \
    B[p] = *(const volatile LAS bf8*)(size_t)(bb + 3584 - 512 * p + 64 * (q));                          \
    s4 lo = *(const volatile LAS s4*)(size_t)(ab + 512 * p + 64 * (q));                                 \
    s4 hi = *(const volatile LAS s4*)(size_t)(ab + 512 * p + 64 * (q) + 8);                             \
    A[p] = cat8(lo, hi);                                                                               \
  }
#define CV_MMA(A, B)                                                                                   \
  _Pragma("unroll") for (int p = 0; p < 8; ++p)                                                        \
  _Pragma("unroll") for (int m = 0; m + p < 8; ++m) acc[p + m] = mfma16(A[p], B[m], acc[p + m]);
#define CV_SCHED()                                                                                     \
  _Pragma("unroll") for (int z = 0; z < 24; ++z) {                                                     \
    __builtin_amdgcn_sched_group_barrier(0x8, 1, 0);                                                   \
    __builtin_amdgcn_sched_group_barrier(0x100, 1, 0);                                                 \
  }                                                                                                    \
  __builtin_amdgcn_sched_group_barrier(0x8, 12, 0);                                                    \
  SCHED_FENCE();
  CV_LOAD(A0, B0, 0);
  SCHED_FENCE();
#pragma unroll 1
  for (int q = 0; q < CONVQ; q += 2) {
    CV_LOAD(A1, B1, q + 1); CV_MMA(A0, B0); CV_SCHED();
    CV_LOAD(A0, B0, (q + 2) & 3); CV_MMA(A1, B1); CV_SCHED();
  }
}

constexpr int g1_h(int i) { return i <= 12 ? 4 : 0; }
constexpr int g1_x(int i) { return ((i > R1 && i <= 9) || i == 13 || i == 14) ? 4 : 0; }
constexpr int g1_n(int i) { return i == 0 ? 8 : i == 1 ? 4 + g1_h(0) + g1_x(0) : (i >= 3 ? g1_x(i - 3) : 0) + g1_h(i - 2) + g1_x(i - 2) + g1_h(i - 1) + g1_x(i - 1); }
constexpr int g2_u(int j) { return j <= 8 ? 5 : 0; }
constexpr int g2_x(int j) { return (j > R2 && j <= 5) ? 4 : 0; }
constexpr int g2_n(int j) { return j == 0 ? 10 : j == 1 ? 5 + g2_u(0) + g2_x(0) : (j >= 3 ? g2_x(j - 3) : 0) + g2_u(j - 2) + g2_x(j - 2) + g2_u(j - 1) + g2_x(j - 1); }

template <int M0, int NM>
DEVI void final_tiles(f4 (&own)[NM], const u2 (&xr)[NM], u2 stt, unsigned b2r, unsigned yab, char* op) {
  const float bb = blo(b2r), m1 = __uint_as_float(stt.x), inv1 = __uint_as_float(stt.y);
#pragma unroll
  for (int m = 0; m < NM; ++m) {
    const u2 ya = __builtin_bit_cast(u2, tr_read(yab + (M0 + m) * 2048));
    const float x0 = blo(xr[m].x), x1 = bhi(xr[m].x), x2 = blo(xr[m].y), x3 = bhi(xr[m].y);
    const float r0 = __builtin_fmaf((x0 - m1) * inv1, blo(ya.x), x0) + (own[m][0] + bb), r1 = __builtin_fmaf((x1 - m1) * inv1, bhi(ya.x), x1) + (own[m][1] + bb);
    const float r2 = __builtin_fmaf((x2 - m1) * inv1, blo(ya.y), x2) + (own[m][2] + bb), r3 = __builtin_fmaf((x3 - m1) * inv1, bhi(ya.y), x3) + (own[m][3] + bb);
    *(u2*)(op + (M0 + m) * 32) = u2{pk(r0, r1), pk(r2, r3)};
  }
}

__global__ __launch_bounds__(512, 2) __attribute__((amdgpu_waves_per_eu(2, 2))) void fused_kernel(
    const char* __restrict__ x, const char* __restrict__ kk, const char* __restrict__ vv, const char* __restrict__ shk,
    const char* __restrict__ ssmk, const char* __restrict__ ssmd, const char* __restrict__ w1, const char* __restrict__ b1,
    const char* __restrict__ w2, const char* __restrict__ b2, char* __restrict__ out, char* ws) {
  extern __shared__ __attribute__((aligned(16))) char smem[];
  const int lane = threadIdx.x & 63, wave = __builtin_amdgcn_readfirstlane(threadIdx.x >> 6);
  const int b = blockIdx.x, xg = b & 7, r = b >> 3;
  const unsigned sb = __builtin_amdgcn_readfirstlane((unsigned)(size_t)smem);
  const unsigned ctl = sb + L_CTL;
  asm volatile("" ::"s"(x), "s"(kk), "s"(vv), "s"(shk), "s"(ssmk), "s"(ssmd), "s"(w1), "s"(b1), "s"(w2), "s"(b2), "s"(out), "s"(ws));
  const rsrc_t rws = __builtin_amdgcn_make_buffer_rsrc(ws, 0, WS_END, 0x00020000);
  if (wave == 0) STAMP(0);
  const unsigned long long clk_r0 = __builtin_amdgcn_s_memrealtime(), clk_c0 = __builtin_amdgcn_s_memtime();
  const int dt2 = r & 7, st = xg * 4 + (r >> 3);

  const u4 srdS = make_srd(ws, WS_END);
  const int g = lane >> 4, q = (lane >> 2) & 3, p = lane & 3;
  unsigned ring = sb + wave * L_RING;
  const bool has1 = r < 30;
  const int dt = MAP1 ? (xg & 1) * 5 + r % 5 : r % 10, ft = MAP1 ? (xg >> 1) * 6 + r / 5 : xg * 3 + r / 10;
  const int rot1 = dt * 16 / 10;
  int rot2 = dt2 * 12 / 8;
  const u4 srdW1 = make_srd(w1, 6291456), srdW2 = make_srd(w2, 6291456);
  unsigned vow[4];
#define DMA1(POL, slot, t, srd, soff)                                                                                          \
  asm volatile("s_mov_b32 m0, %0\n\ts_nop 0\n\tbuffer_load_dwordx4 %1, %2, %3 offen offset:%4" POL " lds" ::"s"(ring + (slot) * 4096), \
               "v"(vow[t]), "s"(srd), "s"(soff), "i"((t) * 1024)                                                               \
               : "memory")
#define KS1(i) (4u * (((i) + rot1) & 15) + wave)
#define KS2(j) (4u * ((j) + rot2 >= 12 ? (j) + rot2 - 12 : (j) + rot2) + wave)
#define W1_PIECE(i, t) do { if ((i) < NT_FIRST) DMA1(" nt", (i) % 7, t, srdW1, KS1(i) * 98304u); else DMA1("", (i) % 7, t, srdW1, KS1(i) * 98304u); } while (0)
#define W2_PIECE(j, t) do { if ((j) < NT_FIRST) DMA1(" nt", ((j) + 2) % 7, t, srdW2, KS2(j) * 131072u); else DMA1("", ((j) + 2) % 7, t, srdW2, KS2(j) * 131072u); } while (0)
#define W1_DMA(i) do { W1_PIECE(i, 0); W1_PIECE(i, 1); W1_PIECE(i, 2); W1_PIECE(i, 3); } while (0)
#define W2_DMA(j) do { W2_PIECE(j, 0); W2_PIECE(j, 1); W2_PIECE(j, 2); W2_PIECE(j, 3); } while (0)
#define W1_THR(i) do { W1_PIECE(i, 0); waitv<THR>(); W1_PIECE(i, 1); waitv<THR>(); W1_PIECE(i, 2); waitv<THR>(); W1_PIECE(i, 3); waitv<THR>(); } while (0)
#define W2_THR(j) do { W2_PIECE(j, 0); waitv<THR>(); W2_PIECE(j, 1); waitv<THR>(); W2_PIECE(j, 2); waitv<THR>(); W2_PIECE(j, 3); waitv<THR>(); } while (0)
#define LD_SC1(dst, voff, soff, imm) \
  asm volatile("buffer_load_dwordx4 %0, %1, %2, %3 offen offset:%4" HPOL : "=&v"(dst) : "v"(voff), "s"(srdS), "s"(soff), "i"(imm) : "memory")
#pragma unroll
  for (int t = 0; t < 4; ++t) vow[t] = (8 * t + (lane >> 3)) * 3072 + ft * 128 + (((lane & 7) ^ (2 * ((lane >> 4) & 1) + 4 * (t & 1))) * 16) - 1024 * t;
  u2 tag;
  {
    unsigned xr[5];
    asm volatile("buffer_load_dword %0, %5, %6, %7 offen nt\n\tbuffer_load_dword %1, %5, %6, %7 offen offset:256 nt\n\t"
                 "buffer_load_dword %2, %5, %6, %7 offen offset:512 nt\n\tbuffer_load_dword %3, %5, %6, %7 offen offset:768 nt\n\t"
                 "buffer_load_dword %4, %5, %6, %7 offen offset:1024 nt"
                 : "=&v"(xr[0]), "=&v"(xr[1]), "=&v"(xr[2]), "=&v"(xr[3]), "=&v"(xr[4])
                 : "v"(lane * 4), "s"(make_srd(x, 2621440)), "s"((b * 8 + wave) * 1280)
                 : "memory");
    {
      const u4 srdK = make_srd(kk, 2621440), srdV = make_srd(vv, 2621440);
#pragma unroll
      for (int i = 0; i < 3; ++i) {
        const int pc = wave + 8 * i;
        const bool isv = pc >= 11;
        const int n = (isv ? pc - 11 : pc) * 64 + lane, g8 = (n * 6473) >> 19, rem = n - g8 * 81, rr = (rem * 205) >> 11;
        const int row = g8 * 8 + rr, c = rem - rr * 10;
        const int grow = st * 64 + row - (isv ? 0 : 3);
        const bool ok = rem < 80 && grow >= 0 && row < (isv ? 64 : 67);
        const unsigned vo = ok ? grow * 1280 + dt2 * 160 + c * 16 : 0x7fffff00u;
        const unsigned dst = sb + L_KBUF + pc * 1024;
        if (isv) asm volatile("s_mov_b32 m0, %0\n\ts_nop 0\n\tbuffer_load_dwordx4 %1, %2, 0 offen nt lds" ::"s"(dst), "v"(vo), "s"(srdV) : "memory");
        else     asm volatile("s_mov_b32 m0, %0\n\ts_nop 0\n\tbuffer_load_dwordx4 %1, %2, 0 offen nt lds" ::"s"(dst), "v"(vo), "s"(srdK) : "memory");
      }
    }
    u2 ep; u4 shw;
    asm volatile("buffer_load_dwordx2 %0, off, %2, 0 sc1\n\tbuffer_load_dwordx4 %1, %3, %4, 0 offen"
                 : "=&v"(ep), "=&v"(shw)
                 : "s"(srdS), "v"((dt2 * 80 + (wave >= 3 ? wave - 3 : 0) * 16 + (lane >> 3) * 2) * 8), "s"(make_srd(shk, 5120))
                 : "memory");
    static_assert(WS_EPOCH == 0, "epoch load above");
    if (wave == 0 && lane < 16) lds_st<unsigned>(ctl + lane * 4, 0u);
    asm volatile("s_waitcnt vmcnt(5)" : "+v"(xr[0]), "+v"(xr[1]), "+v"(xr[2]), "+v"(xr[3]), "+v"(xr[4])::"memory");
    STAMP(1);
    unsigned hp[5]; float m1, inv1;
    ln_row(xr, hp, m1, inv1);
#pragma unroll
    for (int j = 0; j < 5; ++j) lds_st<unsigned>(sb + L_HBUF + wave * 1344 + (lane + 64 * j) * 4, hp[j]);
    if (lane == 0) lds_st<u2>(ctl + 64 + wave * 8, u2{__float_as_uint(m1), __float_as_uint(inv1)});
    STAMP(2);
    asm volatile("s_waitcnt vmcnt(0)" : "+v"(ep), "+v"(shw)::"memory");
    const uint64_t tg = (((uint64_t)ep.y << 32) | ep.x) * 6364136223846793005ull + 1442695040888963407ull;
    tag = u2{(unsigned)__builtin_amdgcn_readfirstlane((unsigned)tg), (unsigned)__builtin_amdgcn_readfirstlane((unsigned)(tg >> 32))};
    STAMP(3);
    raw_barrier();
    if (wave < 3) {
      const unsigned tb = sb + L_HBUF + ((lane >> 2) & 3) * 1344 + (lane >> 4) * 32 + (lane & 3) * 8;
      u4 v[4];
#pragma unroll
      for (int i = 0; i < 4; ++i) { const int it = i < 3 ? wave + 3 * i : 9; v[i] = __builtin_bit_cast(u4, cat8(tr_read(tb + it * 128), tr_read(tb + it * 128 + 4 * 1344))); }
      const u4 sv = lds_ld<u4>(ctl + 64 + (lane & 3) * 16);
#pragma unroll
      for (int i = 0; i < 4; ++i)
        if (i < 3 || wave == 0) st16_sc1(rws, WS_H + (b * 640 + (i < 3 ? wave + 3 * i : 9) * 64 + lane) * 16, v[i]);
      if (wave == 1 && lane < 4) st16_sc1(rws, WS_ST + b * 64 + lane * 16, sv);
    } else {
      const int rg = lane & 7, cp = (wave - 3) * 8 + (lane >> 3);
      const unsigned ka = sb + L_KBUF + rg * 1296 + cp * 4;
      unsigned kr[11], vr[8];
#pragma unroll
      for (int i = 0; i < 11; ++i) kr[i] = lds_ld<unsigned>(ka + i * 160 + (i >> 3) * 16);
#pragma unroll
      for (int i = 0; i < 8; ++i) vr[i] = lds_ld<unsigned>(ka + 11264 + i * 160);
      SCHED_FENCE();
#pragma unroll
      for (int c = 0; c < 2; ++c) {
        const unsigned wa = c ? shw.z : shw.x, wb = c ? shw.w : shw.y;
        const float w0 = blo(wa), w1f = bhi(wa), w2f = blo(wb), w3 = bhi(wb);
        float kf[11], o[8];
#pragma unroll
        for (int i = 0; i < 11; ++i) kf[i] = c ? bhi(kr[i]) : blo(kr[i]);
#pragma unroll
        for (int i = 0; i < 8; ++i) o[i] = (kf[i] * w3 + kf[i + 1] * w2f + kf[i + 2] * w1f + kf[i + 3] * w0) * (c ? bhi(vr[i]) : blo(vr[i]));
        st16_sc1(rws, WS_KV + (dt2 * 80 + cp * 2 + c) * 4096 + (2040 - 64 * st - 8 * rg) * 2, u4{pk(o[7], o[6]), pk(o[5], o[4]), pk(o[3], o[2]), pk(o[1], o[0])});
      }
    }
    STAMP(4);
  }

  if (wave >= 4) {
    const int hw = wave - 4, pair = hw >> 1, half = hw & 1;
    const bool two = pair == 0 && b >= 128;
    const int ch0 = 2 * b + pair, ch1 = two ? 384 + b : ch0;
    Filt f0, f1;
    filt_load(f0, ssmk + (size_t)ch0 * 4096, half, lane);
    filt_load(f1, ssmk + (size_t)ch1 * 4096, half, lane);
    waitv<8>();
    if (ctr_arrive(ctl + C_H * 4, lane) == 7 && lane < 8) st8_sc1(rws, WS_F1 + lane * 4096 + b * 16, tag);
    STAMP(20 + hw);
    unsigned dd0 = *(const unsigned short*)(ssmd + ch0 * 2), dd1 = *(const unsigned short*)(ssmd + ch1 * 2);
    __builtin_amdgcn_s_setprio(HPRIO);
    ctr_wait(ctl + C_H * 4, 8);
    const unsigned cv = sb + L_CONV + pair * CV_BYTES;
    filt_build(f0, cv, half, lane);
    asm volatile("" : "+v"(f1.c[0]), "+v"(f1.c[1]), "+v"(f1.n[0]), "+v"(f1.n[1]), "+v"(dd0), "+v"(dd1));
    ctr_wait(ctl + C_GO1 * 4, 1);
    u4 xv0[2], xv1[2];
#pragma unroll
    for (int j = 0; j < 2; ++j) {
      xv0[j] = ld16_sc1(rws, WS_KV + ch0 * 4096 + (lane + 64 * j + 128 * half) * 16);
      xv1[j] = ld16_sc1(rws, WS_KV + ch1 * 4096 + (lane + 64 * j + 128 * half) * 16);
    }
    if (b == 0 && threadIdx.x == 448) st8_sc1(rws, WS_EPOCH, tag);
    const int c16 = lane & 15, kg = lane >> 4;
    const unsigned pc = ctl + (C_P0 + pair) * 4;
    const unsigned ab = cv + (c16 & 3) * CV_CP + (c16 >> 2) * 8 + kg * 16 + half * 256;
    const unsigned bb = cv + CV_X + 480 - 32 * c16 + 16 * kg + half * 256;
#pragma unroll 1
    for (int round = 0; round < (two ? 2 : 1); ++round) {
      if (round) {
        if (has1) ctr_wait(ctl + C_G3 * 4, 4);
        filt_build(f1, cv, half, lane);
        xv0[0] = xv1[0]; xv0[1] = xv1[1]; dd0 = dd1;
      }
#pragma unroll
      for (int j = 0; j < 2; ++j) lds_st<u4>(cv + CV_X + (lane + 64 * j + 128 * half) * 16, xv0[j]);
      ctr_arrive(pc, lane);
      ctr_wait(pc, round * 8 + 2);
      f4 acc[8];
      conv_phases(acc, ab, bb);
      if (round == 0) STAMP(25 + hw);
      ctr_arrive(pc, lane);
      ctr_wait(pc, round * 8 + 4);
#pragma unroll
      for (int n = 0; n < 4; ++n) lds_st<f4>(cv + half * 4096 + n * 1024 + lane * 16, sel4(half, acc[n], acc[n + 4]));
      ctr_arrive(pc, lane);
      ctr_wait(pc, round * 8 + 6);
      const float dd = blo(dd0);
      u2 y[4];
#pragma unroll
      for (int n = 0; n < 4; ++n) {
        const f4 t = sel4(half, acc[n + 4], acc[n]) + lds_ld<f4>(cv + (half ^ 1) * 4096 + n * 1024 + lane * 16);
        const int t0 = 1024 * half + 256 * n + 16 * c16 + 4 * kg;
        const u2 xq = lds_ld<u2>(cv + CV_X + (2044 - t0) * 2);
        y[n] = u2{pk(__builtin_fmaf(bhi(xq.y), dd, t[0]), __builtin_fmaf(blo(xq.y), dd, t[1])),
                  pk(__builtin_fmaf(bhi(xq.x), dd, t[2]), __builtin_fmaf(blo(xq.x), dd, t[3]))};
      }
      ctr_arrive(pc, lane);
#pragma unroll
      for (int n = 0; n < 4; n += 2) {
        const auto sx = __builtin_amdgcn_permlane16_swap(y[n].x, y[n + 1].x, false, false);
        const auto sy = __builtin_amdgcn_permlane16_swap(y[n].y, y[n + 1].y, false, false);
        st16_sc1(rws, WS_YA + (round ? ch1 : ch0) * 4096 + (1024 * half + 256 * (n + (kg & 1)) + 16 * c16 + 8 * (kg >> 1)) * 2, u4{sx[0], sy[0], sx[1], sy[1]});
      }
      ctr_wait(pc, round * 8 + 8);
    }
    waitv<0>();
    if (ctr_arrive(ctl + (C_Q0 + pair) * 4, lane) == 1 && lane < (two ? 2 : 1)) st8_sc1(rws, WS_F3 + (lane ? ch1 : ch0) * 8, tag);
    STAMP(32 + hw);
    const int s = st * 64 + hw * 16 + c16;
    const size_t xo = (size_t)s * 1280 + (dt2 * 80 + 4 * kg) * 2;
    u2 xq[2] = {*(const u2*)(x + xo + 96), *(const u2*)(x + xo + 128)};
    u2 stt = ld8_sc1(rws, WS_ST + s * 8);
    unsigned b2r = *(const unsigned short*)(b2 + s * 2);
    asm volatile("" : "+v"(xq[0]), "+v"(xq[1]), "+v"(stt), "+v"(b2r));
    if (hw == 3) {
#pragma unroll 1
      for (unsigned n = 0; n < SPIN_CAP; ++n) {
        const u4 a = ld16_sc1(rws, WS_F3 + dt2 * 640 + (lane < 40 ? lane : 0) * 16);
        if (__all(a.x == tag.x && a.y == tag.y && a.z == tag.x && a.w == tag.y)) break;
        __builtin_amdgcn_s_sleep(2);
        asm volatile("" ::: "memory");
      }
      asm volatile("" ::: "memory");
      STAMP(37);
      u4 y[10];
#pragma unroll
      for (int i = 0; i < 10; ++i) y[i] = ld16_sc1(rws, WS_YA + (dt2 * 80 + i * 8 + (lane >> 3)) * 4096 + st * 128 + (lane & 7) * 16);
      SCHED_FENCE();
#pragma unroll
      for (int i = 0; i < 10; ++i) lds_st<u4>(sb + L_YA + i * 1024 + lane * 16, y[i]);
      STAMP(38);
    }
    raw_barrier();
    f4 own[2];
#pragma unroll
    for (int m = 0; m < 2; ++m) {
      const unsigned a = sb + (3 + m) * 1024 + lane * 16;
      own[m] = (lds_ld<f4>(a + hw * L_RING) + lds_ld<f4>(a + ((hw - 1) & 3) * L_RING + 5120)) +
               (lds_ld<f4>(a + ((hw - 2) & 3) * L_RING + 10240) + lds_ld<f4>(a + ((hw - 3) & 3) * L_RING + 15360));
    }
    final_tiles<3, 2>(own, xq, stt, b2r, sb + L_YA + (4 * kg + ((lane >> 2) & 3)) * 128 + hw * 32 + (lane & 3) * 8, out + xo);
    return;
  }

  waitv<0>();
  if (ctr_arrive(ctl + C_H * 4, lane) == 7 && lane < 8) st8_sc1(rws, WS_F1 + lane * 4096 + b * 16, tag);
  STAMP(16 + wave);
  __builtin_amdgcn_s_setprio(GPRIO);
#define POLL_F1()                                                                                                            \
  do {                                                                                                                       \
    if (wave == 0) {                                                                                                         \
      _Pragma("unroll 1") for (unsigned n = 0; n < SPIN_CAP; ++n) {                                                          \
        bool ok = true;                                                                                                      \
        _Pragma("unroll") for (int i = 0; i < 4; ++i) {                                                                      \
          const u2 a = ld8_sc1(rws, WS_F1 + xg * 4096 + i * 1024 + lane * 16);                                               \
          ok = ok && a.x == tag.x && a.y == tag.y;                                                                           \
        }                                                                                                                    \
        if (__all(ok)) break;                                                                                                \
        asm volatile("" ::: "memory");                                                                                       \
      }                                                                                                                      \
      asm volatile("" ::: "memory");                                                                                         \
      if (lane == 0) lds_st<unsigned>(ctl + C_GO1 * 4, 1u);                                                                  \
    } else {                                                                                                                 \
      ctr_wait(ctl + C_GO1 * 4, 1);                                                                                          \
    }                                                                                                                        \
  } while (0)
  unsigned tra[4];
  s4 wf[2][8];

  if (has1) {
    {
      const int fsw = 2 * (q >> 1) + 4 * (g & 1);
#pragma unroll
      for (int m = 0; m < 4; ++m) tra[m] = ring + (8 * g + q) * 128 + (((2 * m) ^ fsw) + (p >> 1)) * 16 + (p & 1) * 8;
    }
    u2 bias[4];
#pragma unroll
    for (int m = 0; m < 4; ++m) bias[m] = *(const u2*)(b1 + (ft * 64 + 16 * m + 4 * g) * 2);
    unsigned voh[4];
#pragma unroll
    for (int n = 0; n < 4; ++n) voh[n] = WS_H + (g * 640 + dt * 64 + ((n + wave) & 3) * 16 + (lane & 15)) * 16;
    u4 hq[4][4];
    f4 acc[4][4];
#pragma unroll
    for (int m = 0; m < 4; ++m)
#pragma unroll
      for (int n = 0; n < 4; ++n) acc[m][n] = f4{0.f, 0.f, 0.f, 0.f};
#define H_PIECE(i, n) LD_SC1(hq[(i) & 3][n], voh[n], KS1(i) * 40960u, 0)
#define H_LOAD(i) do { H_PIECE(i, 0); H_PIECE(i, 1); H_PIECE(i, 2); H_PIECE(i, 3); } while (0)
#define H_WAIT(N, hs) asm volatile("s_waitcnt vmcnt(%4)" : "+v"(hs[0]), "+v"(hs[1]), "+v"(hs[2]), "+v"(hs[3]) : "i"(N) : "memory")
#define G1_RD(i, k) wf[(i) & 1][k] = tr_read(tra[(k) >> 1] + ((i) % 7) * 4096 + ((k) & 1) * 512)
    s4 res[R1][8];
    ctr_wait(ctl + C_H * 4, 8);
    W1_DMA(0); W1_DMA(1); W1_DMA(2); W1_DMA(3); W1_DMA(4); W1_DMA(5); W1_DMA(6);
#pragma unroll
    for (int i = 0; i < R1; ++i) {
      waitv<24>();
#pragma unroll
      for (int k = 0; k < 8; ++k) res[i][k] = tr_read(tra[k >> 1] + i * 4096 + (k & 1) * 512);
      asm volatile("s_waitcnt lgkmcnt(0)" ::: "memory");
      W1_DMA(7 + i);
    }
    asm volatile("" : "+v"(bias[0]), "+v"(bias[1]), "+v"(bias[2]), "+v"(bias[3]));
    STAMP(5 + wave);
    POLL_F1();
    STAMP(9 + wave);
    H_LOAD(0); H_LOAD(1); H_LOAD(2);
#define G1_A(i, k) ((i) < R1 ? cat8(res[(i) < R1 ? (i) : 0][(k) & 6], res[(i) < R1 ? (i) : 0][(k) | 1]) : cat8(wf[(i) & 1][(k) & 6], wf[(i) & 1][(k) | 1]))
#define G1_GROUP(i, k)                                                                                                       \
  do {                                                                                                                       \
    acc[(k) >> 1][((k) & 1) * 2] = mfma16(G1_A(i, k), __builtin_bit_cast(bf8, hq[(i) & 3][((k) & 1) * 2]), acc[(k) >> 1][((k) & 1) * 2]); \
    acc[(k) >> 1][((k) & 1) * 2 + 1] = mfma16(G1_A(i, k), __builtin_bit_cast(bf8, hq[(i) & 3][((k) & 1) * 2 + 1]), acc[(k) >> 1][((k) & 1) * 2 + 1]); \
    SCHED_FENCE();                                                                                                           \
    if ((i) + 1 < 16 && (i) + 1 >= R1) G1_RD((i) + 1, k);                                                                    \
    if ((k) < 4) { if (g1_h(i)) H_PIECE((i) + 3, (k) & 3); }                                                                 \
    else if (g1_x(i) && (i) <= 9) W1_PIECE((i) + 6, (k) & 3);                                                                \
    else if (g1_x(i)) W2_PIECE((i) - 13, (k) & 3);                                                                           \
    SCHED_FENCE();                                                                                                           \
  } while (0)
#define G1_STEP(i)                                                                                                           \
  do {                                                                                                                       \
    H_WAIT(g1_n(i), hq[(i) & 3]);                                                                                            \
    G1_GROUP(i, 0); G1_GROUP(i, 1); G1_GROUP(i, 2); G1_GROUP(i, 3); G1_GROUP(i, 4); G1_GROUP(i, 5); G1_GROUP(i, 6); G1_GROUP(i, 7); \
  } while (0)
    G1_STEP(0); G1_STEP(1); G1_STEP(2); G1_STEP(3); G1_STEP(4); G1_STEP(5); G1_STEP(6); G1_STEP(7); G1_STEP(8); G1_STEP(9);
#pragma unroll
    for (int t = 0; t < 4; ++t) vow[t] = (8 * t + (lane >> 3)) * 4096 + st * 128 + (((lane & 7) ^ ((lane >> 3) & 6)) * 16) - 1024 * t;
    G1_STEP(10); G1_STEP(11); G1_STEP(12); G1_STEP(13); G1_STEP(14); G1_STEP(15);
    STAMP(40 + wave);
#pragma unroll
    for (int n = 1; n < 4; ++n)
#pragma unroll
      for (int m = 0; m < 4; ++m) lds_st<f4>(ring + (n == 1 ? 6 : n - 2) * 4096 + m * 1024 + lane * 16, acc[m][n]);
    ctr_arrive(ctl + C_G1 * 4, lane);
    STAMP(14);
    ctr_wait(ctl + C_G1 * 4, 4);
    STAMP(29);
#pragma unroll
    for (int n = 1; n < 4; ++n) {
      const unsigned a = sb + ((wave - n) & 3) * L_RING + (n == 1 ? 6 : n - 2) * 4096 + lane * 16;
#pragma unroll
      for (int m = 0; m < 4; ++m) acc[m][n] = lds_ld<f4>(a + m * 1024);
    }
    SCHED_FENCE();
#pragma unroll
    for (int m = 0; m < 4; ++m) acc[m][0] = (acc[m][0] + acc[m][1]) + (acc[m][2] + acc[m][3]);
    unsigned o[8];
#pragma unroll
    for (int m = 0; m < 4; ++m) {
      o[2 * m] = pk(gelu_t(acc[m][0][0] + blo(bias[m].x)), gelu_t(acc[m][0][1] + bhi(bias[m].x)));
      o[2 * m + 1] = pk(gelu_t(acc[m][0][2] + blo(bias[m].y)), gelu_t(acc[m][0][3] + bhi(bias[m].y)));
    }
#pragma unroll
    for (int P = 0; P < 2; ++P)
      st16_sc1(rws, WS_U + ((ft * 2 + P) * 40 + dt * 4 + wave) * 1024 + lane * 16, u4{o[4 * P], o[4 * P + 1], o[4 * P + 2], o[4 * P + 3]});
    STAMP(13);
    W2_DMA(2);
    waitv<4>();
    if (ctr_arrive(ctl + C_G3 * 4, lane) == 3 && lane < 8) st8_sc1(rws, WS_F2 + lane * 4096 + (dt * 24 + ft) * 16, tag);
    STAMP(44 + wave);
    ctr_wait(ctl + C_G3 * 4, 4);
    W2_DMA(3); W2_DMA(4); W2_DMA(5); W2_DMA(6);
  } else {
#pragma unroll
    for (int t = 0; t < 4; ++t) vow[t] = (8 * t + (lane >> 3)) * 4096 + st * 128 + (((lane & 7) ^ ((lane >> 3) & 6)) * 16) - 1024 * t;
    POLL_F1();
    W2_DMA(0); W2_DMA(1); W2_DMA(2); W2_DMA(3); W2_DMA(4); W2_DMA(5); W2_DMA(6);
  }

  asm volatile("" : "+s"(ring), "+s"(rot2));
  {
    const int fsw = 4 * (g & 1) + (q & 2);
#pragma unroll
    for (int n = 0; n < 4; ++n) tra[n] = ring + (4 * g + q) * 128 + (((2 * ((n + wave) & 3)) ^ fsw) + (p >> 1)) * 16 + (p & 1) * 8;
  }
  const unsigned vou0 = WS_U + dt2 * 5120 + lane * 16, vou1 = vou0 + 4096;
  u4 uq[4][5];
  f4 acc[5][4];
#pragma unroll
  for (int m = 0; m < 5; ++m)
#pragma unroll
    for (int n = 0; n < 4; ++n) acc[m][n] = f4{0.f, 0.f, 0.f, 0.f};
#define U_PIECE(j, m) LD_SC1(uq[(j) & 3][m], (m) < 4 ? vou0 : vou1, KS2(j) * 40960u, ((m) & 3) * 1024)
#define U_LOAD(j) do { U_PIECE(j, 0); U_PIECE(j, 1); U_PIECE(j, 2); U_PIECE(j, 3); U_PIECE(j, 4); } while (0)
#define U_WAIT(N, us) asm volatile("s_waitcnt vmcnt(%5)" : "+v"(us[0]), "+v"(us[1]), "+v"(us[2]), "+v"(us[3]), "+v"(us[4]) : "i"(N) : "memory")
#define G2_RD(j, k) wf[(j) & 1][k] = tr_read(tra[(k) >> 1] + (((j) + 2) % 7) * 4096 + ((k) & 1) * 2048)
#define G2_B(j, n) ((j) < R2 ? cat8(res2[(j) < R2 ? (j) : 0][2 * (n)], res2[(j) < R2 ? (j) : 0][2 * (n) + 1]) : cat8(wf[(j) & 1][2 * (n)], wf[(j) & 1][2 * (n) + 1]))
#define G2_MM(j, m, n) acc[m][n] = mfma16(__builtin_bit_cast(bf8, uq[(j) & 3][m]), G2_B(j, n), acc[m][n])
#define G2_GROUP(j, k)                                                                                                       \
  do {                                                                                                                       \
    G2_MM(j, (2 * (k)) % 5, (2 * (k)) / 5); G2_MM(j, (2 * (k) + 1) % 5, (2 * (k) + 1) / 5);                                  \
    SCHED_FENCE();                                                                                                           \
    if ((k) < 8 && (j) + 1 < 12 && (j) + 1 >= R2) G2_RD((j) + 1, (k) & 7);                                                   \
    if ((k) < 5) { if (g2_u(j)) U_PIECE((j) + 3, (k) % 5); }                                                                 \
    else if ((k) < 9 && g2_x(j)) W2_PIECE((j) + 6, ((k) - 5) & 3);                                                           \
    SCHED_FENCE();                                                                                                           \
  } while (0)
#define G2_STEP(j)                                                                                                           \
  do {                                                                                                                       \
    U_WAIT(g2_n(j), uq[(j) & 3]);                                                                                                  \
    G2_GROUP(j, 0); G2_GROUP(j, 1); G2_GROUP(j, 2); G2_GROUP(j, 3); G2_GROUP(j, 4);                                          \
    G2_GROUP(j, 5); G2_GROUP(j, 6); G2_GROUP(j, 7); G2_GROUP(j, 8); G2_GROUP(j, 9);                                          \
  } while (0)
  s4 res2[R2][8];
  static_assert(R2 <= 3, "wait below");
  waitv<16>();
#pragma unroll
  for (int j = 0; j < R2; ++j) {
#pragma unroll
    for (int k = 0; k < 8; ++k) res2[j][k] = tr_read(tra[k >> 1] + ((j + 2) % 7) * 4096 + (k & 1) * 2048);
    asm volatile("s_waitcnt lgkmcnt(0)" ::: "memory");
    W2_DMA(7 + j);
  }
  const int s = st * 64 + wave * 16 + (lane & 15);
  const size_t xo = (size_t)s * 1280 + (dt2 * 80 + 4 * g) * 2;
  u2 xr[3];
#pragma unroll
  for (int m = 0; m < 3; ++m) xr[m] = *(const u2*)(x + xo + m * 32);
  u2 stt = ld8_sc1(rws, WS_ST + s * 8);
  unsigned b2r = *(const unsigned short*)(b2 + s * 2);
  asm volatile("" : "+v"(xr[0]), "+v"(xr[1]), "+v"(xr[2]), "+v"(stt), "+v"(b2r));
  waitv<0>();
  STAMP(39);
  if (wave == 0) {
    const int dta = (dt2 * 5) >> 2;
#pragma unroll 1
    for (unsigned n = 0; n < SPIN_CAP; ++n) {
      const u2 a = ld8_sc1(rws, WS_F2 + xg * 4096 + (dta * 24 + (lane < 48 ? lane : 0)) * 16);
      if (__all(a.x == tag.x && a.y == tag.y)) break;
      asm volatile("" ::: "memory");
    }
    asm volatile("" ::: "memory");
    if (lane == 0) lds_st<unsigned>(ctl + C_GO2 * 4, 1u);
  } else {
    ctr_wait(ctl + C_GO2 * 4, 1);
  }
  STAMP(48 + wave);
  U_LOAD(0); U_LOAD(1); U_LOAD(2);
  G2_STEP(0); G2_STEP(1); G2_STEP(2); G2_STEP(3); G2_STEP(4); G2_STEP(5); G2_STEP(6); G2_STEP(7); G2_STEP(8); G2_STEP(9); G2_STEP(10); G2_STEP(11);
  STAMP(52 + wave);
#pragma unroll
  for (int n = 0; n < 4; ++n)
#pragma unroll
    for (int m = (n == 0 ? 3 : 0); m < 5; ++m) lds_st<f4>(ring + (n * 5 + m) * 1024 + lane * 16, acc[m][n]);
  f4 own[3] = {acc[0][0], acc[1][0], acc[2][0]};
  raw_barrier();
  STAMP(56 + wave);
#pragma unroll
  for (int w = 1; w < 4; ++w) {
    const unsigned a = sb + ((wave - w) & 3) * L_RING + w * 5120 + lane * 16;
#pragma unroll
    for (int m = 0; m < 3; ++m) own[m] += lds_ld<f4>(a + m * 1024);
  }
  final_tiles<0, 3>(own, xr, stt, b2r, sb + L_YA + (4 * g + q) * 128 + wave * 32 + p * 8, out + xo);
  STAMP(60 + wave);
  if (threadIdx.x == 0) ((unsigned long long*)(ws + WS_DBG))[blockIdx.x * 16 + 15] = clk_r0 + (__builtin_amdgcn_s_memtime() - clk_c0) / 24;
}

__global__ void clr(char* ws) { unsigned long long* p = (unsigned long long*)(ws + WS_DBG); for (int i = threadIdx.x + blockIdx.x * 256; i < 4 * 512 * 16; i += 256 * 32) p[i] = 0; }
__global__ void dump(char* ws) {
  unsigned long long* p = (unsigned long long*)(ws + WS_DBG);
  __shared__ unsigned long long t0s;
  int s = threadIdx.x;
  unsigned long long mn = ~0ull, mx = 0;
  for (int b = 0; b < 256; ++b) { unsigned long long v = p[((s >> 4) * 512 + b) * 16 + (s & 15)]; if (v) { mn = v < mn ? v : mn; mx = v > mx ? v : mx; } }
  if (s == 0) t0s = mn;
  __syncthreads();
  if (mx) printf("s%d[%d,%d]\n", s, (int)(mn - t0s), (int)(mx - t0s));
}
extern "C" void kernel_launch(void* const* d_in, const int* in_sizes, int n_in, void* d_out, int out_size, void* d_ws, size_t ws_size,
                              hipStream_t stream) {
  (void)hipFuncSetAttribute((const void*)fused_kernel, hipFuncAttributeMaxDynamicSharedMemorySize, L_TOTAL);
  clr<<<32, 256, 0, stream>>>((char*)d_ws);
  fused_kernel<<<256, 512, L_TOTAL, stream>>>((const char*)d_in[0], (const char*)d_in[1], (const char*)d_in[2], (const char*)d_in[3],
                                              (const char*)d_in[4], (const char*)d_in[5], (const char*)d_in[7], (const char*)d_in[8],
                                              (const char*)d_in[9], (const char*)d_in[10], (char*)d_out, (char*)d_ws);
  dump<<<1, 64, 0, stream>>>((char*)d_ws);
}
```

```cpp
#include <hip/hip_runtime.h>
#include <hip/hip_bf16.h>
#include <stdint.h>

typedef __attribute__((ext_vector_type(8))) __bf16 bf8;
typedef __attribute__((ext_vector_type(2))) __bf16 bf2;
typedef __attribute__((ext_vector_type(4))) short s4;
typedef __attribute__((ext_vector_type(4))) float f4;
typedef __attribute__((ext_vector_type(2))) float f2;
typedef __attribute__((ext_vector_type(4))) unsigned u4;
typedef __attribute__((ext_vector_type(2))) unsigned u2;
#define LAS __attribute__((address_space(3)))
#define DEVI __device__ __forceinline__
#define SCHED_FENCE() __builtin_amdgcn_sched_barrier(0)

#define PF_W2 0
#define CONVQ 4
#define HPOL " sc1"
#define MAP1 0
#define R1 5
#define R2 3
#define THR 5
#define GPRIO 0
#define HPRIO 0
#define KVFIRST 1
#define LA 3
#define CODEPF 1
#define R2WAIT 1
#define HPOLL2 0
#define NT_FIRST 0

constexpr unsigned WS_EPOCH = 0;
constexpr unsigned WS_F1 = 4096;
constexpr unsigned WS_F2 = WS_F1 + 32768;
constexpr unsigned WS_F4 = WS_F2 + 32768;
constexpr unsigned WS_F3 = WS_F4 + 32768;
constexpr unsigned WS_ST = WS_F3 + 8192;
constexpr unsigned WS_H = WS_ST + 16384;
constexpr unsigned WS_KV = WS_H + 2621440;
constexpr unsigned WS_YA = WS_KV + 2621440;
constexpr unsigned WS_U = WS_YA + 2621440;
constexpr unsigned WS_END = WS_U + 1966080;
constexpr size_t WS_DBG = WS_END;

#define STAMP(idx) do {} while (0)

constexpr unsigned L_RING = 28672;
constexpr unsigned CV_CP = 4160;
constexpr unsigned CV_X = 4 * CV_CP;
constexpr unsigned CV_BYTES = 21248;
constexpr unsigned L_CONV = 4 * L_RING;
constexpr unsigned L_HBUF = L_CONV;
constexpr unsigned L_KBUF = L_CONV + 10752;
constexpr unsigned L_VBUF = L_KBUF + 11264;
constexpr unsigned L_YA = L_CONV + CV_BYTES;
constexpr unsigned L_CTL = L_CONV + 2 * CV_BYTES;
constexpr unsigned L_TOTAL = L_CTL + 128;
enum { C_H = 0, C_GO1, C_G1, C_G2, C_G3, C_GO2, C_P0, C_P1, C_Q0, C_Q1, C_KV, C_K2 };
constexpr unsigned SPIN_CAP = 1u << 16;

DEVI float blo(unsigned u) { return __uint_as_float(u << 16); }
DEVI float bhi(unsigned u) { return __uint_as_float(u & 0xffff0000u); }
DEVI unsigned pk(float a, float b) { f2 v = {a, b}; return __builtin_bit_cast(unsigned, __builtin_convertvector(v, bf2)); }
DEVI float rbf(float a) { return blo(pk(a, a)); }
template <int CTRL, int RM> DEVI float dppf(float x) {
  return __builtin_bit_cast(float, __builtin_amdgcn_update_dpp(0, __builtin_bit_cast(int, x), CTRL, RM, 0xf, true));
}
template <class T> DEVI T ldnt(const void* p) { return __builtin_nontemporal_load((const T*)p); }
DEVI u4 make_srd(const void* p, unsigned bytes) {
  uint64_t a = (uint64_t)p;
  u4 r = {(unsigned)a, (unsigned)(a >> 32) & 0xffffu, bytes, 0x00020000u};
  return r;
}
DEVI s4 tr_read(unsigned addr) { return __builtin_amdgcn_ds_read_tr16_b64_v4i16((LAS s4*)(size_t)addr); }
DEVI bf8 cat8(s4 a, s4 b) {
  typedef __attribute__((ext_vector_type(8))) short s8;
  s8 v = __builtin_shufflevector(a, b, 0, 1, 2, 3, 4, 5, 6, 7);
  return __builtin_bit_cast(bf8, v);
}
DEVI f4 mfma16(bf8 a, bf8 b, f4 c) { return __builtin_amdgcn_mfma_f32_16x16x32_bf16(a, b, c, 0, 0, 0); }
template <int N> DEVI void waitv() { asm volatile("s_waitcnt vmcnt(%0)" ::"i"(N) : "memory"); }
template <class T> DEVI T lds_ld(unsigned a) { return *(const LAS T*)(size_t)a; }
template <class T> DEVI void lds_st(unsigned a, T v) { *(LAS T*)(size_t)a = v; }
DEVI void raw_barrier() { asm volatile("s_waitcnt lgkmcnt(0)\n\ts_barrier" ::: "memory"); }
DEVI unsigned ctr_arrive(unsigned a, int lane) {
  asm volatile("s_waitcnt lgkmcnt(0)" ::: "memory");
  unsigned old = 0;
  if (lane == 0) old = __hip_atomic_fetch_add((LAS unsigned*)(size_t)a, 1u, __ATOMIC_RELAXED, __HIP_MEMORY_SCOPE_WORKGROUP);
  old = __builtin_amdgcn_readfirstlane(old);
  asm volatile("" ::: "memory");
  return old;
}
DEVI void ctr_wait(unsigned a, unsigned n) {
  while (__builtin_amdgcn_readfirstlane(__hip_atomic_load((LAS unsigned*)(size_t)a, __ATOMIC_RELAXED, __HIP_MEMORY_SCOPE_WORKGROUP)) < n)
    __builtin_amdgcn_s_sleep(1);
  asm volatile("" ::: "memory");
}
typedef __amdgpu_buffer_rsrc_t rsrc_t;
DEVI u4 ld16_sc1(rsrc_t r, unsigned off) { return __builtin_amdgcn_raw_buffer_load_b128(r, off, 0, 16); }
DEVI u2 ld8_sc1(rsrc_t r, unsigned off) { return __builtin_amdgcn_raw_buffer_load_b64(r, off, 0, 16); }
DEVI void st16_sc1(rsrc_t r, unsigned off, u4 v) { __builtin_amdgcn_raw_buffer_store_b128(v, r, off, 0, 16); }
DEVI void st8_sc1(rsrc_t r, unsigned off, u2 v) { __builtin_amdgcn_raw_buffer_store_b64(v, r, off, 0, 16); }

DEVI f4 sel4(bool c, f4 a, f4 b) {
  asm("" : "+v"(a)); asm("" : "+v"(b));
  return c ? a : b;
}
DEVI float gelu_t(float x) {
  const float t = x * __builtin_fmaf(x * x, -0.10294324f, -2.3022082f);
  return x * __builtin_amdgcn_rcpf(1.f + __builtin_amdgcn_exp2f(t));
}

DEVI void ln_row(const unsigned (&xr)[5], unsigned (&hp)[5], float& m1, float& inv1) {
  float e[10], s = 0.f, q = 0.f;
  const float piv = blo(__builtin_amdgcn_readfirstlane(xr[0]));
#pragma unroll
  for (int j = 0; j < 5; ++j) { e[2 * j] = blo(xr[j]); e[2 * j + 1] = bhi(xr[j]); }
#pragma unroll
  for (int j = 0; j < 10; ++j) { const float d = e[j] - piv; s += d; q = __builtin_fmaf(d, d, q); }
  s += dppf<0xB1, 0xf>(s);  q += dppf<0xB1, 0xf>(q);
  s += dppf<0x4E, 0xf>(s);  q += dppf<0x4E, 0xf>(q);
  s += dppf<0x141, 0xf>(s); q += dppf<0x141, 0xf>(q);
  s += dppf<0x140, 0xf>(s); q += dppf<0x140, 0xf>(q);
  s += dppf<0x142, 0xa>(s); q += dppf<0x142, 0xa>(q);
  s += dppf<0x143, 0xc>(s); q += dppf<0x143, 0xc>(q);
  const float md = __builtin_bit_cast(float, __builtin_amdgcn_readlane(__builtin_bit_cast(int, s), 63)) * (1.f / 640.f);
  const float qq = __builtin_bit_cast(float, __builtin_amdgcn_readlane(__builtin_bit_cast(int, q), 63)) * (1.f / 640.f);
  const float var = __builtin_fmaxf(qq - md * md, 0.f), mean = piv + md;
  m1 = rbf(mean);
  inv1 = __builtin_amdgcn_rcpf(rbf(__builtin_amdgcn_sqrtf(rbf(rbf(var) + 1e-5f))));
  const float m2 = rbf((mean - m1) * inv1);
  const float inv2 = __builtin_amdgcn_rcpf(rbf(__builtin_amdgcn_sqrtf(rbf(rbf(var * inv1 * inv1) + 1e-5f))));
  const float A = inv1 * inv2, B = -m2 * inv2;
#pragma unroll
  for (int j = 0; j < 5; ++j) hp[j] = pk(__builtin_fmaf(e[2 * j] - m1, A, B), __builtin_fmaf(e[2 * j + 1] - m1, A, B));
}

struct Filt { u4 c[2]; u2 n[2]; };
DEVI void filt_load(Filt& f, const char* krow, int half, int lane) {
#pragma unroll
  for (int j = 0; j < 2; ++j) {
    const int z = lane + 64 * j + 128 * half;
    f.c[j] = ldnt<u4>(krow + z * 16);
    f.n[j] = ldnt<u2>(krow + (z < 255 ? z * 16 + 16 : 0));
  }
}
DEVI void filt_build(Filt& f, unsigned cv, int half, int lane) {
  const u4 zero = {0u, 0u, 0u, 0u};
  if (half == 1) {
    if (lane == 63) f.n[1] = u2{0u, 0u};
    if (lane < 30) lds_st<u4>(cv + CV_X + 4096 + lane * 16, zero);
  } else {
    if (lane < 4) lds_st<u4>(cv + lane * CV_CP, zero);
    if (lane == 0) {
      const unsigned d4 = f.c[0].x, d5 = f.c[0].y;
      lds_st<u4>(cv + 16, u4{0u, 0u, 0u, d4 << 16});
      lds_st<u4>(cv + CV_CP + 16, u4{0u, 0u, 0u, d4});
      lds_st<u4>(cv + 2 * CV_CP + 16, u4{0u, 0u, d4 << 16, __builtin_amdgcn_alignbit(d5, d4, 16)});
      lds_st<u4>(cv + 3 * CV_CP + 16, u4{0u, 0u, d4, d5});
    }
  }
#pragma unroll
  for (int j = 0; j < 2; ++j) {
    const u4 c = f.c[j]; const u2 n = f.n[j];
    const unsigned a0 = __builtin_amdgcn_alignbit(c.y, c.x, 16), a1 = __builtin_amdgcn_alignbit(c.z, c.y, 16), a2 = __builtin_amdgcn_alignbit(c.w, c.z, 16),
                   a3 = __builtin_amdgcn_alignbit(n.x, c.w, 16), a4 = __builtin_amdgcn_alignbit(n.y, n.x, 16);
    const unsigned p = cv + (lane + 64 * j + 128 * half + 2) * 16;
    lds_st<u4>(p, u4{a0, a1, a2, a3});
    lds_st<u4>(p + CV_CP, u4{c.y, c.z, c.w, n.x});
    lds_st<u4>(p + 2 * CV_CP, u4{a1, a2, a3, a4});
    lds_st<u4>(p + 3 * CV_CP, u4{c.z, c.w, n.x, n.y});
  }
}

DEVI void conv_phases(f4 (&acc)[8], unsigned ab, unsigned bb) {
  bf8 A0[8], B0[8], A1[8], B1[8];
#pragma unroll
  for (int n = 0; n < 8; ++n) acc[n] = f4{0.f, 0.f, 0.f, 0.f};
#define CV_LOAD(A, B, q)                                                                              \
  _Pragma("unroll") for (int p = 0; p < 8; ++p) {                                                      \
    B[p] = *(const volatile LAS bf8*)(size_t)(bb + 3584 - 512 * p + 64 * (q));                          \
    s4 lo = *(const volatile LAS s4*)(size_t)(ab + 512 * p + 64 * (q));                                 \
    s4 hi = *(const volatile LAS s4*)(size_t)(ab + 512 * p + 64 * (q) + 8);                             \
    A[p] = cat8(lo, hi);                                                                               \
  }
#define CV_MMA(A, B)                                                                                   \
  _Pragma("unroll") for (int p = 0; p < 8; ++p)                                                        \
  _Pragma("unroll") for (int m = 0; m + p < 8; ++m) acc[p + m] = mfma16(A[p], B[m], acc[p + m]);
#define CV_SCHED()                                                                                     \
  _Pragma("unroll") for (int z = 0; z < 24; ++z) {                                                     \
    __builtin_amdgcn_sched_group_barrier(0x8, 1, 0);                                                   \
    __builtin_amdgcn_sched_group_barrier(0x100, 1, 0);                                                 \
  }                                                                                                    \
  __builtin_amdgcn_sched_group_barrier(0x8, 12, 0);                                                    \
  SCHED_FENCE();
  CV_LOAD(A0, B0, 0);
  SCHED_FENCE();
#pragma unroll 1
  for (int q = 0; q < CONVQ; q += 2) {
    CV_LOAD(A1, B1, q + 1); CV_MMA(A0, B0); CV_SCHED();
    CV_LOAD(A0, B0, (q + 2) & 3); CV_MMA(A1, B1); CV_SCHED();
  }
}

constexpr int g1_h(int i) { return i < 0 ? 4 : i + LA < 16 ? 4 : 0; }
constexpr int g1_x(int i) { return ((i > R1 && i <= 9) || i == 13 || i == 14) ? 4 : 0; }
constexpr int g1_n(int i) { int n = g1_x(i - LA); for (int t = i - LA + 1; t < i; ++t) n += g1_h(t) + g1_x(t); return n; }
constexpr int g2_u(int j) { return j < 0 ? 5 : j + LA < 12 ? 5 : 0; }
constexpr int g2_x(int j) { return (j > R2 && j <= 5) ? 4 : 0; }
constexpr int g2_n(int j) { int n = g2_x(j - LA); for (int t = j - LA + 1; t < j; ++t) n += g2_u(t) + g2_x(t); return n; }

template <int M0, int NM>
DEVI void final_tiles(f4 (&own)[NM], const u2 (&xr)[NM], u2 stt, unsigned b2r, unsigned yab, char* op) {
  const float bb = blo(b2r), m1 = __uint_as_float(stt.x), inv1 = __uint_as_float(stt.y);
#pragma unroll
  for (int m = 0; m < NM; ++m) {
    const u2 ya = __builtin_bit_cast(u2, tr_read(yab + (M0 + m) * 2048));
    const float x0 = blo(xr[m].x), x1 = bhi(xr[m].x), x2 = blo(xr[m].y), x3 = bhi(xr[m].y);
    const float r0 = __builtin_fmaf((x0 - m1) * inv1, blo(ya.x), x0) + (own[m][0] + bb), r1 = __builtin_fmaf((x1 - m1) * inv1, bhi(ya.x), x1) + (own[m][1] + bb);
    const float r2 = __builtin_fmaf((x2 - m1) * inv1, blo(ya.y), x2) + (own[m][2] + bb), r3 = __builtin_fmaf((x3 - m1) * inv1, bhi(ya.y), x3) + (own[m][3] + bb);
    *(u2*)(op + (M0 + m) * 32) = u2{pk(r0, r1), pk(r2, r3)};
  }
}

__global__ __launch_bounds__(512, 2) __attribute__((amdgpu_waves_per_eu(2, 2))) void fused_kernel(
    const char* __restrict__ x, const char* __restrict__ kk, const char* __restrict__ vv, const char* __restrict__ shk,
    const char* __restrict__ ssmk, const char* __restrict__ ssmd, const char* __restrict__ w1, const char* __restrict__ b1,
    const char* __restrict__ w2, const char* __restrict__ b2, char* __restrict__ out, char* ws) {
  extern __shared__ __attribute__((aligned(16))) char smem[];
  const int lane = threadIdx.x & 63, wave = __builtin_amdgcn_readfirstlane(threadIdx.x >> 6);
  const int b = blockIdx.x, xg = b & 7, r = b >> 3;
  const unsigned sb = __builtin_amdgcn_readfirstlane((unsigned)(size_t)smem);
  const unsigned ctl = sb + L_CTL;
  asm volatile("" ::"s"(x), "s"(kk), "s"(vv), "s"(shk), "s"(ssmk), "s"(ssmd), "s"(w1), "s"(b1), "s"(w2), "s"(b2), "s"(out), "s"(ws));
  const rsrc_t rws = __builtin_amdgcn_make_buffer_rsrc(ws, 0, WS_END, 0x00020000);
  if (wave == 0) STAMP(0);
  const int dt2 = r & 7, st = xg * 4 + (r >> 3);

  const u4 srdS = make_srd(ws, WS_END);
  const int g = lane >> 4, q = (lane >> 2) & 3, p = lane & 3;
  unsigned ring = sb + wave * L_RING;
  const bool has1 = r < 30;
  const int dt = MAP1 ? (xg & 1) * 5 + r % 5 : r % 10, ft = MAP1 ? (xg >> 1) * 6 + r / 5 : xg * 3 + r / 10;
  const int rot1 = dt * 16 / 10;
  int rot2 = dt2 * 12 / 8;
  const u4 srdW1 = make_srd(w1, 6291456), srdW2 = make_srd(w2, 6291456);
  unsigned vow[4];
#define DMA1(POL, slot, t, srd, soff)                                                                                          \
  asm volatile("s_mov_b32 m0, %0\n\ts_nop 0\n\tbuffer_load_dwordx4 %1, %2, %3 offen offset:%4" POL " lds" ::"s"(ring + (slot) * 4096), \
               "v"(vow[t]), "s"(srd), "s"(soff), "i"((t) * 1024)                                                               \
               : "memory")
#define KS1(i) (4u * (((i) + rot1) & 15) + wave)
#define KS2(j) (4u * ((j) + rot2 >= 12 ? (j) + rot2 - 12 : (j) + rot2) + wave)
#define W1_PIECE(i, t) do { if ((i) < NT_FIRST) DMA1(" nt", (i) % 7, t, srdW1, KS1(i) * 98304u); else DMA1("", (i) % 7, t, srdW1, KS1(i) * 98304u); } while (0)
#define W2_PIECE(j, t) do { if ((j) < NT_FIRST) DMA1(" nt", ((j) + 2) % 7, t, srdW2, KS2(j) * 131072u); else DMA1("", ((j) + 2) % 7, t, srdW2, KS2(j) * 131072u); } while (0)
#define W1_DMA(i) do { W1_PIECE(i, 0); W1_PIECE(i, 1); W1_PIECE(i, 2); W1_PIECE(i, 3); } while (0)
#define W2_DMA(j) do { W2_PIECE(j, 0); W2_PIECE(j, 1); W2_PIECE(j, 2); W2_PIECE(j, 3); } while (0)
#define W1_THR(i) do { W1_PIECE(i, 0); waitv<THR>(); W1_PIECE(i, 1); waitv<THR>(); W1_PIECE(i, 2); waitv<THR>(); W1_PIECE(i, 3); waitv<THR>(); } while (0)
#define W2_THR(j) do { W2_PIECE(j, 0); waitv<THR>(); W2_PIECE(j, 1); waitv<THR>(); W2_PIECE(j, 2); waitv<THR>(); W2_PIECE(j, 3); waitv<THR>(); } while (0)
#define LD_SC1(dst, voff, soff, imm) \
  asm volatile("buffer_load_dwordx4 %0, %1, %2, %3 offen offset:%4" HPOL : "=&v"(dst) : "v"(voff), "s"(srdS), "s"(soff), "i"(imm) : "memory")
#pragma unroll
  for (int t = 0; t < 4; ++t) vow[t] = (8 * t + (lane >> 3)) * 3072 + ft * 128 + (((lane & 7) ^ (2 * ((lane >> 4) & 1) + 4 * (t & 1))) * 16) - 1024 * t;
  u2 tag;
  {
    unsigned xr[5];
    asm volatile("buffer_load_dword %0, %5, %6, %7 offen nt\n\tbuffer_load_dword %1, %5, %6, %7 offen offset:256 nt\n\t"
                 "buffer_load_dword %2, %5, %6, %7 offen offset:512 nt\n\tbuffer_load_dword %3, %5, %6, %7 offen offset:768 nt\n\t"
                 "buffer_load_dword %4, %5, %6, %7 offen offset:1024 nt"
                 : "=&v"(xr[0]), "=&v"(xr[1]), "=&v"(xr[2]), "=&v"(xr[3]), "=&v"(xr[4])
                 : "v"(lane * 4), "s"(make_srd(x, 2621440)), "s"((b * 8 + wave) * 1280)
                 : "memory");
    {
      const u4 srdK = make_srd(kk, 2621440), srdV = make_srd(vv, 2621440);
#pragma unroll
      for (int i = 0; i < 3; ++i) {
        const int pc = wave + 8 * i;
        const bool isv = pc >= 11;
        const int n = (isv ? pc - 11 : pc) * 64 + lane, g8 = (n * 6473) >> 19, rem = n - g8 * 81, rr = (rem * 205) >> 11;
        const int row = g8 * 8 + rr, c = rem - rr * 10;
        const int grow = st * 64 + row - (isv ? 0 : 3);
        const bool ok = rem < 80 && grow >= 0 && row < (isv ? 64 : 67);
        const unsigned vo = ok ? grow * 1280 + dt2 * 160 + c * 16 : 0x7fffff00u;
        const unsigned dst = sb + L_KBUF + pc * 1024;
        if (isv) asm volatile("s_mov_b32 m0, %0\n\ts_nop 0\n\tbuffer_load_dwordx4 %1, %2, 0 offen nt lds" ::"s"(dst), "v"(vo), "s"(srdV) : "memory");
        else     asm volatile("s_mov_b32 m0, %0\n\ts_nop 0\n\tbuffer_load_dwordx4 %1, %2, 0 offen nt lds" ::"s"(dst), "v"(vo), "s"(srdK) : "memory");
      }
    }
    u2 ep; u4 shw;
    asm volatile("buffer_load_dwordx2 %0, off, %2, 0 sc1\n\tbuffer_load_dwordx4 %1, %3, %4, 0 offen"
                 : "=&v"(ep), "=&v"(shw)
                 : "s"(srdS), "v"((dt2 * 80 + (wave >= 3 ? wave - 3 : 0) * 16 + (lane >> 3) * 2) * 8), "s"(make_srd(shk, 5120))
                 : "memory");
    static_assert(WS_EPOCH == 0, "epoch load above");
    unsigned cpf = 0;
    if (CODEPF && wave == 7) {
      uint64_t pc; unsigned len;
      asm volatile("s_getpc_b64 %0\n.Lfk_here:\n\ts_mov_b32 %1, .Lfk_tail-.Lfk_here" : "=s"(pc), "=s"(len));
      const unsigned o = (r * 16 + (lane & 15)) * 128;
      asm volatile("global_load_dword %0, %1, %2" : "=v"(cpf) : "v"(o < len ? o : 0u), "s"(pc) : "memory");
    }
    if (wave == 0 && lane < 16) lds_st<unsigned>(ctl + lane * 4, 0u);
    asm volatile("s_waitcnt vmcnt(5)" : "+v"(xr[0]), "+v"(xr[1]), "+v"(xr[2]), "+v"(xr[3]), "+v"(xr[4])::"memory");
    STAMP(1);
    unsigned hp[5]; float m1, inv1;
    ln_row(xr, hp, m1, inv1);
#pragma unroll
    for (int j = 0; j < 5; ++j) lds_st<unsigned>(sb + L_HBUF + wave * 1344 + (lane + 64 * j) * 4, hp[j]);
    if (lane == 0) lds_st<u2>(ctl + 64 + wave * 8, u2{__float_as_uint(m1), __float_as_uint(inv1)});
    STAMP(2);
    asm volatile("s_waitcnt vmcnt(0)" : "+v"(ep), "+v"(shw), "+v"(cpf)::"memory");
    const uint64_t tg = (((uint64_t)ep.y << 32) | ep.x) * 6364136223846793005ull + 1442695040888963407ull;
    tag = u2{(unsigned)__builtin_amdgcn_readfirstlane((unsigned)tg), (unsigned)__builtin_amdgcn_readfirstlane((unsigned)(tg >> 32))};
    STAMP(3);
    raw_barrier();
    if (wave < 3) {
      const unsigned tb = sb + L_HBUF + ((lane >> 2) & 3) * 1344 + (lane >> 4) * 32 + (lane & 3) * 8;
      u4 v[4];
#pragma unroll
      for (int i = 0; i < 4; ++i) { const int it = i < 3 ? wave + 3 * i : 9; v[i] = __builtin_bit_cast(u4, cat8(tr_read(tb + it * 128), tr_read(tb + it * 128 + 4 * 1344))); }
      const u4 sv = lds_ld<u4>(ctl + 64 + (lane & 3) * 16);
#pragma unroll
      for (int i = 0; i < 4; ++i)
        if (i < 3 || wave == 0) st16_sc1(rws, WS_H + (b * 640 + (i < 3 ? wave + 3 * i : 9) * 64 + lane) * 16, v[i]);
      if (wave == 1 && lane < 4) st16_sc1(rws, WS_ST + b * 64 + lane * 16, sv);
    } else {
      const int rg = lane & 7, cp = (wave - 3) * 8 + (lane >> 3);
      const unsigned ka = sb + L_KBUF + rg * 1296 + cp * 4;
      unsigned kr[11], vr[8];
#pragma unroll
      for (int i = 0; i < 11; ++i) kr[i] = lds_ld<unsigned>(ka + i * 160 + (i >> 3) * 16);
#pragma unroll
      for (int i = 0; i < 8; ++i) vr[i] = lds_ld<unsigned>(ka + 11264 + i * 160);
      SCHED_FENCE();
      const f2 w0 = {blo(shw.x), blo(shw.z)}, w1f = {bhi(shw.x), bhi(shw.z)}, w2f = {blo(shw.y), blo(shw.w)}, w3 = {bhi(shw.y), bhi(shw.w)};
      f2 kf[11], o[8];
#pragma unroll
      for (int i = 0; i < 11; ++i) kf[i] = f2{blo(kr[i]), bhi(kr[i])};
#pragma unroll
      for (int i = 0; i < 8; ++i)
        o[i] = __builtin_elementwise_fma(kf[i], w3, __builtin_elementwise_fma(kf[i + 1], w2f, __builtin_elementwise_fma(kf[i + 2], w1f, kf[i + 3] * w0))) * f2{blo(vr[i]), bhi(vr[i])};
#pragma unroll
      for (int c = 0; c < 2; ++c)
        st16_sc1(rws, WS_KV + (dt2 * 80 + cp * 2 + c) * 4096 + (2040 - 64 * st - 8 * rg) * 2,
                 u4{pk(o[7][c], o[6][c]), pk(o[5][c], o[4][c]), pk(o[3][c], o[2][c]), pk(o[1][c], o[0][c])});
    }
    STAMP(4);
  }

  if (wave >= 4) {
    const int hw = wave - 4, pair = hw >> 1, half = hw & 1;
    const bool extra = b < 64 && pair == 0;
    const int nrounds = has1 ? (extra ? 2 : 1) : 3;
    const int ch0 = has1 ? (extra ? 576 + b : 2 * b + pair) : 480 + (b - 240) * 6 + pair, chs = extra ? b - 576 : 2;
    Filt f;
    filt_load(f, ssmk + (size_t)ch0 * 4096, half, lane);
    waitv<4>();
    if (ctr_arrive(ctl + C_H * 4, lane) == 7 && lane < 8) st8_sc1(rws, WS_F1 + lane * 4096 + b * 16, tag);
    STAMP(20 + hw);
    unsigned dd0 = *(const unsigned short*)(ssmd + ch0 * 2);
    __builtin_amdgcn_s_setprio(HPRIO);
    ctr_wait(ctl + C_H * 4, 8);
    const unsigned cv = sb + L_CONV + pair * CV_BYTES;
    filt_build(f, cv, half, lane);
    ctr_wait(ctl + C_G2 * 4, 1);
    u4 xv[2];
#pragma unroll
    for (int j = 0; j < 2; ++j) xv[j] = ld16_sc1(rws, WS_KV + ch0 * 4096 + (lane + 64 * j + 128 * half) * 16);
    ctr_arrive(ctl + C_GO1 * 4, lane);
    if (b == 0 && threadIdx.x == 448) st8_sc1(rws, WS_EPOCH, tag);
    const int c16 = lane & 15, kg = lane >> 4;
    const unsigned pc = ctl + (C_P0 + pair) * 4;
    const unsigned ab = cv + (c16 & 3) * CV_CP + (c16 >> 2) * 8 + kg * 16 + half * 256;
    const unsigned bb = cv + CV_X + 480 - 32 * c16 + 16 * kg + half * 256;
    asm volatile("" : "+v"(xv[0]), "+v"(xv[1]), "+v"(dd0));
#pragma unroll 1
    for (int round = 0; round < nrounds; ++round) {
      const int ch = ch0 + chs * round;
      if (round) filt_build(f, cv, half, lane);
#pragma unroll
      for (int j = 0; j < 2; ++j) lds_st<u4>(cv + CV_X + (lane + 64 * j + 128 * half) * 16, xv[j]);
      const float dd = blo(dd0);
      if (round + 1 < nrounds) {
        filt_load(f, ssmk + (size_t)(ch + chs) * 4096, half, lane);
#pragma unroll
        for (int j = 0; j < 2; ++j) xv[j] = ld16_sc1(rws, WS_KV + (ch + chs) * 4096 + (lane + 64 * j + 128 * half) * 16);
        dd0 = *(const unsigned short*)(ssmd + (ch + chs) * 2);
      }
      ctr_arrive(pc, lane);
      ctr_wait(pc, round * 8 + 2);
      if (has1 && round + 1 == nrounds) ctr_wait(ctl + C_KV * 4, 4);
      f4 acc[8];
      if (round == 0) STAMP(30); else STAMP(31);
      conv_phases(acc, ab, bb);
      if (round == 1) STAMP(36);
      if (round == 0) STAMP(25 + hw);
      ctr_arrive(pc, lane);
      ctr_wait(pc, round * 8 + 4);
#pragma unroll
      for (int n = 0; n < 4; ++n) lds_st<f4>(cv + half * 4096 + n * 1024 + lane * 16, sel4(half, acc[n], acc[n + 4]));
      ctr_arrive(pc, lane);
      ctr_wait(pc, round * 8 + 6);
      u2 y[4];
#pragma unroll
      for (int n = 0; n < 4; ++n) {
        const f4 t = sel4(half, acc[n + 4], acc[n]) + lds_ld<f4>(cv + (half ^ 1) * 4096 + n * 1024 + lane * 16);
        const int t0 = 1024 * half + 256 * n + 16 * c16 + 4 * kg;
        const u2 xq = lds_ld<u2>(cv + CV_X + (2044 - t0) * 2);
        y[n] = u2{pk(__builtin_fmaf(bhi(xq.y), dd, t[0]), __builtin_fmaf(blo(xq.y), dd, t[1])),
                  pk(__builtin_fmaf(bhi(xq.x), dd, t[2]), __builtin_fmaf(blo(xq.x), dd, t[3]))};
      }
      asm volatile("" : "+v"(f.c[0]), "+v"(f.c[1]), "+v"(f.n[0]), "+v"(f.n[1]), "+v"(xv[0]), "+v"(xv[1]), "+v"(dd0));
      ctr_arrive(pc, lane);
#pragma unroll
      for (int n = 0; n < 4; n += 2) {
        const auto sx = __builtin_amdgcn_permlane16_swap(y[n].x, y[n + 1].x, false, false);
        const auto sy = __builtin_amdgcn_permlane16_swap(y[n].y, y[n + 1].y, false, false);
        st16_sc1(rws, WS_YA + ch * 4096 + (1024 * half + 256 * (n + (kg & 1)) + 16 * c16 + 8 * (kg >> 1)) * 2, u4{sx[0], sy[0], sx[1], sy[1]});
      }
      ctr_wait(pc, round * 8 + 8);
    }
    waitv<0>();
    if (ctr_arrive(ctl + (C_Q0 + pair) * 4, lane) == 1 && lane < nrounds) st8_sc1(rws, WS_F3 + (ch0 + chs * lane) * 8, tag);
    STAMP(32 + hw);
    const int s = st * 64 + hw * 16 + c16;
    const size_t xo = (size_t)s * 1280 + (dt2 * 80 + 4 * kg) * 2;
    u2 xq[2] = {*(const u2*)(x + xo + 96), *(const u2*)(x + xo + 128)};
    u2 stt = ld8_sc1(rws, WS_ST + s * 8);
    unsigned b2r = *(const unsigned short*)(b2 + s * 2);
    asm volatile("" : "+v"(xq[0]), "+v"(xq[1]), "+v"(stt), "+v"(b2r));
    if (hw == 3) {
      if (HPOLL2 && has1) {
        const int dta = (dt2 * 5) >> 2;
#pragma unroll 1
        for (unsigned n = 0; n < SPIN_CAP; ++n) {
          const u2 a = ld8_sc1(rws, WS_F2 + xg * 4096 + (dta * 24 + (lane < 48 ? lane : 0)) * 16);
          if (__all(a.x == tag.x && a.y == tag.y)) break;
          asm volatile("" ::: "memory");
        }
        asm volatile("" ::: "memory");
        if (lane == 0) lds_st<unsigned>(ctl + C_GO2 * 4, 1u);
      }
#pragma unroll 1
      for (unsigned n = 0; n < SPIN_CAP; ++n) {
        const u4 a = ld16_sc1(rws, WS_F3 + dt2 * 640 + (lane < 40 ? lane : 0) * 16);
        if (__all(a.x == tag.x && a.y == tag.y && a.z == tag.x && a.w == tag.y)) break;
        __builtin_amdgcn_s_sleep(2);
        asm volatile("" ::: "memory");
      }
      asm volatile("" ::: "memory");
      STAMP(37);
      u4 y[10];
#pragma unroll
      for (int i = 0; i < 10; ++i) y[i] = ld16_sc1(rws, WS_YA + (dt2 * 80 + i * 8 + (lane >> 3)) * 4096 + st * 128 + (lane & 7) * 16);
      SCHED_FENCE();
#pragma unroll
      for (int i = 0; i < 10; ++i) lds_st<u4>(sb + L_YA + i * 1024 + lane * 16, y[i]);
      STAMP(38);
    }
    raw_barrier();
    asm volatile(".Lfk_tail:");
    f4 own[2];
#pragma unroll
    for (int m = 0; m < 2; ++m) {
      const unsigned a = sb + (3 + m) * 1024 + lane * 16;
      own[m] = (lds_ld<f4>(a + hw * L_RING) + lds_ld<f4>(a + ((hw - 1) & 3) * L_RING + 5120)) +
               (lds_ld<f4>(a + ((hw - 2) & 3) * L_RING + 10240) + lds_ld<f4>(a + ((hw - 3) & 3) * L_RING + 15360));
    }
    final_tiles<3, 2>(own, xq, stt, b2r, sb + L_YA + (4 * kg + ((lane >> 2) & 3)) * 128 + hw * 32 + (lane & 3) * 8, out + xo);
    return;
  }

  waitv<0>();
  if (ctr_arrive(ctl + C_H * 4, lane) == 7 && lane < 8) st8_sc1(rws, WS_F1 + lane * 4096 + b * 16, tag);
  STAMP(16 + wave);
  __builtin_amdgcn_s_setprio(GPRIO);
#define POLL_F1()                                                                                                            \
  do {                                                                                                                       \
    if (wave == 0) {                                                                                                         \
      _Pragma("unroll 1") for (unsigned n = 0; n < SPIN_CAP; ++n) {                                                          \
        bool ok = true;                                                                                                      \
        _Pragma("unroll") for (int i = 0; i < 4; ++i) {                                                                      \
          const u2 a = ld8_sc1(rws, WS_F1 + xg * 4096 + i * 1024 + lane * 16);                                               \
          ok = ok && a.x == tag.x && a.y == tag.y;                                                                           \
        }                                                                                                                    \
        if (__all(ok)) break;                                                                                                \
        asm volatile("" ::: "memory");                                                                                       \
      }                                                                                                                      \
      asm volatile("" ::: "memory");                                                                                         \
      if (lane == 0) lds_st<unsigned>(ctl + C_G2 * 4, 1u);                                                                   \
    }                                                                                                                        \
    if (KVFIRST) ctr_wait(ctl + C_GO1 * 4, 4); else ctr_wait(ctl + C_G2 * 4, 1);                                             \
  } while (0)
  unsigned tra[4];
  s4 wf[2][8];

  const int s = st * 64 + wave * 16 + (lane & 15);
  const size_t xo = (size_t)s * 1280 + (dt2 * 80 + 4 * g) * 2;
  u2 xr[3], stt;
  unsigned b2r;
#define EPI_LOADS()                                                                                                          \
  do {                                                                                                                       \
    _Pragma("unroll") for (int m = 0; m < 3; ++m) xr[m] = *(const u2*)(x + xo + m * 32);                                     \
    stt = ld8_sc1(rws, WS_ST + s * 8);                                                                                       \
    b2r = *(const unsigned short*)(b2 + s * 2);                                                                              \
  } while (0)
#define EPI_PIN() asm volatile("" : "+v"(xr[0]), "+v"(xr[1]), "+v"(xr[2]), "+v"(stt), "+v"(b2r))
  if (has1) {
    {
      const int fsw = 2 * (q >> 1) + 4 * (g & 1);
#pragma unroll
      for (int m = 0; m < 4; ++m) tra[m] = ring + (8 * g + q) * 128 + (((2 * m) ^ fsw) + (p >> 1)) * 16 + (p & 1) * 8;
    }
    u2 bias[4];
#pragma unroll
    for (int m = 0; m < 4; ++m) bias[m] = *(const u2*)(b1 + (ft * 64 + 16 * m + 4 * g) * 2);
    unsigned voh[4];
#pragma unroll
    for (int n = 0; n < 4; ++n) voh[n] = WS_H + (g * 640 + dt * 64 + ((n + wave) & 3) * 16 + (lane & 15)) * 16;
    u4 hq[4][4];
    f4 acc[4][4];
#pragma unroll
    for (int m = 0; m < 4; ++m)
#pragma unroll
      for (int n = 0; n < 4; ++n) acc[m][n] = f4{0.f, 0.f, 0.f, 0.f};
#define H_PIECE(i, n) LD_SC1(hq[(i) & 3][n], voh[n], KS1(i) * 40960u, 0)
#define H_LOAD(i) do { H_PIECE(i, 0); H_PIECE(i, 1); H_PIECE(i, 2); H_PIECE(i, 3); } while (0)
#define H_WAIT(N, hs) asm volatile("s_waitcnt vmcnt(%4)" : "+v"(hs[0]), "+v"(hs[1]), "+v"(hs[2]), "+v"(hs[3]) : "i"(N) : "memory")
#define G1_RD(i, k) wf[(i) & 1][k] = tr_read(tra[(k) >> 1] + ((i) % 7) * 4096 + ((k) & 1) * 512)
    s4 res[R1][8];
    ctr_wait(ctl + C_H * 4, 8);
    W1_DMA(0); W1_DMA(1); W1_DMA(2); W1_DMA(3); W1_DMA(4); W1_DMA(5); W1_DMA(6);
#pragma unroll
    for (int i = 0; i < R1; ++i) {
      waitv<24>();
#pragma unroll
      for (int k = 0; k < 8; ++k) res[i][k] = tr_read(tra[k >> 1] + i * 4096 + (k & 1) * 512);
      asm volatile("s_waitcnt lgkmcnt(0)" ::: "memory");
      W1_DMA(7 + i);
    }
    asm volatile("" : "+v"(bias[0]), "+v"(bias[1]), "+v"(bias[2]), "+v"(bias[3]));
    STAMP(5 + wave);
    POLL_F1();
    STAMP(9 + wave);
    H_LOAD(0); H_LOAD(1); if (LA > 2) H_LOAD(2);
#define G1_A(i, k) ((i) < R1 ? cat8(res[(i) < R1 ? (i) : 0][(k) & 6], res[(i) < R1 ? (i) : 0][(k) | 1]) : cat8(wf[(i) & 1][(k) & 6], wf[(i) & 1][(k) | 1]))
#define G1_GROUP(i, k)                                                                                                       \
  do {                                                                                                                       \
    acc[(k) >> 1][((k) & 1) * 2] = mfma16(G1_A(i, k), __builtin_bit_cast(bf8, hq[(i) & 3][((k) & 1) * 2]), acc[(k) >> 1][((k) & 1) * 2]); \
    acc[(k) >> 1][((k) & 1) * 2 + 1] = mfma16(G1_A(i, k), __builtin_bit_cast(bf8, hq[(i) & 3][((k) & 1) * 2 + 1]), acc[(k) >> 1][((k) & 1) * 2 + 1]); \
    SCHED_FENCE();                                                                                                           \
    if ((i) + 1 < 16 && (i) + 1 >= R1) G1_RD((i) + 1, k);                                                                    \
    if ((k) < 4) { if (g1_h(i)) H_PIECE((i) + LA, (k) & 3); }                                                                 \
    else if (g1_x(i) && (i) <= 9) W1_PIECE((i) + 6, (k) & 3);                                                                \
    else if (g1_x(i)) W2_PIECE((i) - 13, (k) & 3);                                                                           \
    SCHED_FENCE();                                                                                                           \
  } while (0)
#define G1_STEP(i)                                                                                                           \
  do {                                                                                                                       \
    H_WAIT(g1_n(i), hq[(i) & 3]);                                                                                            \
    G1_GROUP(i, 0); G1_GROUP(i, 1); G1_GROUP(i, 2); G1_GROUP(i, 3); G1_GROUP(i, 4); G1_GROUP(i, 5); G1_GROUP(i, 6); G1_GROUP(i, 7); \
  } while (0)
    G1_STEP(0); G1_STEP(1); G1_STEP(2); G1_STEP(3); G1_STEP(4); G1_STEP(5); G1_STEP(6); G1_STEP(7); G1_STEP(8); G1_STEP(9);
#pragma unroll
    for (int t = 0; t < 4; ++t) vow[t] = (8 * t + (lane >> 3)) * 4096 + st * 128 + (((lane & 7) ^ ((lane >> 3) & 6)) * 16) - 1024 * t;
    G1_STEP(10); G1_STEP(11); G1_STEP(12); G1_STEP(13); G1_STEP(14); G1_STEP(15);
    STAMP(40 + wave);
    EPI_LOADS();
#pragma unroll
    for (int n = 1; n < 4; ++n)
#pragma unroll
      for (int m = 0; m < 4; ++m) lds_st<f4>(ring + (n == 1 ? 6 : n - 2) * 4096 + m * 1024 + lane * 16, acc[m][n]);
    ctr_arrive(ctl + C_G1 * 4, lane);
    STAMP(14);
    ctr_wait(ctl + C_G1 * 4, 4);
    STAMP(29);
#pragma unroll
    for (int n = 1; n < 4; ++n) {
      const unsigned a = sb + ((wave - n) & 3) * L_RING + (n == 1 ? 6 : n - 2) * 4096 + lane * 16;
#pragma unroll
      for (int m = 0; m < 4; ++m) acc[m][n] = lds_ld<f4>(a + m * 1024);
    }
    SCHED_FENCE();
#pragma unroll
    for (int m = 0; m < 4; ++m) acc[m][0] = (acc[m][0] + acc[m][1]) + (acc[m][2] + acc[m][3]);
    unsigned o[8];
#pragma unroll
    for (int m = 0; m < 4; ++m) {
      o[2 * m] = pk(gelu_t(acc[m][0][0] + blo(bias[m].x)), gelu_t(acc[m][0][1] + bhi(bias[m].x)));
      o[2 * m + 1] = pk(gelu_t(acc[m][0][2] + blo(bias[m].y)), gelu_t(acc[m][0][3] + bhi(bias[m].y)));
    }
#pragma unroll
    for (int P = 0; P < 2; ++P)
      st16_sc1(rws, WS_U + ((ft * 2 + P) * 40 + dt * 4 + wave) * 1024 + lane * 16, u4{o[4 * P], o[4 * P + 1], o[4 * P + 2], o[4 * P + 3]});
    ctr_arrive(ctl + C_KV * 4, lane);
    STAMP(13);
    EPI_PIN();
    W2_DMA(2);
    waitv<4>();
    if (ctr_arrive(ctl + C_G3 * 4, lane) == 3 && lane < 8) st8_sc1(rws, WS_F2 + lane * 4096 + (dt * 24 + ft) * 16, tag);
    STAMP(44 + wave);
    ctr_wait(ctl + C_G3 * 4, 4);
    W2_DMA(3); W2_DMA(4); W2_DMA(5); W2_DMA(6);
  } else {
#pragma unroll
    for (int t = 0; t < 4; ++t) vow[t] = (8 * t + (lane >> 3)) * 4096 + st * 128 + (((lane & 7) ^ ((lane >> 3) & 6)) * 16) - 1024 * t;
    POLL_F1();
    EPI_LOADS();
    EPI_PIN();
    W2_DMA(0); W2_DMA(1); W2_DMA(2); W2_DMA(3); W2_DMA(4); W2_DMA(5); W2_DMA(6);
  }

  asm volatile("" : "+s"(ring), "+s"(rot2));
  {
    const int fsw = 4 * (g & 1) + (q & 2);
#pragma unroll
    for (int n = 0; n < 4; ++n) tra[n] = ring + (4 * g + q) * 128 + (((2 * ((n + wave) & 3)) ^ fsw) + (p >> 1)) * 16 + (p & 1) * 8;
  }
  const unsigned vou0 = WS_U + dt2 * 5120 + lane * 16, vou1 = vou0 + 4096;
  u4 uq[4][5];
  f4 acc[5][4];
#pragma unroll
  for (int m = 0; m < 5; ++m)
#pragma unroll
    for (int n = 0; n < 4; ++n) acc[m][n] = f4{0.f, 0.f, 0.f, 0.f};
#define U_PIECE(j, m) LD_SC1(uq[(j) & 3][m], (m) < 4 ? vou0 : vou1, KS2(j) * 40960u, ((m) & 3) * 1024)
#define U_LOAD(j) do { U_PIECE(j, 0); U_PIECE(j, 1); U_PIECE(j, 2); U_PIECE(j, 3); U_PIECE(j, 4); } while (0)
#define U_WAIT(N, us) asm volatile("s_waitcnt vmcnt(%5)" : "+v"(us[0]), "+v"(us[1]), "+v"(us[2]), "+v"(us[3]), "+v"(us[4]) : "i"(N) : "memory")
#define G2_RD(j, k) wf[(j) & 1][k] = tr_read(tra[(k) >> 1] + (((j) + 2) % 7) * 4096 + ((k) & 1) * 2048)
#define G2_B(j, n) ((j) < R2 ? cat8(res2[(j) < R2 ? (j) : 0][2 * (n)], res2[(j) < R2 ? (j) : 0][2 * (n) + 1]) : cat8(wf[(j) & 1][2 * (n)], wf[(j) & 1][2 * (n) + 1]))
#define G2_MM(j, m, n) acc[m][n] = mfma16(__builtin_bit_cast(bf8, uq[(j) & 3][m]), G2_B(j, n), acc[m][n])
#define G2_GROUP(j, k)                                                                                                       \
  do {                                                                                                                       \
    G2_MM(j, (2 * (k)) % 5, (2 * (k)) / 5); G2_MM(j, (2 * (k) + 1) % 5, (2 * (k) + 1) / 5);                                  \
    SCHED_FENCE();                                                                                                           \
    if ((k) < 8 && (j) + 1 < 12 && (j) + 1 >= R2) G2_RD((j) + 1, (k) & 7);                                                   \
    if ((k) < 5) { if (g2_u(j)) U_PIECE((j) + LA, (k) % 5); }                                                                 \
    else if ((k) < 9 && g2_x(j)) W2_PIECE((j) + 6, ((k) - 5) & 3);                                                           \
    SCHED_FENCE();                                                                                                           \
  } while (0)
#define G2_STEP(j)                                                                                                           \
  do {                                                                                                                       \
    U_WAIT(g2_n(j), uq[(j) & 3]);                                                                                                  \
    G2_GROUP(j, 0); G2_GROUP(j, 1); G2_GROUP(j, 2); G2_GROUP(j, 3); G2_GROUP(j, 4);                                          \
    G2_GROUP(j, 5); G2_GROUP(j, 6); G2_GROUP(j, 7); G2_GROUP(j, 8); G2_GROUP(j, 9);                                          \
  } while (0)
  s4 res2[R2][8];
  static_assert(R2 <= 3, "wait below");
  waitv<16>();
#pragma unroll
  for (int j = 0; j < R2; ++j) {
#pragma unroll
    for (int k = 0; k < 8; ++k) res2[j][k] = tr_read(tra[k >> 1] + ((j + 2) % 7) * 4096 + (k & 1) * 2048);
    asm volatile("s_waitcnt lgkmcnt(0)" ::: "memory");
    W2_DMA(7 + j);
  }
  STAMP(39);
  if ((!HPOLL2 || !has1) && wave == 0) {
    const int dta = (dt2 * 5) >> 2;
#pragma unroll 1
    for (unsigned n = 0; n < SPIN_CAP; ++n) {
      const u2 a = ld8_sc1(rws, WS_F2 + xg * 4096 + (dta * 24 + (lane < 48 ? lane : 0)) * 16);
      if (__all(a.x == tag.x && a.y == tag.y)) break;
      asm volatile("" ::: "memory");
    }
    asm volatile("" ::: "memory");
    if (lane == 0) lds_st<unsigned>(ctl + C_GO2 * 4, 1u);
  } else {
    ctr_wait(ctl + C_GO2 * 4, 1);
  }
  STAMP(48 + wave);
  U_LOAD(0); U_LOAD(1); if (LA > 2) U_LOAD(2);
  G2_STEP(0); G2_STEP(1); G2_STEP(2); G2_STEP(3); G2_STEP(4); G2_STEP(5); G2_STEP(6); G2_STEP(7); G2_STEP(8); G2_STEP(9); G2_STEP(10); G2_STEP(11);
  STAMP(52 + wave);
#pragma unroll
  for (int n = 0; n < 4; ++n)
#pragma unroll
    for (int m = (n == 0 ? 3 : 0); m < 5; ++m) lds_st<f4>(ring + (n * 5 + m) * 1024 + lane * 16, acc[m][n]);
  f4 own[3] = {acc[0][0], acc[1][0], acc[2][0]};
  raw_barrier();
  STAMP(56 + wave);
#pragma unroll
  for (int w = 1; w < 4; ++w) {
    const unsigned a = sb + ((wave - w) & 3) * L_RING + w * 5120 + lane * 16;
#pragma unroll
    for (int m = 0; m < 3; ++m) own[m] += lds_ld<f4>(a + m * 1024);
  }
  final_tiles<0, 3>(own, xr, stt, b2r, sb + L_YA + (4 * g + q) * 128 + wave * 32 + p * 8, out + xo);
  STAMP(60 + wave);
}

extern "C" void kernel_launch(void* const* d_in, const int* in_sizes, int n_in, void* d_out, int out_size, void* d_ws, size_t ws_size,
                              hipStream_t stream) {
  (void)hipFuncSetAttribute((const void*)fused_kernel, hipFuncAttributeMaxDynamicSharedMemorySize, L_TOTAL);
  fused_kernel<<<256, 512, L_TOTAL, stream>>>((const char*)d_in[0], (const char*)d_in[1], (const char*)d_in[2], (const char*)d_in[3],
                                              (const char*)d_in[4], (const char*)d_in[5], (const char*)d_in[7], (const char*)d_in[8],
                                              (const char*)d_in[9], (const char*)d_in[10], (char*)d_out, (char*)d_ws);
}
```

```cpp
#include <hip/hip_runtime.h>
#include <hip/hip_bf16.h>
#include <stdint.h>

typedef __attribute__((ext_vector_type(8))) __bf16 bf8;
typedef __attribute__((ext_vector_type(2))) __bf16 bf2;
typedef __attribute__((ext_vector_type(4))) short s4;
typedef __attribute__((ext_vector_type(4))) float f4;
typedef __attribute__((ext_vector_type(2))) float f2;
typedef __attribute__((ext_vector_type(4))) unsigned u4;
typedef __attribute__((ext_vector_type(2))) unsigned u2;
#define LAS __attribute__((address_space(3)))
#define DEVI __device__ __forceinline__
#define SCHED_FENCE() __builtin_amdgcn_sched_barrier(0)

#define PF_W2 0
#define CONVQ 4
#define HPOL " sc1"
#define MAP1 0
#define R1 5
#define R2 3
#define THR 5
#define GPRIO 0
#define HPRIO 0
#define KVFIRST 1
#define LA 3
#define CODEPF 1
#define R2WAIT 1
#define HPOLL2 0
#define TLBPF 1
#define OUTPOL ""
#define NT_FIRST 0

constexpr unsigned WS_EPOCH = 0;
constexpr unsigned WS_F1 = 4096;
constexpr unsigned WS_F2 = WS_F1 + 32768;
constexpr unsigned WS_F4 = WS_F2 + 32768;
constexpr unsigned WS_F3 = WS_F4 + 32768;
constexpr unsigned WS_ST = WS_F3 + 8192;
constexpr unsigned WS_H = WS_ST + 16384;
constexpr unsigned WS_KV = WS_H + 2621440;
constexpr unsigned WS_YA = WS_KV + 2621440;
constexpr unsigned WS_U = WS_YA + 2621440;
constexpr unsigned WS_END = WS_U + 1966080;
constexpr size_t WS_DBG = WS_END;

#define STAMP(idx) do {} while (0)

constexpr unsigned L_RING = 28672;
constexpr unsigned CV_CP = 4160;
constexpr unsigned CV_X = 4 * CV_CP;
constexpr unsigned CV_BYTES = 21248;
constexpr unsigned L_CONV = 4 * L_RING;
constexpr unsigned L_HBUF = L_CONV;
constexpr unsigned L_KBUF = L_CONV + 10752;
constexpr unsigned L_VBUF = L_KBUF + 11264;
constexpr unsigned L_YA = L_CONV + CV_BYTES;
constexpr unsigned L_CTL = L_CONV + 2 * CV_BYTES;
constexpr unsigned L_TOTAL = L_CTL + 128;
enum { C_H = 0, C_GO1, C_G1, C_G2, C_G3, C_GO2, C_P0, C_P1, C_Q0, C_Q1, C_KV, C_K2 };
constexpr unsigned SPIN_CAP = 1u << 16;

DEVI float blo(unsigned u) { return __uint_as_float(u << 16); }
DEVI float bhi(unsigned u) { return __uint_as_float(u & 0xffff0000u); }
DEVI unsigned pk(float a, float b) { f2 v = {a, b}; return __builtin_bit_cast(unsigned, __builtin_convertvector(v, bf2)); }
DEVI float rbf(float a) { return blo(pk(a, a)); }
template <int CTRL, int RM> DEVI float dppf(float x) {
  return __builtin_bit_cast(float, __builtin_amdgcn_update_dpp(0, __builtin_bit_cast(int, x), CTRL, RM, 0xf, true));
}
template <class T> DEVI T ldnt(const void* p) { return __builtin_nontemporal_load((const T*)p); }
DEVI u4 make_srd(const void* p, unsigned bytes) {
  uint64_t a = (uint64_t)p;
  u4 r = {(unsigned)a, (unsigned)(a >> 32) & 0xffffu, bytes, 0x00020000u};
  return r;
}
DEVI s4 tr_read(unsigned addr) { return __builtin_amdgcn_ds_read_tr16_b64_v4i16((LAS s4*)(size_t)addr); }
DEVI bf8 cat8(s4 a, s4 b) {
  typedef __attribute__((ext_vector_type(8))) short s8;
  s8 v = __builtin_shufflevector(a, b, 0, 1, 2, 3, 4, 5, 6, 7);
  return __builtin_bit_cast(bf8, v);
}
DEVI f4 mfma16(bf8 a, bf8 b, f4 c) { return __builtin_amdgcn_mfma_f32_16x16x32_bf16(a, b, c, 0, 0, 0); }
template <int N> DEVI void waitv() { asm volatile("s_waitcnt vmcnt(%0)" ::"i"(N) : "memory"); }
template <class T> DEVI T lds_ld(unsigned a) { return *(const LAS T*)(size_t)a; }
template <class T> DEVI void lds_st(unsigned a, T v) { *(LAS T*)(size_t)a = v; }
DEVI void raw_barrier() { asm volatile("s_waitcnt lgkmcnt(0)\n\ts_barrier" ::: "memory"); }
DEVI unsigned ctr_arrive(unsigned a, int lane) {
  asm volatile("s_waitcnt lgkmcnt(0)" ::: "memory");
  unsigned old = 0;
  if (lane == 0) old = __hip_atomic_fetch_add((LAS unsigned*)(size_t)a, 1u, __ATOMIC_RELAXED, __HIP_MEMORY_SCOPE_WORKGROUP);
  old = __builtin_amdgcn_readfirstlane(old);
  asm volatile("" ::: "memory");
  return old;
}
DEVI void ctr_wait(unsigned a, unsigned n) {
  while (__builtin_amdgcn_readfirstlane(__hip_atomic_load((LAS unsigned*)(size_t)a, __ATOMIC_RELAXED, __HIP_MEMORY_SCOPE_WORKGROUP)) < n)
    __builtin_amdgcn_s_sleep(1);
  asm volatile("" ::: "memory");
}
typedef __amdgpu_buffer_rsrc_t rsrc_t;
DEVI u4 ld16_sc1(rsrc_t r, unsigned off) { return __builtin_amdgcn_raw_buffer_load_b128(r, off, 0, 16); }
DEVI u2 ld8_sc1(rsrc_t r, unsigned off) { return __builtin_amdgcn_raw_buffer_load_b64(r, off, 0, 16); }
DEVI void st16_sc1(rsrc_t r, unsigned off, u4 v) { __builtin_amdgcn_raw_buffer_store_b128(v, r, off, 0, 16); }
DEVI void st8_sc1(rsrc_t r, unsigned off, u2 v) { __builtin_amdgcn_raw_buffer_store_b64(v, r, off, 0, 16); }

DEVI f4 sel4(bool c, f4 a, f4 b) {
  asm("" : "+v"(a)); asm("" : "+v"(b));
  return c ? a : b;
}
DEVI float gelu_t(float x) {
  const float t = x * __builtin_fmaf(x * x, -0.10294324f, -2.3022082f);
  return x * __builtin_amdgcn_rcpf(1.f + __builtin_amdgcn_exp2f(t));
}

DEVI void ln_row(const unsigned (&xr)[5], unsigned (&hp)[5], float& m1, float& inv1) {
  float e[10], s = 0.f, q = 0.f;
  const float piv = blo(__builtin_amdgcn_readfirstlane(xr[0]));
#pragma unroll
  for (int j = 0; j < 5; ++j) { e[2 * j] = blo(xr[j]); e[2 * j + 1] = bhi(xr[j]); }
#pragma unroll
  for (int j = 0; j < 10; ++j) { const float d = e[j] - piv; s += d; q = __builtin_fmaf(d, d, q); }
  s += dppf<0xB1, 0xf>(s);  q += dppf<0xB1, 0xf>(q);
  s += dppf<0x4E, 0xf>(s);  q += dppf<0x4E, 0xf>(q);
  s += dppf<0x141, 0xf>(s); q += dppf<0x141, 0xf>(q);
  s += dppf<0x140, 0xf>(s); q += dppf<0x140, 0xf>(q);
  s += dppf<0x142, 0xa>(s); q += dppf<0x142, 0xa>(q);
  s += dppf<0x143, 0xc>(s); q += dppf<0x143, 0xc>(q);
  const float md = __builtin_bit_cast(float, __builtin_amdgcn_readlane(__builtin_bit_cast(int, s), 63)) * (1.f / 640.f);
  const float qq = __builtin_bit_cast(float, __builtin_amdgcn_readlane(__builtin_bit_cast(int, q), 63)) * (1.f / 640.f);
  const float var = __builtin_fmaxf(qq - md * md, 0.f), mean = piv + md;
  m1 = rbf(mean);
  inv1 = __builtin_amdgcn_rcpf(rbf(__builtin_amdgcn_sqrtf(rbf(rbf(var) + 1e-5f))));
  const float m2 = rbf((mean - m1) * inv1);
  const float inv2 = __builtin_amdgcn_rcpf(rbf(__builtin_amdgcn_sqrtf(rbf(rbf(var * inv1 * inv1) + 1e-5f))));
  const float A = inv1 * inv2, B = -m2 * inv2;
#pragma unroll
  for (int j = 0; j < 5; ++j) hp[j] = pk(__builtin_fmaf(e[2 * j] - m1, A, B), __builtin_fmaf(e[2 * j + 1] - m1, A, B));
}

struct Filt { u4 c[2]; u2 n[2]; };
DEVI void filt_load(Filt& f, const char* krow, int half, int lane) {
#pragma unroll
  for (int j = 0; j < 2; ++j) {
    const int z = lane + 64 * j + 128 * half;
    f.c[j] = ldnt<u4>(krow + z * 16);
    f.n[j] = ldnt<u2>(krow + (z < 255 ? z * 16 + 16 : 0));
  }
}
DEVI void filt_build(Filt& f, unsigned cv, int half, int lane) {
  const u4 zero = {0u, 0u, 0u, 0u};
  if (half == 1) {
    if (lane == 63) f.n[1] = u2{0u, 0u};
    if (lane < 30) lds_st<u4>(cv + CV_X + 4096 + lane * 16, zero);
  } else {
    if (lane < 4) lds_st<u4>(cv + lane * CV_CP, zero);
    if (lane == 0) {
      const unsigned d4 = f.c[0].x, d5 = f.c[0].y;
      lds_st<u4>(cv + 16, u4{0u, 0u, 0u, d4 << 16});
      lds_st<u4>(cv + CV_CP + 16, u4{0u, 0u, 0u, d4});
      lds_st<u4>(cv + 2 * CV_CP + 16, u4{0u, 0u, d4 << 16, __builtin_amdgcn_alignbit(d5, d4, 16)});
      lds_st<u4>(cv + 3 * CV_CP + 16, u4{0u, 0u, d4, d5});
    }
  }
#pragma unroll
  for (int j = 0; j < 2; ++j) {
    const u4 c = f.c[j]; const u2 n = f.n[j];
    const unsigned a0 = __builtin_amdgcn_alignbit(c.y, c.x, 16), a1 = __builtin_amdgcn_alignbit(c.z, c.y, 16), a2 = __builtin_amdgcn_alignbit(c.w, c.z, 16),
                   a3 = __builtin_amdgcn_alignbit(n.x, c.w, 16), a4 = __builtin_amdgcn_alignbit(n.y, n.x, 16);
    const unsigned p = cv + (lane + 64 * j + 128 * half + 2) * 16;
    lds_st<u4>(p, u4{a0, a1, a2, a3});
    lds_st<u4>(p + CV_CP, u4{c.y, c.z, c.w, n.x});
    lds_st<u4>(p + 2 * CV_CP, u4{a1, a2, a3, a4});
    lds_st<u4>(p + 3 * CV_CP, u4{c.z, c.w, n.x, n.y});
  }
}

DEVI void conv_phases(f4 (&acc)[8], unsigned ab, unsigned bb) {
  bf8 A0[8], B0[8], A1[8], B1[8];
#pragma unroll
  for (int n = 0; n < 8; ++n) acc[n] = f4{0.f, 0.f, 0.f, 0.f};
#define CV_LOAD(A, B, q)                                                                              \
  _Pragma("unroll") for (int p = 0; p < 8; ++p) {                                                      \
    B[p] = *(const volatile LAS bf8*)(size_t)(bb + 3584 - 512 * p + 64 * (q));                          \
    s4 lo = *(const volatile LAS s4*)(size_t)(ab + 512 * p + 64 * (q));                                 \
    s4 hi = *(const volatile LAS s4*)(size_t)(ab + 512 * p + 64 * (q) + 8);                             \
    A[p] = cat8(lo, hi);                                                                               \
  }
#define CV_MMA(A, B)                                                                                   \
  _Pragma("unroll") for (int p = 0; p < 8; ++p)                                                        \
  _Pragma("unroll") for (int m = 0; m + p < 8; ++m) acc[p + m] = mfma16(A[p], B[m], acc[p + m]);
#define CV_SCHED()                                                                                     \
  _Pragma("unroll") for (int z = 0; z < 24; ++z) {                                                     \
    __builtin_amdgcn_sched_group_barrier(0x8, 1, 0);                                                   \
    __builtin_amdgcn_sched_group_barrier(0x100, 1, 0);                                                 \
  }                                                                                                    \
  __builtin_amdgcn_sched_group_barrier(0x8, 12, 0);                                                    \
  SCHED_FENCE();
  CV_LOAD(A0, B0, 0);
  SCHED_FENCE();
#pragma unroll 1
  for (int q = 0; q < CONVQ; q += 2) {
    CV_LOAD(A1, B1, q + 1); CV_MMA(A0, B0); CV_SCHED();
    CV_LOAD(A0, B0, (q + 2) & 3); CV_MMA(A1, B1); CV_SCHED();
  }
}

constexpr int g1_h(int i) { return i < 0 ? 4 : i + LA < 16 ? 4 : 0; }
constexpr int g1_x(int i) { return ((i > R1 && i <= 9) || i == 13 || i == 14) ? 4 : 0; }
constexpr int g1_n(int i) { int n = g1_x(i - LA); for (int t = i - LA + 1; t < i; ++t) n += g1_h(t) + g1_x(t); return n; }
constexpr int g2_u(int j) { return j < 0 ? 5 : j + LA < 12 ? 5 : 0; }
constexpr int g2_x(int j) { return (j > R2 && j <= 5) ? 4 : 0; }
constexpr int g2_n(int j) { int n = g2_x(j - LA); for (int t = j - LA + 1; t < j; ++t) n += g2_u(t) + g2_x(t); return n; }

template <int M0, int NM>
DEVI void final_tiles(f4 (&own)[NM], const u2 (&xr)[NM], u2 stt, unsigned b2r, unsigned yab, char* op) {
  const float bb = blo(b2r), m1 = __uint_as_float(stt.x), inv1 = __uint_as_float(stt.y);
#pragma unroll
  for (int m = 0; m < NM; ++m) {
    const u2 ya = __builtin_bit_cast(u2, tr_read(yab + (M0 + m) * 2048));
    const float x0 = blo(xr[m].x), x1 = bhi(xr[m].x), x2 = blo(xr[m].y), x3 = bhi(xr[m].y);
    const float r0 = __builtin_fmaf((x0 - m1) * inv1, blo(ya.x), x0) + (own[m][0] + bb), r1 = __builtin_fmaf((x1 - m1) * inv1, bhi(ya.x), x1) + (own[m][1] + bb);
    const float r2 = __builtin_fmaf((x2 - m1) * inv1, blo(ya.y), x2) + (own[m][2] + bb), r3 = __builtin_fmaf((x3 - m1) * inv1, bhi(ya.y), x3) + (own[m][3] + bb);
    const u2 ov = {pk(r0, r1), pk(r2, r3)};
    asm volatile("global_store_dwordx2 %0, %1, off offset:%2" OUTPOL ::"v"(op), "v"(ov), "i"((M0 + m) * 32) : "memory");
  }
}

__global__ __launch_bounds__(512, 2) __attribute__((amdgpu_waves_per_eu(2, 2))) void fused_kernel(
    const char* __restrict__ x, const char* __restrict__ kk, const char* __restrict__ vv, const char* __restrict__ shk,
    const char* __restrict__ ssmk, const char* __restrict__ ssmd, const char* __restrict__ w1, const char* __restrict__ b1,
    const char* __restrict__ w2, const char* __restrict__ b2, char* __restrict__ out, char* ws) {
  extern __shared__ __attribute__((aligned(16))) char smem[];
  const int lane = threadIdx.x & 63, wave = __builtin_amdgcn_readfirstlane(threadIdx.x >> 6);
  const int b = blockIdx.x, xg = b & 7, r = b >> 3;
  const unsigned sb = __builtin_amdgcn_readfirstlane((unsigned)(size_t)smem);
  const unsigned ctl = sb + L_CTL;
  asm volatile("" ::"s"(x), "s"(kk), "s"(vv), "s"(shk), "s"(ssmk), "s"(ssmd), "s"(w1), "s"(b1), "s"(w2), "s"(b2), "s"(out), "s"(ws));
  const rsrc_t rws = __builtin_amdgcn_make_buffer_rsrc(ws, 0, WS_END, 0x00020000);
  if (wave == 0) STAMP(0);
  const int dt2 = r & 7, st = xg * 4 + (r >> 3);

  const u4 srdS = make_srd(ws, WS_END);
  const int g = lane >> 4, q = (lane >> 2) & 3, p = lane & 3;
  unsigned ring = sb + wave * L_RING;
  const bool has1 = r < 30;
  const int dt = MAP1 ? (xg & 1) * 5 + r % 5 : r % 10, ft = MAP1 ? (xg >> 1) * 6 + r / 5 : xg * 3 + r / 10;
  const int rot1 = dt * 16 / 10;
  int rot2 = dt2 * 12 / 8;
  const u4 srdW1 = make_srd(w1, 6291456), srdW2 = make_srd(w2, 6291456);
  unsigned vow[4];
#define DMA1(POL, slot, t, srd, soff)                                                                                          \
  asm volatile("s_mov_b32 m0, %0\n\ts_nop 0\n\tbuffer_load_dwordx4 %1, %2, %3 offen offset:%4" POL " lds" ::"s"(ring + (slot) * 4096), \
               "v"(vow[t]), "s"(srd), "s"(soff), "i"((t) * 1024)                                                               \
               : "memory")
#define KS1(i) (4u * (((i) + rot1) & 15) + wave)
#define KS2(j) (4u * ((j) + rot2 >= 12 ? (j) + rot2 - 12 : (j) + rot2) + wave)
#define W1_PIECE(i, t) do { if ((i) < NT_FIRST) DMA1(" nt", (i) % 7, t, srdW1, KS1(i) * 98304u); else DMA1("", (i) % 7, t, srdW1, KS1(i) * 98304u); } while (0)
#define W2_PIECE(j, t) do { if ((j) < NT_FIRST) DMA1(" nt", ((j) + 2) % 7, t, srdW2, KS2(j) * 131072u); else DMA1("", ((j) + 2) % 7, t, srdW2, KS2(j) * 131072u); } while (0)
#define W1_DMA(i) do { W1_PIECE(i, 0); W1_PIECE(i, 1); W1_PIECE(i, 2); W1_PIECE(i, 3); } while (0)
#define W2_DMA(j) do { W2_PIECE(j, 0); W2_PIECE(j, 1); W2_PIECE(j, 2); W2_PIECE(j, 3); } while (0)
#define W1_THR(i) do { W1_PIECE(i, 0); waitv<THR>(); W1_PIECE(i, 1); waitv<THR>(); W1_PIECE(i, 2); waitv<THR>(); W1_PIECE(i, 3); waitv<THR>(); } while (0)
#define W2_THR(j) do { W2_PIECE(j, 0); waitv<THR>(); W2_PIECE(j, 1); waitv<THR>(); W2_PIECE(j, 2); waitv<THR>(); W2_PIECE(j, 3); waitv<THR>(); } while (0)
#define LD_SC1(dst, voff, soff, imm) \
  asm volatile("buffer_load_dwordx4 %0, %1, %2, %3 offen offset:%4" HPOL : "=&v"(dst) : "v"(voff), "s"(srdS), "s"(soff), "i"(imm) : "memory")
#pragma unroll
  for (int t = 0; t < 4; ++t) vow[t] = (8 * t + (lane >> 3)) * 3072 + ft * 128 + (((lane & 7) ^ (2 * ((lane >> 4) & 1) + 4 * (t & 1))) * 16) - 1024 * t;
  u2 tag;
  {
    unsigned xr[5];
    asm volatile("buffer_load_dword %0, %5, %6, %7 offen nt\n\tbuffer_load_dword %1, %5, %6, %7 offen offset:256 nt\n\t"
                 "buffer_load_dword %2, %5, %6, %7 offen offset:512 nt\n\tbuffer_load_dword %3, %5, %6, %7 offen offset:768 nt\n\t"
                 "buffer_load_dword %4, %5, %6, %7 offen offset:1024 nt"
                 : "=&v"(xr[0]), "=&v"(xr[1]), "=&v"(xr[2]), "=&v"(xr[3]), "=&v"(xr[4])
                 : "v"(lane * 4), "s"(make_srd(x, 2621440)), "s"((b * 8 + wave) * 1280)
                 : "memory");
    {
      const u4 srdK = make_srd(kk, 2621440), srdV = make_srd(vv, 2621440);
#pragma unroll
      for (int i = 0; i < 3; ++i) {
        const int pc = wave + 8 * i;
        const bool isv = pc >= 11;
        const int n = (isv ? pc - 11 : pc) * 64 + lane, g8 = (n * 6473) >> 19, rem = n - g8 * 81, rr = (rem * 205) >> 11;
        const int row = g8 * 8 + rr, c = rem - rr * 10;
        const int grow = st * 64 + row - (isv ? 0 : 3);
        const bool ok = rem < 80 && grow >= 0 && row < (isv ? 64 : 67);
        const unsigned vo = ok ? grow * 1280 + dt2 * 160 + c * 16 : 0x7fffff00u;
        const unsigned dst = sb + L_KBUF + pc * 1024;
        if (isv) asm volatile("s_mov_b32 m0, %0\n\ts_nop 0\n\tbuffer_load_dwordx4 %1, %2, 0 offen nt lds" ::"s"(dst), "v"(vo), "s"(srdV) : "memory");
        else     asm volatile("s_mov_b32 m0, %0\n\ts_nop 0\n\tbuffer_load_dwordx4 %1, %2, 0 offen nt lds" ::"s"(dst), "v"(vo), "s"(srdK) : "memory");
      }
    }
    u2 ep; u4 shw;
    asm volatile("buffer_load_dwordx2 %0, off, %2, 0 sc1\n\tbuffer_load_dwordx4 %1, %3, %4, 0 offen"
                 : "=&v"(ep), "=&v"(shw)
                 : "s"(srdS), "v"((dt2 * 80 + (wave >= 3 ? wave - 3 : 0) * 16 + (lane >> 3) * 2) * 8), "s"(make_srd(shk, 5120))
                 : "memory");
    static_assert(WS_EPOCH == 0, "epoch load above");
    unsigned cpf = 0;
    if (CODEPF && wave == 7) {
      uint64_t pc; unsigned len;
      asm volatile("s_getpc_b64 %0\n.Lfk_here:\n\ts_mov_b32 %1, .Lfk_tail-.Lfk_here" : "=s"(pc), "=s"(len));
      const unsigned o = (r * 16 + (lane & 15)) * 128;
      asm volatile("global_load_dword %0, %1, %2" : "=v"(cpf) : "v"(o < len ? o : 0u), "s"(pc) : "memory");
    }
    if (TLBPF && wave == 6) {
      const int j = r * 16 + (lane & 15);
      const char* p = j < 96 ? w1 + j * 65536 : j < 192 ? w2 + (j - 96) * 65536 : j < 232 ? ssmk + (j - 192) * 65536
                    : j < 272 ? out + (j - 232) * 65536 : j < 424 ? ws + (j - 272) * 65536 : x;
      asm volatile("global_load_dword %0, %1, off sc1" : "=v"(cpf) : "v"(p) : "memory");
    }
    if (wave == 0 && lane < 16) lds_st<unsigned>(ctl + lane * 4, 0u);
    asm volatile("s_waitcnt vmcnt(5)" : "+v"(xr[0]), "+v"(xr[1]), "+v"(xr[2]), "+v"(xr[3]), "+v"(xr[4])::"memory");
    STAMP(1);
    unsigned hp[5]; float m1, inv1;
    ln_row(xr, hp, m1, inv1);
#pragma unroll
    for (int j = 0; j < 5; ++j) lds_st<unsigned>(sb + L_HBUF + wave * 1344 + (lane + 64 * j) * 4, hp[j]);
    if (lane == 0) lds_st<u2>(ctl + 64 + wave * 8, u2{__float_as_uint(m1), __float_as_uint(inv1)});
    STAMP(2);
    asm volatile("s_waitcnt vmcnt(0)" : "+v"(ep), "+v"(shw), "+v"(cpf)::"memory");
    const uint64_t tg = (((uint64_t)ep.y << 32) | ep.x) * 6364136223846793005ull + 1442695040888963407ull;
    tag = u2{(unsigned)__builtin_amdgcn_readfirstlane((unsigned)tg), (unsigned)__builtin_amdgcn_readfirstlane((unsigned)(tg >> 32))};
    STAMP(3);
    raw_barrier();
    if (wave < 3) {
      const unsigned tb = sb + L_HBUF + ((lane >> 2) & 3) * 1344 + (lane >> 4) * 32 + (lane & 3) * 8;
      u4 v[4];
#pragma unroll
      for (int i = 0; i < 4; ++i) { const int it = i < 3 ? wave + 3 * i : 9; v[i] = __builtin_bit_cast(u4, cat8(tr_read(tb + it * 128), tr_read(tb + it * 128 + 4 * 1344))); }
      const u4 sv = lds_ld<u4>(ctl + 64 + (lane & 3) * 16);
#pragma unroll
      for (int i = 0; i < 4; ++i)
        if (i < 3 || wave == 0) st16_sc1(rws, WS_H + (b * 640 + (i < 3 ? wave + 3 * i : 9) * 64 + lane) * 16, v[i]);
      if (wave == 1 && lane < 4) st16_sc1(rws, WS_ST + b * 64 + lane * 16, sv);
    } else {
      const int rg = lane & 7, cp = (wave - 3) * 8 + (lane >> 3);
      const unsigned ka = sb + L_KBUF + rg * 1296 + cp * 4;
      unsigned kr[11], vr[8];
#pragma unroll
      for (int i = 0; i < 11; ++i) kr[i] = lds_ld<unsigned>(ka + i * 160 + (i >> 3) * 16);
#pragma unroll
      for (int i = 0; i < 8; ++i) vr[i] = lds_ld<unsigned>(ka + 11264 + i * 160);
      SCHED_FENCE();
      const f2 w0 = {blo(shw.x), blo(shw.z)}, w1f = {bhi(shw.x), bhi(shw.z)}, w2f = {blo(shw.y), blo(shw.w)}, w3 = {bhi(shw.y), bhi(shw.w)};
      f2 kf[11], o[8];
#pragma unroll
      for (int i = 0; i < 11; ++i) kf[i] = f2{blo(kr[i]), bhi(kr[i])};
#pragma unroll
      for (int i = 0; i < 8; ++i)
        o[i] = __builtin_elementwise_fma(kf[i], w3, __builtin_elementwise_fma(kf[i + 1], w2f, __builtin_elementwise_fma(kf[i + 2], w1f, kf[i + 3] * w0))) * f2{blo(vr[i]), bhi(vr[i])};
#pragma unroll
      for (int c = 0; c < 2; ++c)
        st16_sc1(rws, WS_KV + (dt2 * 80 + cp * 2 + c) * 4096 + (2040 - 64 * st - 8 * rg) * 2,
                 u4{pk(o[7][c], o[6][c]), pk(o[5][c], o[4][c]), pk(o[3][c], o[2][c]), pk(o[1][c], o[0][c])});
    }
    STAMP(4);
  }

  if (wave >= 4) {
    const int hw = wave - 4, pair = hw >> 1, half = hw & 1;
    const bool extra = b < 64 && pair == 0;
    const int nrounds = has1 ? (extra ? 2 : 1) : 3;
    const int ch0 = has1 ? (extra ? 576 + b : 2 * b + pair) : 480 + (b - 240) * 6 + pair, chs = extra ? b - 576 : 2;
    Filt f;
    filt_load(f, ssmk + (size_t)ch0 * 4096, half, lane);
    waitv<4>();
    if (ctr_arrive(ctl + C_H * 4, lane) == 7 && lane < 8) st8_sc1(rws, WS_F1 + lane * 4096 + b * 16, tag);
    STAMP(20 + hw);
    unsigned dd0 = *(const unsigned short*)(ssmd + ch0 * 2);
    __builtin_amdgcn_s_setprio(HPRIO);
    ctr_wait(ctl + C_H * 4, 8);
    const unsigned cv = sb + L_CONV + pair * CV_BYTES;
    filt_build(f, cv, half, lane);
    ctr_wait(ctl + C_G2 * 4, 1);
    u4 xv[2];
#pragma unroll
    for (int j = 0; j < 2; ++j) xv[j] = ld16_sc1(rws, WS_KV + ch0 * 4096 + (lane + 64 * j + 128 * half) * 16);
    ctr_arrive(ctl + C_GO1 * 4, lane);
    if (b == 0 && threadIdx.x == 448) st8_sc1(rws, WS_EPOCH, tag);
    const int c16 = lane & 15, kg = lane >> 4;
    const unsigned pc = ctl + (C_P0 + pair) * 4;
    const unsigned ab = cv + (c16 & 3) * CV_CP + (c16 >> 2) * 8 + kg * 16 + half * 256;
    const unsigned bb = cv + CV_X + 480 - 32 * c16 + 16 * kg + half * 256;
    asm volatile("" : "+v"(xv[0]), "+v"(xv[1]), "+v"(dd0));
#pragma unroll 1
    for (int round = 0; round < nrounds; ++round) {
      const int ch = ch0 + chs * round;
      if (round) filt_build(f, cv, half, lane);
#pragma unroll
      for (int j = 0; j < 2; ++j) lds_st<u4>(cv + CV_X + (lane + 64 * j + 128 * half) * 16, xv[j]);
      const float dd = blo(dd0);
      if (round + 1 < nrounds) {
        filt_load(f, ssmk + (size_t)(ch + chs) * 4096, half, lane);
#pragma unroll
        for (int j = 0; j < 2; ++j) xv[j] = ld16_sc1(rws, WS_KV + (ch + chs) * 4096 + (lane + 64 * j + 128 * half) * 16);
        dd0 = *(const unsigned short*)(ssmd + (ch + chs) * 2);
      }
      ctr_arrive(pc, lane);
      ctr_wait(pc, round * 8 + 2);
      if (has1 && round + 1 == nrounds) ctr_wait(ctl + C_KV * 4, 4);
      f4 acc[8];
      if (round == 0) STAMP(30); else STAMP(31);
      conv_phases(acc, ab, bb);
      if (round == 1) STAMP(36);
      if (round == 0) STAMP(25 + hw);
      ctr_arrive(pc, lane);
      ctr_wait(pc, round * 8 + 4);
#pragma unroll
      for (int n = 0; n < 4; ++n) lds_st<f4>(cv + half * 4096 + n * 1024 + lane * 16, sel4(half, acc[n], acc[n + 4]));
      ctr_arrive(pc, lane);
      ctr_wait(pc, round * 8 + 6);
      u2 y[4];
#pragma unroll
      for (int n = 0; n < 4; ++n) {
        const f4 t = sel4(half, acc[n + 4], acc[n]) + lds_ld<f4>(cv + (half ^ 1) * 4096 + n * 1024 + lane * 16);
        const int t0 = 1024 * half + 256 * n + 16 * c16 + 4 * kg;
        const u2 xq = lds_ld<u2>(cv + CV_X + (2044 - t0) * 2);
        y[n] = u2{pk(__builtin_fmaf(bhi(xq.y), dd, t[0]), __builtin_fmaf(blo(xq.y), dd, t[1])),
                  pk(__builtin_fmaf(bhi(xq.x), dd, t[2]), __builtin_fmaf(blo(xq.x), dd, t[3]))};
      }
      asm volatile("" : "+v"(f.c[0]), "+v"(f.c[1]), "+v"(f.n[0]), "+v"(f.n[1]), "+v"(xv[0]), "+v"(xv[1]), "+v"(dd0));
      ctr_arrive(pc, lane);
#pragma unroll
      for (int n = 0; n < 4; n += 2) {
        const auto sx = __builtin_amdgcn_permlane16_swap(y[n].x, y[n + 1].x, false, false);
        const auto sy = __builtin_amdgcn_permlane16_swap(y[n].y, y[n + 1].y, false, false);
        st16_sc1(rws, WS_YA + ch * 4096 + (1024 * half + 256 * (n + (kg & 1)) + 16 * c16 + 8 * (kg >> 1)) * 2, u4{sx[0], sy[0], sx[1], sy[1]});
      }
      ctr_wait(pc, round * 8 + 8);
    }
    waitv<0>();
    if (ctr_arrive(ctl + (C_Q0 + pair) * 4, lane) == 1 && lane < nrounds) st8_sc1(rws, WS_F3 + (ch0 + chs * lane) * 8, tag);
    STAMP(32 + hw);
    const int s = st * 64 + hw * 16 + c16;
    const size_t xo = (size_t)s * 1280 + (dt2 * 80 + 4 * kg) * 2;
    u2 xq[2] = {*(const u2*)(x + xo + 96), *(const u2*)(x + xo + 128)};
    u2 stt = ld8_sc1(rws, WS_ST + s * 8);
    unsigned b2r = *(const unsigned short*)(b2 + s * 2);
    asm volatile("" : "+v"(xq[0]), "+v"(xq[1]), "+v"(stt), "+v"(b2r));
    if (hw == 3) {
      if (HPOLL2 && has1) {
        const int dta = (dt2 * 5) >> 2;
#pragma unroll 1
        for (unsigned n = 0; n < SPIN_CAP; ++n) {
          const u2 a = ld8_sc1(rws, WS_F2 + xg * 4096 + (dta * 24 + (lane < 48 ? lane : 0)) * 16);
          if (__all(a.x == tag.x && a.y == tag.y)) break;
          asm volatile("" ::: "memory");
        }
        asm volatile("" ::: "memory");
        if (lane == 0) lds_st<unsigned>(ctl + C_GO2 * 4, 1u);
      }
#pragma unroll 1
      for (unsigned n = 0; n < SPIN_CAP; ++n) {
        const u4 a = ld16_sc1(rws, WS_F3 + dt2 * 640 + (lane < 40 ? lane : 0) * 16);
        if (__all(a.x == tag.x && a.y == tag.y && a.z == tag.x && a.w == tag.y)) break;
        __builtin_amdgcn_s_sleep(2);
        asm volatile("" ::: "memory");
      }
      asm volatile("" ::: "memory");
      STAMP(37);
      u4 y[10];
#pragma unroll
      for (int i = 0; i < 10; ++i) y[i] = ld16_sc1(rws, WS_YA + (dt2 * 80 + i * 8 + (lane >> 3)) * 4096 + st * 128 + (lane & 7) * 16);
      SCHED_FENCE();
#pragma unroll
      for (int i = 0; i < 10; ++i) lds_st<u4>(sb + L_YA + i * 1024 + lane * 16, y[i]);
      STAMP(38);
    }
    raw_barrier();
    asm volatile(".Lfk_tail:");
    f4 own[2];
#pragma unroll
    for (int m = 0; m < 2; ++m) {
      const unsigned a = sb + (3 + m) * 1024 + lane * 16;
      own[m] = (lds_ld<f4>(a + hw * L_RING) + lds_ld<f4>(a + ((hw - 1) & 3) * L_RING + 5120)) +
               (lds_ld<f4>(a + ((hw - 2) & 3) * L_RING + 10240) + lds_ld<f4>(a + ((hw - 3) & 3) * L_RING + 15360));
    }
    final_tiles<3, 2>(own, xq, stt, b2r, sb + L_YA + (4 * kg + ((lane >> 2) & 3)) * 128 + hw * 32 + (lane & 3) * 8, out + xo);
    return;
  }

  waitv<0>();
  if (ctr_arrive(ctl + C_H * 4, lane) == 7 && lane < 8) st8_sc1(rws, WS_F1 + lane * 4096 + b * 16, tag);
  STAMP(16 + wave);
  __builtin_amdgcn_s_setprio(GPRIO);
#define POLL_F1()                                                                                                            \
  do {                                                                                                                       \
    if (wave == 0) {                                                                                                         \
      _Pragma("unroll 1") for (unsigned n = 0; n < SPIN_CAP; ++n) {                                                          \
        bool ok = true;                                                                                                      \
        _Pragma("unroll") for (int i = 0; i < 4; ++i) {                                                                      \
          const u2 a = ld8_sc1(rws, WS_F1 + xg * 4096 + i * 1024 + lane * 16);                                               \
          ok = ok && a.x == tag.x && a.y == tag.y;                                                                           \
        }                                                                                                                    \
        if (__all(ok)) break;                                                                                                \
        asm volatile("" ::: "memory");                                                                                       \
      }                                                                                                                      \
      asm volatile("" ::: "memory");                                                                                         \
      if (lane == 0) lds_st<unsigned>(ctl + C_G2 * 4, 1u);                                                                   \
    }                                                                                                                        \
    if (KVFIRST) ctr_wait(ctl + C_GO1 * 4, 4); else ctr_wait(ctl + C_G2 * 4, 1);                                             \
  } while (0)
  unsigned tra[4];
  s4 wf[2][8];

  const int s = st * 64 + wave * 16 + (lane & 15);
  const size_t xo = (size_t)s * 1280 + (dt2 * 80 + 4 * g) * 2;
  u2 xr[3], stt;
  unsigned b2r;
#define EPI_LOADS()                                                                                                          \
  do {                                                                                                                       \
    _Pragma("unroll") for (int m = 0; m < 3; ++m) xr[m] = *(const u2*)(x + xo + m * 32);                                     \
    stt = ld8_sc1(rws, WS_ST + s * 8);                                                                                       \
    b2r = *(const unsigned short*)(b2 + s * 2);                                                                              \
  } while (0)
#define EPI_PIN() asm volatile("" : "+v"(xr[0]), "+v"(xr[1]), "+v"(xr[2]), "+v"(stt), "+v"(b2r))
  if (has1) {
    {
      const int fsw = 2 * (q >> 1) + 4 * (g & 1);
#pragma unroll
      for (int m = 0; m < 4; ++m) tra[m] = ring + (8 * g + q) * 128 + (((2 * m) ^ fsw) + (p >> 1)) * 16 + (p & 1) * 8;
    }
    u2 bias[4];
#pragma unroll
    for (int m = 0; m < 4; ++m) bias[m] = *(const u2*)(b1 + (ft * 64 + 16 * m + 4 * g) * 2);
    unsigned voh[4];
#pragma unroll
    for (int n = 0; n < 4; ++n) voh[n] = WS_H + (g * 640 + dt * 64 + ((n + wave) & 3) * 16 + (lane & 15)) * 16;
    u4 hq[4][4];
    f4 acc[4][4];
#pragma unroll
    for (int m = 0; m < 4; ++m)
#pragma unroll
      for (int n = 0; n < 4; ++n) acc[m][n] = f4{0.f, 0.f, 0.f, 0.f};
#define H_PIECE(i, n) LD_SC1(hq[(i) & 3][n], voh[n], KS1(i) * 40960u, 0)
#define H_LOAD(i) do { H_PIECE(i, 0); H_PIECE(i, 1); H_PIECE(i, 2); H_PIECE(i, 3); } while (0)
#define H_WAIT(N, hs) asm volatile("s_waitcnt vmcnt(%4)" : "+v"(hs[0]), "+v"(hs[1]), "+v"(hs[2]), "+v"(hs[3]) : "i"(N) : "memory")
#define G1_RD(i, k) wf[(i) & 1][k] = tr_read(tra[(k) >> 1] + ((i) % 7) * 4096 + ((k) & 1) * 512)
    s4 res[R1][8];
    ctr_wait(ctl + C_H * 4, 8);
    W1_DMA(0); W1_DMA(1); W1_DMA(2); W1_DMA(3); W1_DMA(4); W1_DMA(5); W1_DMA(6);
#pragma unroll
    for (int i = 0; i < R1; ++i) {
      waitv<24>();
#pragma unroll
      for (int k = 0; k < 8; ++k) res[i][k] = tr_read(tra[k >> 1] + i * 4096 + (k & 1) * 512);
      asm volatile("s_waitcnt lgkmcnt(0)" ::: "memory");
      W1_DMA(7 + i);
    }
    asm volatile("" : "+v"(bias[0]), "+v"(bias[1]), "+v"(bias[2]), "+v"(bias[3]));
    STAMP(5 + wave);
    POLL_F1();
    STAMP(9 + wave);
    H_LOAD(0); H_LOAD(1); if (LA > 2) H_LOAD(2);
#define G1_A(i, k) ((i) < R1 ? cat8(res[(i) < R1 ? (i) : 0][(k) & 6], res[(i) < R1 ? (i) : 0][(k) | 1]) : cat8(wf[(i) & 1][(k) & 6], wf[(i) & 1][(k) | 1]))
#define G1_GROUP(i, k)                                                                                                       \
  do {                                                                                                                       \
    acc[(k) >> 1][((k) & 1) * 2] = mfma16(G1_A(i, k), __builtin_bit_cast(bf8, hq[(i) & 3][((k) & 1) * 2]), acc[(k) >> 1][((k) & 1) * 2]); \
    acc[(k) >> 1][((k) & 1) * 2 + 1] = mfma16(G1_A(i, k), __builtin_bit_cast(bf8, hq[(i) & 3][((k) & 1) * 2 + 1]), acc[(k) >> 1][((k) & 1) * 2 + 1]); \
    SCHED_FENCE();                                                                                                           \
    if ((i) + 1 < 16 && (i) + 1 >= R1) G1_RD((i) + 1, k);                                                                    \
    if ((k) < 4) { if (g1_h(i)) H_PIECE((i) + LA, (k) & 3); }                                                                 \
    else if (g1_x(i) && (i) <= 9) W1_PIECE((i) + 6, (k) & 3);                                                                \
    else if (g1_x(i)) W2_PIECE((i) - 13, (k) & 3);                                                                           \
    SCHED_FENCE();                                                                                                           \
  } while (0)
#define G1_STEP(i)                                                                                                           \
  do {                                                                                                                       \
    H_WAIT(g1_n(i), hq[(i) & 3]);                                                                                            \
    G1_GROUP(i, 0); G1_GROUP(i, 1); G1_GROUP(i, 2); G1_GROUP(i, 3); G1_GROUP(i, 4); G1_GROUP(i, 5); G1_GROUP(i, 6); G1_GROUP(i, 7); \
  } while (0)
    G1_STEP(0); G1_STEP(1); G1_STEP(2); G1_STEP(3); G1_STEP(4); G1_STEP(5); G1_STEP(6); G1_STEP(7); G1_STEP(8); G1_STEP(9);
#pragma unroll
    for (int t = 0; t < 4; ++t) vow[t] = (8 * t + (lane >> 3)) * 4096 + st * 128 + (((lane & 7) ^ ((lane >> 3) & 6)) * 16) - 1024 * t;
    G1_STEP(10); G1_STEP(11); G1_STEP(12); G1_STEP(13); G1_STEP(14); G1_STEP(15);
    STAMP(40 + wave);
    EPI_LOADS();
#pragma unroll
    for (int n = 1; n < 4; ++n)
#pragma unroll
      for (int m = 0; m < 4; ++m) lds_st<f4>(ring + (n == 1 ? 6 : n - 2) * 4096 + m * 1024 + lane * 16, acc[m][n]);
    ctr_arrive(ctl + C_G1 * 4, lane);
    STAMP(14);
    ctr_wait(ctl + C_G1 * 4, 4);
    STAMP(29);
#pragma unroll
    for (int n = 1; n < 4; ++n) {
      const unsigned a = sb + ((wave - n) & 3) * L_RING + (n == 1 ? 6 : n - 2) * 4096 + lane * 16;
#pragma unroll
      for (int m = 0; m < 4; ++m) acc[m][n] = lds_ld<f4>(a + m * 1024);
    }
    SCHED_FENCE();
#pragma unroll
    for (int m = 0; m < 4; ++m) acc[m][0] = (acc[m][0] + acc[m][1]) + (acc[m][2] + acc[m][3]);
    unsigned o[8];
#pragma unroll
    for (int m = 0; m < 4; ++m) {
      o[2 * m] = pk(gelu_t(acc[m][0][0] + blo(bias[m].x)), gelu_t(acc[m][0][1] + bhi(bias[m].x)));
      o[2 * m + 1] = pk(gelu_t(acc[m][0][2] + blo(bias[m].y)), gelu_t(acc[m][0][3] + bhi(bias[m].y)));
    }
#pragma unroll
    for (int P = 0; P < 2; ++P)
      st16_sc1(rws, WS_U + ((ft * 2 + P) * 40 + dt * 4 + wave) * 1024 + lane * 16, u4{o[4 * P], o[4 * P + 1], o[4 * P + 2], o[4 * P + 3]});
    ctr_arrive(ctl + C_KV * 4, lane);
    STAMP(13);
    EPI_PIN();
    W2_DMA(2);
    waitv<4>();
    if (ctr_arrive(ctl + C_G3 * 4, lane) == 3 && lane < 8) st8_sc1(rws, WS_F2 + lane * 4096 + (dt * 24 + ft) * 16, tag);
    STAMP(44 + wave);
    ctr_wait(ctl + C_G3 * 4, 4);
    W2_DMA(3); W2_DMA(4); W2_DMA(5); W2_DMA(6);
  } else {
#pragma unroll
    for (int t = 0; t < 4; ++t) vow[t] = (8 * t + (lane >> 3)) * 4096 + st * 128 + (((lane & 7) ^ ((lane >> 3) & 6)) * 16) - 1024 * t;
    POLL_F1();
    EPI_LOADS();
    EPI_PIN();
    W2_DMA(0); W2_DMA(1); W2_DMA(2); W2_DMA(3); W2_DMA(4); W2_DMA(5); W2_DMA(6);
  }

  asm volatile("" : "+s"(ring), "+s"(rot2));
  {
    const int fsw = 4 * (g & 1) + (q & 2);
#pragma unroll
    for (int n = 0; n < 4; ++n) tra[n] = ring + (4 * g + q) * 128 + (((2 * ((n + wave) & 3)) ^ fsw) + (p >> 1)) * 16 + (p & 1) * 8;
  }
  const unsigned vou0 = WS_U + dt2 * 5120 + lane * 16, vou1 = vou0 + 4096;
  u4 uq[4][5];
  f4 acc[5][4];
#pragma unroll
  for (int m = 0; m < 5; ++m)
#pragma unroll
    for (int n = 0; n < 4; ++n) acc[m][n] = f4{0.f, 0.f, 0.f, 0.f};
#define U_PIECE(j, m) LD_SC1(uq[(j) & 3][m], (m) < 4 ? vou0 : vou1, KS2(j) * 40960u, ((m) & 3) * 1024)
#define U_LOAD(j) do { U_PIECE(j, 0); U_PIECE(j, 1); U_PIECE(j, 2); U_PIECE(j, 3); U_PIECE(j, 4); } while (0)
#define U_WAIT(N, us) asm volatile("s_waitcnt vmcnt(%5)" : "+v"(us[0]), "+v"(us[1]), "+v"(us[2]), "+v"(us[3]), "+v"(us[4]) : "i"(N) : "memory")
#define G2_RD(j, k) wf[(j) & 1][k] = tr_read(tra[(k) >> 1] + (((j) + 2) % 7) * 4096 + ((k) & 1) * 2048)
#define G2_B(j, n) ((j) < R2 ? cat8(res2[(j) < R2 ? (j) : 0][2 * (n)], res2[(j) < R2 ? (j) : 0][2 * (n) + 1]) : cat8(wf[(j) & 1][2 * (n)], wf[(j) & 1][2 * (n) + 1]))
#define G2_MM(j, m, n) acc[m][n] = mfma16(__builtin_bit_cast(bf8, uq[(j) & 3][m]), G2_B(j, n), acc[m][n])
#define G2_GROUP(j, k)                                                                                                       \
  do {                                                                                                                       \
    G2_MM(j, (2 * (k)) % 5, (2 * (k)) / 5); G2_MM(j, (2 * (k) + 1) % 5, (2 * (k) + 1) / 5);                                  \
    SCHED_FENCE();                                                                                                           \
    if ((k) < 8 && (j) + 1 < 12 && (j) + 1 >= R2) G2_RD((j) + 1, (k) & 7);                                                   \
    if ((k) < 5) { if (g2_u(j)) U_PIECE((j) + LA, (k) % 5); }                                                                 \
    else if ((k) < 9 && g2_x(j)) W2_PIECE((j) + 6, ((k) - 5) & 3);                                                           \
    SCHED_FENCE();                                                                                                           \
  } while (0)
#define G2_STEP(j)                                                                                                           \
  do {                                                                                                                       \
    U_WAIT(g2_n(j), uq[(j) & 3]);                                                                                                  \
    G2_GROUP(j, 0); G2_GROUP(j, 1); G2_GROUP(j, 2); G2_GROUP(j, 3); G2_GROUP(j, 4);                                          \
    G2_GROUP(j, 5); G2_GROUP(j, 6); G2_GROUP(j, 7); G2_GROUP(j, 8); G2_GROUP(j, 9);                                          \
  } while (0)
  s4 res2[R2][8];
  static_assert(R2 <= 3, "wait below");
  waitv<16>();
#pragma unroll
  for (int j = 0; j < R2; ++j) {
#pragma unroll
    for (int k = 0; k < 8; ++k) res2[j][k] = tr_read(tra[k >> 1] + ((j + 2) % 7) * 4096 + (k & 1) * 2048);
    asm volatile("s_waitcnt lgkmcnt(0)" ::: "memory");
    W2_DMA(7 + j);
  }
  STAMP(39);
  if ((!HPOLL2 || !has1) && wave == 0) {
    const int dta = (dt2 * 5) >> 2;
#pragma unroll 1
    for (unsigned n = 0; n < SPIN_CAP; ++n) {
      const u2 a = ld8_sc1(rws, WS_F2 + xg * 4096 + (dta * 24 + (lane < 48 ? lane : 0)) * 16);
      if (__all(a.x == tag.x && a.y == tag.y)) break;
      asm volatile("" ::: "memory");
    }
    asm volatile("" ::: "memory");
    if (lane == 0) lds_st<unsigned>(ctl + C_GO2 * 4, 1u);
  } else {
    ctr_wait(ctl + C_GO2 * 4, 1);
  }
  STAMP(48 + wave);
  U_LOAD(0); U_LOAD(1); if (LA > 2) U_LOAD(2);
  G2_STEP(0); G2_STEP(1); G2_STEP(2); G2_STEP(3); G2_STEP(4); G2_STEP(5); G2_STEP(6); G2_STEP(7); G2_STEP(8); G2_STEP(9); G2_STEP(10); G2_STEP(11);
  STAMP(52 + wave);
#pragma unroll
  for (int n = 0; n < 4; ++n)
#pragma unroll
    for (int m = (n == 0 ? 3 : 0); m < 5; ++m) lds_st<f4>(ring + (n * 5 + m) * 1024 + lane * 16, acc[m][n]);
  f4 own[3] = {acc[0][0], acc[1][0], acc[2][0]};
  raw_barrier();
  STAMP(56 + wave);
#pragma unroll
  for (int w = 1; w < 4; ++w) {
    const unsigned a = sb + ((wave - w) & 3) * L_RING + w * 5120 + lane * 16;
#pragma unroll
    for (int m = 0; m < 3; ++m) own[m] += lds_ld<f4>(a + m * 1024);
  }
  final_tiles<0, 3>(own, xr, stt, b2r, sb + L_YA + (4 * g + q) * 128 + wave * 32 + p * 8, out + xo);
  STAMP(60 + wave);
}

extern "C" void kernel_launch(void* const* d_in, const int* in_sizes, int n_in, void* d_out, int out_size, void* d_ws, size_t ws_size,
                              hipStream_t stream) {
  (void)hipFuncSetAttribute((const void*)fused_kernel, hipFuncAttributeMaxDynamicSharedMemorySize, L_TOTAL);
  fused_kernel<<<256, 512, L_TOTAL, stream>>>((const char*)d_in[0], (const char*)d_in[1], (const char*)d_in[2], (const char*)d_in[3],
                                              (const char*)d_in[4], (const char*)d_in[5], (const char*)d_in[7], (const char*)d_in[8],
                                              (const char*)d_in[9], (const char*)d_in[10], (char*)d_out, (char*)d_ws);
}
```

```cpp
#include <hip/hip_runtime.h>
#include <hip/hip_bf16.h>
#include <stdint.h>

typedef __attribute__((ext_vector_type(8))) __bf16 bf8;
typedef __attribute__((ext_vector_type(2))) __bf16 bf2;
typedef __attribute__((ext_vector_type(4))) short s4;
typedef __attribute__((ext_vector_type(4))) float f4;
typedef __attribute__((ext_vector_type(2))) float f2;
typedef __attribute__((ext_vector_type(4))) unsigned u4;
typedef __attribute__((ext_vector_type(2))) unsigned u2;
#define LAS __attribute__((address_space(3)))
#define DEVI __device__ __forceinline__
#define SCHED_FENCE() __builtin_amdgcn_sched_barrier(0)

#define PF_W2 0
#define CONVQ 4
#define HPOL " sc1"
#define MAP1 0
#define R1 5
#define R2 3
#define THR 5
#define GPRIO 0
#define HPRIO 0
#define KVFIRST 1
#define LA 3
#define CODEPF 1
#define R2WAIT 1
#define HPOLL2 0
#define TLBPF 1
#define OUTPOL ""
#define SPR 2
#define NT_FIRST 0

constexpr unsigned WS_EPOCH = 0;
constexpr unsigned WS_F1 = 4096;
constexpr unsigned WS_F2 = WS_F1 + 32768;
constexpr unsigned WS_F4 = WS_F2 + 32768;
constexpr unsigned WS_F3 = WS_F4 + 32768;
constexpr unsigned WS_ST = WS_F3 + 8192;
constexpr unsigned WS_H = WS_ST + 16384;
constexpr unsigned WS_KV = WS_H + 2621440;
constexpr unsigned WS_YA = WS_KV + 2621440;
constexpr unsigned WS_U = WS_YA + 2621440;
constexpr unsigned WS_END = WS_U + 1966080;
constexpr size_t WS_DBG = WS_END;

#define STAMP(idx) do {} while (0)

constexpr unsigned L_RING = 28672;
constexpr unsigned CV_CP = 4160;
constexpr unsigned CV_X = 4 * CV_CP;
constexpr unsigned CV_BYTES = 21248;
constexpr unsigned L_CONV = 4 * L_RING;
constexpr unsigned L_HBUF = L_CONV;
constexpr unsigned L_KBUF = L_CONV + 10752;
constexpr unsigned L_VBUF = L_KBUF + 11264;
constexpr unsigned L_YA = L_CONV + CV_BYTES;
constexpr unsigned L_CTL = L_CONV + 2 * CV_BYTES;
constexpr unsigned L_TOTAL = L_CTL + 128;
enum { C_H = 0, C_GO1, C_G1, C_G2, C_G3, C_GO2, C_P0, C_P1, C_Q0, C_Q1, C_KV, C_K2 };
constexpr unsigned SPIN_CAP = 1u << 16;

DEVI float blo(unsigned u) { return __uint_as_float(u << 16); }
DEVI float bhi(unsigned u) { return __uint_as_float(u & 0xffff0000u); }
DEVI unsigned pk(float a, float b) { f2 v = {a, b}; return __builtin_bit_cast(unsigned, __builtin_convertvector(v, bf2)); }
DEVI float rbf(float a) { return blo(pk(a, a)); }
template <int CTRL, int RM> DEVI float dppf(float x) {
  return __builtin_bit_cast(float, __builtin_amdgcn_update_dpp(0, __builtin_bit_cast(int, x), CTRL, RM, 0xf, true));
}
template <class T> DEVI T ldnt(const void* p) { return __builtin_nontemporal_load((const T*)p); }
DEVI u4 make_srd(const void* p, unsigned bytes) {
  uint64_t a = (uint64_t)p;
  u4 r = {(unsigned)a, (unsigned)(a >> 32) & 0xffffu, bytes, 0x00020000u};
  return r;
}
DEVI s4 tr_read(unsigned addr) { return __builtin_amdgcn_ds_read_tr16_b64_v4i16((LAS s4*)(size_t)addr); }
DEVI bf8 cat8(s4 a, s4 b) {
  typedef __attribute__((ext_vector_type(8))) short s8;
  s8 v = __builtin_shufflevector(a, b, 0, 1, 2, 3, 4, 5, 6, 7);
  return __builtin_bit_cast(bf8, v);
}
DEVI f4 mfma16(bf8 a, bf8 b, f4 c) { return __builtin_amdgcn_mfma_f32_16x16x32_bf16(a, b, c, 0, 0, 0); }
template <int N> DEVI void waitv() { asm volatile("s_waitcnt vmcnt(%0)" ::"i"(N) : "memory"); }
template <class T> DEVI T lds_ld(unsigned a) { return *(const LAS T*)(size_t)a; }
template <class T> DEVI void lds_st(unsigned a, T v) { *(LAS T*)(size_t)a = v; }
DEVI void raw_barrier() { asm volatile("s_waitcnt lgkmcnt(0)\n\ts_barrier" ::: "memory"); }
DEVI unsigned ctr_arrive(unsigned a, int lane) {
  asm volatile("s_waitcnt lgkmcnt(0)" ::: "memory");
  unsigned old = 0;
  if (lane == 0) old = __hip_atomic_fetch_add((LAS unsigned*)(size_t)a, 1u, __ATOMIC_RELAXED, __HIP_MEMORY_SCOPE_WORKGROUP);
  old = __builtin_amdgcn_readfirstlane(old);
  asm volatile("" ::: "memory");
  return old;
}
DEVI void ctr_wait(unsigned a, unsigned n) {
  while (__builtin_amdgcn_readfirstlane(__hip_atomic_load((LAS unsigned*)(size_t)a, __ATOMIC_RELAXED, __HIP_MEMORY_SCOPE_WORKGROUP)) < n)
    __builtin_amdgcn_s_sleep(1);
  asm volatile("" ::: "memory");
}
typedef __amdgpu_buffer_rsrc_t rsrc_t;
DEVI u4 ld16_sc1(rsrc_t r, unsigned off) { return __builtin_amdgcn_raw_buffer_load_b128(r, off, 0, 16); }
DEVI u2 ld8_sc1(rsrc_t r, unsigned off) { return __builtin_amdgcn_raw_buffer_load_b64(r, off, 0, 16); }
DEVI void st16_sc1(rsrc_t r, unsigned off, u4 v) { __builtin_amdgcn_raw_buffer_store_b128(v, r, off, 0, 16); }
DEVI void st8_sc1(rsrc_t r, unsigned off, u2 v) { __builtin_amdgcn_raw_buffer_store_b64(v, r, off, 0, 16); }

DEVI f4 sel4(bool c, f4 a, f4 b) {
  asm("" : "+v"(a)); asm("" : "+v"(b));
  return c ? a : b;
}
DEVI float gelu_t(float x) {
  const float t = x * __builtin_fmaf(x * x, -0.10294324f, -2.3022082f);
  return x * __builtin_amdgcn_rcpf(1.f + __builtin_amdgcn_exp2f(t));
}

DEVI void ln_row(const unsigned (&xr)[5], unsigned (&hp)[5], float& m1, float& inv1) {
  float e[10], s = 0.f, q = 0.f;
  const float piv = blo(__builtin_amdgcn_readfirstlane(xr[0]));
#pragma unroll
  for (int j = 0; j < 5; ++j) { e[2 * j] = blo(xr[j]); e[2 * j + 1] = bhi(xr[j]); }
#pragma unroll
  for (int j = 0; j < 10; ++j) { const float d = e[j] - piv; s += d; q = __builtin_fmaf(d, d, q); }
  s += dppf<0xB1, 0xf>(s);  q += dppf<0xB1, 0xf>(q);
  s += dppf<0x4E, 0xf>(s);  q += dppf<0x4E, 0xf>(q);
  s += dppf<0x141, 0xf>(s); q += dppf<0x141, 0xf>(q);
  s += dppf<0x140, 0xf>(s); q += dppf<0x140, 0xf>(q);
  s += dppf<0x142, 0xa>(s); q += dppf<0x142, 0xa>(q);
  s += dppf<0x143, 0xc>(s); q += dppf<0x143, 0xc>(q);
  const float md = __builtin_bit_cast(float, __builtin_amdgcn_readlane(__builtin_bit_cast(int, s), 63)) * (1.f / 640.f);
  const float qq = __builtin_bit_cast(float, __builtin_amdgcn_readlane(__builtin_bit_cast(int, q), 63)) * (1.f / 640.f);
  const float var = __builtin_fmaxf(qq - md * md, 0.f), mean = piv + md;
  m1 = rbf(mean);
  inv1 = __builtin_amdgcn_rcpf(rbf(__builtin_amdgcn_sqrtf(rbf(rbf(var) + 1e-5f))));
  const float m2 = rbf((mean - m1) * inv1);
  const float inv2 = __builtin_amdgcn_rcpf(rbf(__builtin_amdgcn_sqrtf(rbf(rbf(var * inv1 * inv1) + 1e-5f))));
  const float A = inv1 * inv2, B = -m2 * inv2;
#pragma unroll
  for (int j = 0; j < 5; ++j) hp[j] = pk(__builtin_fmaf(e[2 * j] - m1, A, B), __builtin_fmaf(e[2 * j + 1] - m1, A, B));
}

struct Filt { u4 c[2]; u2 n[2]; };
DEVI void filt_load(Filt& f, const char* krow, int half, int lane) {
#pragma unroll
  for (int j = 0; j < 2; ++j) {
    const int z = lane + 64 * j + 128 * half;
    f.c[j] = ldnt<u4>(krow + z * 16);
    f.n[j] = ldnt<u2>(krow + (z < 255 ? z * 16 + 16 : 0));
  }
}
DEVI void filt_build(Filt& f, unsigned cv, int half, int lane) {
  const u4 zero = {0u, 0u, 0u, 0u};
  if (half == 1) {
    if (lane == 63) f.n[1] = u2{0u, 0u};
    if (lane < 30) lds_st<u4>(cv + CV_X + 4096 + lane * 16, zero);
  } else {
    if (lane < 4) lds_st<u4>(cv + lane * CV_CP, zero);
    if (lane == 0) {
      const unsigned d4 = f.c[0].x, d5 = f.c[0].y;
      lds_st<u4>(cv + 16, u4{0u, 0u, 0u, d4 << 16});
      lds_st<u4>(cv + CV_CP + 16, u4{0u, 0u, 0u, d4});
      lds_st<u4>(cv + 2 * CV_CP + 16, u4{0u, 0u, d4 << 16, __builtin_amdgcn_alignbit(d5, d4, 16)});
      lds_st<u4>(cv + 3 * CV_CP + 16, u4{0u, 0u, d4, d5});
    }
  }
#pragma unroll
  for (int j = 0; j < 2; ++j) {
    const u4 c = f.c[j]; const u2 n = f.n[j];
    const unsigned a0 = __builtin_amdgcn_alignbit(c.y, c.x, 16), a1 = __builtin_amdgcn_alignbit(c.z, c.y, 16), a2 = __builtin_amdgcn_alignbit(c.w, c.z, 16),
                   a3 = __builtin_amdgcn_alignbit(n.x, c.w, 16), a4 = __builtin_amdgcn_alignbit(n.y, n.x, 16);
    const unsigned p = cv + (lane + 64 * j + 128 * half + 2) * 16;
    lds_st<u4>(p, u4{a0, a1, a2, a3});
    lds_st<u4>(p + CV_CP, u4{c.y, c.z, c.w, n.x});
    lds_st<u4>(p + 2 * CV_CP, u4{a1, a2, a3, a4});
    lds_st<u4>(p + 3 * CV_CP, u4{c.z, c.w, n.x, n.y});
  }
}

DEVI void conv_phases(f4 (&acc)[8], unsigned ab, unsigned bb) {
  bf8 A0[8], B0[8], A1[8], B1[8];
#pragma unroll
  for (int n = 0; n < 8; ++n) acc[n] = f4{0.f, 0.f, 0.f, 0.f};
#define CV_LOAD(A, B, q)                                                                              \
  _Pragma("unroll") for (int p = 0; p < 8; ++p) {                                                      \
    B[p] = *(const volatile LAS bf8*)(size_t)(bb + 3584 - 512 * p + 64 * (q));                          \
    s4 lo = *(const volatile LAS s4*)(size_t)(ab + 512 * p + 64 * (q));                                 \
    s4 hi = *(const volatile LAS s4*)(size_t)(ab + 512 * p + 64 * (q) + 8);                             \
    A[p] = cat8(lo, hi);                                                                               \
  }
#define CV_MMA(A, B)                                                                                   \
  _Pragma("unroll") for (int p = 0; p < 8; ++p)                                                        \
  _Pragma("unroll") for (int m = 0; m + p < 8; ++m) acc[p + m] = mfma16(A[p], B[m], acc[p + m]);
#define CV_SCHED()                                                                                     \
  _Pragma("unroll") for (int z = 0; z < 24; ++z) {                                                     \
    __builtin_amdgcn_sched_group_barrier(0x8, 1, 0);                                                   \
    __builtin_amdgcn_sched_group_barrier(0x100, 1, 0);                                                 \
  }                                                                                                    \
  __builtin_amdgcn_sched_group_barrier(0x8, 12, 0);                                                    \
  SCHED_FENCE();
  CV_LOAD(A0, B0, 0);
  SCHED_FENCE();
#pragma unroll 1
  for (int q = 0; q < CONVQ; q += 2) {
    CV_LOAD(A1, B1, q + 1); CV_MMA(A0, B0); CV_SCHED();
    CV_LOAD(A0, B0, (q + 2) & 3); CV_MMA(A1, B1); CV_SCHED();
  }
}

constexpr int g1_h(int i) { return i < 0 ? 4 : i + LA < 16 ? 4 : 0; }
constexpr int g1_x(int i) { return ((i > R1 && i <= 9) || i >= 13) ? 4 : 0; }
constexpr int g1_n(int i) { int n = g1_x(i - LA); for (int t = i - LA + 1; t < i; ++t) n += g1_h(t) + g1_x(t); return n; }
constexpr int g2_u(int j) { return j < 0 ? 5 : j + LA < 12 ? 5 : 0; }
constexpr int g2_x(int j) { return (j > R2 && j <= 5) ? 4 : 0; }
constexpr int g2_n(int j) { int n = g2_x(j - LA); for (int t = j - LA + 1; t < j; ++t) n += g2_u(t) + g2_x(t); return n; }

template <int M0, int NM>
DEVI void final_tiles(f4 (&own)[NM], const u2 (&xr)[NM], u2 stt, unsigned b2r, unsigned yab, char* op) {
  const float bb = blo(b2r), m1 = __uint_as_float(stt.x), inv1 = __uint_as_float(stt.y);
#pragma unroll
  for (int m = 0; m < NM; ++m) {
    const u2 ya = __builtin_bit_cast(u2, tr_read(yab + (M0 + m) * 2048));
    const float x0 = blo(xr[m].x), x1 = bhi(xr[m].x), x2 = blo(xr[m].y), x3 = bhi(xr[m].y);
    const float r0 = __builtin_fmaf((x0 - m1) * inv1, blo(ya.x), x0) + (own[m][0] + bb), r1 = __builtin_fmaf((x1 - m1) * inv1, bhi(ya.x), x1) + (own[m][1] + bb);
    const float r2 = __builtin_fmaf((x2 - m1) * inv1, blo(ya.y), x2) + (own[m][2] + bb), r3 = __builtin_fmaf((x3 - m1) * inv1, bhi(ya.y), x3) + (own[m][3] + bb);
    const u2 ov = {pk(r0, r1), pk(r2, r3)};
    asm volatile("global_store_dwordx2 %0, %1, off offset:%2" OUTPOL ::"v"(op), "v"(ov), "i"((M0 + m) * 32) : "memory");
  }
}

__global__ __launch_bounds__(512, 2) __attribute__((amdgpu_waves_per_eu(2, 2))) void fused_kernel(
    const char* __restrict__ x, const char* __restrict__ kk, const char* __restrict__ vv, const char* __restrict__ shk,
    const char* __restrict__ ssmk, const char* __restrict__ ssmd, const char* __restrict__ w1, const char* __restrict__ b1,
    const char* __restrict__ w2, const char* __restrict__ b2, char* __restrict__ out, char* ws) {
  extern __shared__ __attribute__((aligned(16))) char smem[];
  const int lane = threadIdx.x & 63, wave = __builtin_amdgcn_readfirstlane(threadIdx.x >> 6);
  const int b = blockIdx.x, xg = b & 7, r = b >> 3;
  const unsigned sb = __builtin_amdgcn_readfirstlane((unsigned)(size_t)smem);
  const unsigned ctl = sb + L_CTL;
  asm volatile("" ::"s"(x), "s"(kk), "s"(vv), "s"(shk), "s"(ssmk), "s"(ssmd), "s"(w1), "s"(b1), "s"(w2), "s"(b2), "s"(out), "s"(ws));
  const rsrc_t rws = __builtin_amdgcn_make_buffer_rsrc(ws, 0, WS_END, 0x00020000);
  if (wave == 0) STAMP(0);
  const int dt2 = r & 7, st = xg * 4 + (r >> 3);

  const u4 srdS = make_srd(ws, WS_END);
  const int g = lane >> 4, q = (lane >> 2) & 3, p = lane & 3;
  unsigned ring = sb + wave * L_RING;
  const bool has1 = r < 30;
  const int dt = MAP1 ? (xg & 1) * 5 + r % 5 : r % 10, ft = MAP1 ? (xg >> 1) * 6 + r / 5 : xg * 3 + r / 10;
  const int rot1 = dt * 16 / 10;
  int rot2 = dt2 * 12 / 8;
  const u4 srdW1 = make_srd(w1, 6291456), srdW2 = make_srd(w2, 6291456);
  unsigned vow[4];
#define DMA1(POL, slot, t, srd, soff)                                                                                          \
  asm volatile("s_mov_b32 m0, %0\n\ts_nop 0\n\tbuffer_load_dwordx4 %1, %2, %3 offen offset:%4" POL " lds" ::"s"(ring + (slot) * 4096), \
               "v"(vow[t]), "s"(srd), "s"(soff), "i"((t) * 1024)                                                               \
               : "memory")
#define KS1(i) (4u * (((i) + rot1) & 15) + wave)
#define KS2(j) (4u * ((j) + rot2 >= 12 ? (j) + rot2 - 12 : (j) + rot2) + wave)
#define W1_PIECE(i, t) do { if ((i) < NT_FIRST) DMA1(" nt", (i) % 7, t, srdW1, KS1(i) * 98304u); else DMA1("", (i) % 7, t, srdW1, KS1(i) * 98304u); } while (0)
#define W2_PIECE(j, t) do { if ((j) < NT_FIRST) DMA1(" nt", ((j) + 2) % 7, t, srdW2, KS2(j) * 131072u); else DMA1("", ((j) + 2) % 7, t, srdW2, KS2(j) * 131072u); } while (0)
#define W1_DMA(i) do { W1_PIECE(i, 0); W1_PIECE(i, 1); W1_PIECE(i, 2); W1_PIECE(i, 3); } while (0)
#define W2_DMA(j) do { W2_PIECE(j, 0); W2_PIECE(j, 1); W2_PIECE(j, 2); W2_PIECE(j, 3); } while (0)
#define W1_THR(i) do { W1_PIECE(i, 0); waitv<THR>(); W1_PIECE(i, 1); waitv<THR>(); W1_PIECE(i, 2); waitv<THR>(); W1_PIECE(i, 3); waitv<THR>(); } while (0)
#define W2_THR(j) do { W2_PIECE(j, 0); waitv<THR>(); W2_PIECE(j, 1); waitv<THR>(); W2_PIECE(j, 2); waitv<THR>(); W2_PIECE(j, 3); waitv<THR>(); } while (0)
#define LD_SC1(dst, voff, soff, imm) \
  asm volatile("buffer_load_dwordx4 %0, %1, %2, %3 offen offset:%4" HPOL : "=&v"(dst) : "v"(voff), "s"(srdS), "s"(soff), "i"(imm) : "memory")
#pragma unroll
  for (int t = 0; t < 4; ++t) vow[t] = (8 * t + (lane >> 3)) * 3072 + ft * 128 + (((lane & 7) ^ (2 * ((lane >> 4) & 1) + 4 * (t & 1))) * 16) - 1024 * t;
  u2 tag;
  unsigned cpf = 0;
  {
    unsigned xr[5];
    asm volatile("buffer_load_dword %0, %5, %6, %7 offen nt\n\tbuffer_load_dword %1, %5, %6, %7 offen offset:256 nt\n\t"
                 "buffer_load_dword %2, %5, %6, %7 offen offset:512 nt\n\tbuffer_load_dword %3, %5, %6, %7 offen offset:768 nt\n\t"
                 "buffer_load_dword %4, %5, %6, %7 offen offset:1024 nt"
                 : "=&v"(xr[0]), "=&v"(xr[1]), "=&v"(xr[2]), "=&v"(xr[3]), "=&v"(xr[4])
                 : "v"(lane * 4), "s"(make_srd(x, 2621440)), "s"((b * 8 + wave) * 1280)
                 : "memory");
    {
      const u4 srdK = make_srd(kk, 2621440), srdV = make_srd(vv, 2621440);
#pragma unroll
      for (int i = 0; i < 3; ++i) {
        const int pc = wave + 8 * i;
        const bool isv = pc >= 11;
        const int n = (isv ? pc - 11 : pc) * 64 + lane, g8 = (n * 6473) >> 19, rem = n - g8 * 81, rr = (rem * 205) >> 11;
        const int row = g8 * 8 + rr, c = rem - rr * 10;
        const int grow = st * 64 + row - (isv ? 0 : 3);
        const bool ok = rem < 80 && grow >= 0 && row < (isv ? 64 : 67);
        const unsigned vo = ok ? grow * 1280 + dt2 * 160 + c * 16 : 0x7fffff00u;
        const unsigned dst = sb + L_KBUF + pc * 1024;
        if (isv) asm volatile("s_mov_b32 m0, %0\n\ts_nop 0\n\tbuffer_load_dwordx4 %1, %2, 0 offen nt lds" ::"s"(dst), "v"(vo), "s"(srdV) : "memory");
        else     asm volatile("s_mov_b32 m0, %0\n\ts_nop 0\n\tbuffer_load_dwordx4 %1, %2, 0 offen nt lds" ::"s"(dst), "v"(vo), "s"(srdK) : "memory");
      }
    }
    u2 ep; u4 shw;
    asm volatile("buffer_load_dwordx2 %0, off, %2, 0 sc1\n\tbuffer_load_dwordx4 %1, %3, %4, 0 offen"
                 : "=&v"(ep), "=&v"(shw)
                 : "s"(srdS), "v"((dt2 * 80 + (wave >= 3 ? wave - 3 : 0) * 16 + (lane >> 3) * 2) * 8), "s"(make_srd(shk, 5120))
                 : "memory");
    static_assert(WS_EPOCH == 0, "epoch load above");
    if (CODEPF && wave == 7) {
      uint64_t pc; unsigned len;
      asm volatile("s_getpc_b64 %0\n.Lfk_here:\n\ts_mov_b32 %1, .Lfk_tail-.Lfk_here" : "=s"(pc), "=s"(len));
      const unsigned o = (r * 16 + (lane & 15)) * 128;
      asm volatile("global_load_dword %0, %1, %2" : "=v"(cpf) : "v"(o < len ? o : 0u), "s"(pc) : "memory");
    }
    if (TLBPF && wave == 6) {
      const int j = r * 16 + (lane & 15);
      const char* p = j < 96 ? w1 + j * 65536 : j < 192 ? w2 + (j - 96) * 65536 : j < 232 ? ssmk + (j - 192) * 65536
                    : j < 272 ? out + (j - 232) * 65536 : j < 424 ? ws + (j - 272) * 65536 : x;
      asm volatile("global_load_dword %0, %1, off sc1" : "=v"(cpf) : "v"(p) : "memory");
    }
    if (wave == 0 && lane < 16) lds_st<unsigned>(ctl + lane * 4, 0u);
    if (wave >= 6) waitv<6>(); else waitv<5>();
    asm volatile("" : "+v"(xr[0]), "+v"(xr[1]), "+v"(xr[2]), "+v"(xr[3]), "+v"(xr[4])::"memory");
    STAMP(1);
    unsigned hp[5]; float m1, inv1;
    ln_row(xr, hp, m1, inv1);
#pragma unroll
    for (int j = 0; j < 5; ++j) lds_st<unsigned>(sb + L_HBUF + wave * 1344 + (lane + 64 * j) * 4, hp[j]);
    if (lane == 0) lds_st<u2>(ctl + 64 + wave * 8, u2{__float_as_uint(m1), __float_as_uint(inv1)});
    STAMP(2);
    if (wave >= 6) waitv<1>(); else waitv<0>();
    asm volatile("" : "+v"(ep), "+v"(shw)::"memory");
    const uint64_t tg = (((uint64_t)ep.y << 32) | ep.x) * 6364136223846793005ull + 1442695040888963407ull;
    tag = u2{(unsigned)__builtin_amdgcn_readfirstlane((unsigned)tg), (unsigned)__builtin_amdgcn_readfirstlane((unsigned)(tg >> 32))};
    STAMP(3);
    raw_barrier();
    if (wave < 3) {
      const unsigned tb = sb + L_HBUF + ((lane >> 2) & 3) * 1344 + (lane >> 4) * 32 + (lane & 3) * 8;
      u4 v[4];
#pragma unroll
      for (int i = 0; i < 4; ++i) { const int it = i < 3 ? wave + 3 * i : 9; v[i] = __builtin_bit_cast(u4, cat8(tr_read(tb + it * 128), tr_read(tb + it * 128 + 4 * 1344))); }
      const u4 sv = lds_ld<u4>(ctl + 64 + (lane & 3) * 16);
#pragma unroll
      for (int i = 0; i < 4; ++i)
        if (i < 3 || wave == 0) st16_sc1(rws, WS_H + (b * 640 + (i < 3 ? wave + 3 * i : 9) * 64 + lane) * 16, v[i]);
      if (wave == 1 && lane < 4) st16_sc1(rws, WS_ST + b * 64 + lane * 16, sv);
    } else {
      const int rg = lane & 7, cp = (wave - 3) * 8 + (lane >> 3);
      const unsigned ka = sb + L_KBUF + rg * 1296 + cp * 4;
      unsigned kr[11], vr[8];
#pragma unroll
      for (int i = 0; i < 11; ++i) kr[i] = lds_ld<unsigned>(ka + i * 160 + (i >> 3) * 16);
#pragma unroll
      for (int i = 0; i < 8; ++i) vr[i] = lds_ld<unsigned>(ka + 11264 + i * 160);
      SCHED_FENCE();
      const f2 w0 = {blo(shw.x), blo(shw.z)}, w1f = {bhi(shw.x), bhi(shw.z)}, w2f = {blo(shw.y), blo(shw.w)}, w3 = {bhi(shw.y), bhi(shw.w)};
      f2 kf[11], o[8];
#pragma unroll
      for (int i = 0; i < 11; ++i) kf[i] = f2{blo(kr[i]), bhi(kr[i])};
#pragma unroll
      for (int i = 0; i < 8; ++i)
        o[i] = __builtin_elementwise_fma(kf[i], w3, __builtin_elementwise_fma(kf[i + 1], w2f, __builtin_elementwise_fma(kf[i + 2], w1f, kf[i + 3] * w0))) * f2{blo(vr[i]), bhi(vr[i])};
#pragma unroll
      for (int c = 0; c < 2; ++c)
        st16_sc1(rws, WS_KV + (dt2 * 80 + cp * 2 + c) * 4096 + (2040 - 64 * st - 8 * rg) * 2,
                 u4{pk(o[7][c], o[6][c]), pk(o[5][c], o[4][c]), pk(o[3][c], o[2][c]), pk(o[1][c], o[0][c])});
    }
    STAMP(4);
  }

  if (wave >= 4) {
    const int hw = wave - 4, pair = hw >> 1, half = hw & 1;
    const bool extra = b < 160 - 32 * SPR && pair == 0;
    const int nrounds = has1 ? (extra ? 2 : 1) : SPR;
    const int ch0 = has1 ? (extra ? 480 + 32 * SPR + b : 2 * b + pair) : 480 + (b - 240) * 2 * SPR + pair, chs = extra ? b - 480 - 32 * SPR : 2;
    Filt f;
    filt_load(f, ssmk + (size_t)ch0 * 4096, half, lane);
    waitv<4>();
    asm volatile("" : "+v"(cpf));
    if (ctr_arrive(ctl + C_H * 4, lane) == 7 && lane < 8) st8_sc1(rws, WS_F1 + lane * 4096 + b * 16, tag);
    STAMP(20 + hw);
    unsigned dd0 = *(const unsigned short*)(ssmd + ch0 * 2);
    __builtin_amdgcn_s_setprio(HPRIO);
    ctr_wait(ctl + C_H * 4, 8);
    const unsigned cv = sb + L_CONV + pair * CV_BYTES;
    filt_build(f, cv, half, lane);
    if (hw == 3) {
#pragma unroll 1
      for (unsigned n = 0; n < SPIN_CAP; ++n) {
        bool ok = true;
#pragma unroll
        for (int i = 0; i < 4; ++i) { const u2 a = ld8_sc1(rws, WS_F1 + xg * 4096 + i * 1024 + lane * 16); ok = ok && a.x == tag.x && a.y == tag.y; }
        if (__all(ok)) break;
        asm volatile("" ::: "memory");
      }
      asm volatile("" ::: "memory");
      if (lane == 0) lds_st<unsigned>(ctl + C_G2 * 4, 1u);
      STAMP(24);
    } else {
      ctr_wait(ctl + C_G2 * 4, 1);
    }
    u4 xv[2];
#pragma unroll
    for (int j = 0; j < 2; ++j) xv[j] = ld16_sc1(rws, WS_KV + ch0 * 4096 + (lane + 64 * j + 128 * half) * 16);
    ctr_arrive(ctl + C_GO1 * 4, lane);
    if (b == 0 && threadIdx.x == 448) st8_sc1(rws, WS_EPOCH, tag);
    const int c16 = lane & 15, kg = lane >> 4;
    const unsigned pc = ctl + (C_P0 + pair) * 4;
    const unsigned ab = cv + (c16 & 3) * CV_CP + (c16 >> 2) * 8 + kg * 16 + half * 256;
    const unsigned bb = cv + CV_X + 480 - 32 * c16 + 16 * kg + half * 256;
    asm volatile("" : "+v"(xv[0]), "+v"(xv[1]), "+v"(dd0));
#pragma unroll 1
    for (int round = 0; round < nrounds; ++round) {
      const int ch = ch0 + chs * round;
      if (round) filt_build(f, cv, half, lane);
#pragma unroll
      for (int j = 0; j < 2; ++j) lds_st<u4>(cv + CV_X + (lane + 64 * j + 128 * half) * 16, xv[j]);
      const float dd = blo(dd0);
      if (round + 1 < nrounds) {
        filt_load(f, ssmk + (size_t)(ch + chs) * 4096, half, lane);
#pragma unroll
        for (int j = 0; j < 2; ++j) xv[j] = ld16_sc1(rws, WS_KV + (ch + chs) * 4096 + (lane + 64 * j + 128 * half) * 16);
        dd0 = *(const unsigned short*)(ssmd + (ch + chs) * 2);
      }
      ctr_arrive(pc, lane);
      ctr_wait(pc, round * 8 + 2);
      if (has1 && round + 1 == nrounds) ctr_wait(ctl + C_KV * 4, 4);
      f4 acc[8];
      if (round == 0) STAMP(30); else STAMP(31);
      conv_phases(acc, ab, bb);
      if (round == 1) STAMP(36);
      if (round == 0) STAMP(25 + hw);
      ctr_arrive(pc, lane);
      ctr_wait(pc, round * 8 + 4);
#pragma unroll
      for (int n = 0; n < 4; ++n) lds_st<f4>(cv + half * 4096 + n * 1024 + lane * 16, sel4(half, acc[n], acc[n + 4]));
      ctr_arrive(pc, lane);
      ctr_wait(pc, round * 8 + 6);
      u2 y[4];
#pragma unroll
      for (int n = 0; n < 4; ++n) {
        const f4 t = sel4(half, acc[n + 4], acc[n]) + lds_ld<f4>(cv + (half ^ 1) * 4096 + n * 1024 + lane * 16);
        const int t0 = 1024 * half + 256 * n + 16 * c16 + 4 * kg;
        const u2 xq = lds_ld<u2>(cv + CV_X + (2044 - t0) * 2);
        y[n] = u2{pk(__builtin_fmaf(bhi(xq.y), dd, t[0]), __builtin_fmaf(blo(xq.y), dd, t[1])),
                  pk(__builtin_fmaf(bhi(xq.x), dd, t[2]), __builtin_fmaf(blo(xq.x), dd, t[3]))};
      }
      asm volatile("" : "+v"(f.c[0]), "+v"(f.c[1]), "+v"(f.n[0]), "+v"(f.n[1]), "+v"(xv[0]), "+v"(xv[1]), "+v"(dd0));
      ctr_arrive(pc, lane);
#pragma unroll
      for (int n = 0; n < 4; n += 2) {
        const auto sx = __builtin_amdgcn_permlane16_swap(y[n].x, y[n + 1].x, false, false);
        const auto sy = __builtin_amdgcn_permlane16_swap(y[n].y, y[n + 1].y, false, false);
        st16_sc1(rws, WS_YA + ch * 4096 + (1024 * half + 256 * (n + (kg & 1)) + 16 * c16 + 8 * (kg >> 1)) * 2, u4{sx[0], sy[0], sx[1], sy[1]});
      }
      ctr_wait(pc, round * 8 + 8);
    }
    waitv<0>();
    if (ctr_arrive(ctl + (C_Q0 + pair) * 4, lane) == 1 && lane < nrounds) st8_sc1(rws, WS_F3 + (ch0 + chs * lane) * 8, tag);
    STAMP(32 + hw);
    const int s = st * 64 + hw * 16 + c16;
    const size_t xo = (size_t)s * 1280 + (dt2 * 80 + 4 * kg) * 2;
    u2 xq[2] = {*(const u2*)(x + xo + 96), *(const u2*)(x + xo + 128)};
    u2 stt = ld8_sc1(rws, WS_ST + s * 8);
    unsigned b2r = *(const unsigned short*)(b2 + s * 2);
    asm volatile("" : "+v"(xq[0]), "+v"(xq[1]), "+v"(stt), "+v"(b2r));
    if (hw == 3) {
#pragma unroll 1
      for (unsigned n = 0; n < SPIN_CAP; ++n) {
        const u4 a = ld16_sc1(rws, WS_F3 + dt2 * 640 + (lane < 40 ? lane : 0) * 16);
        if (__all(a.x == tag.x && a.y == tag.y && a.z == tag.x && a.w == tag.y)) break;
        __builtin_amdgcn_s_sleep(2);
        asm volatile("" ::: "memory");
      }
      asm volatile("" ::: "memory");
      STAMP(37);
      u4 y[10];
#pragma unroll
      for (int i = 0; i < 10; ++i) y[i] = ld16_sc1(rws, WS_YA + (dt2 * 80 + i * 8 + (lane >> 3)) * 4096 + st * 128 + (lane & 7) * 16);
      SCHED_FENCE();
#pragma unroll
      for (int i = 0; i < 10; ++i) lds_st<u4>(sb + L_YA + i * 1024 + lane * 16, y[i]);
      STAMP(38);
    }
    raw_barrier();
    asm volatile(".Lfk_tail:");
    f4 own[2];
#pragma unroll
    for (int m = 0; m < 2; ++m) {
      const unsigned a = sb + (3 + m) * 1024 + lane * 16;
      own[m] = (lds_ld<f4>(a + hw * L_RING) + lds_ld<f4>(a + ((hw - 1) & 3) * L_RING + 5120)) +
               (lds_ld<f4>(a + ((hw - 2) & 3) * L_RING + 10240) + lds_ld<f4>(a + ((hw - 3) & 3) * L_RING + 15360));
    }
    final_tiles<3, 2>(own, xq, stt, b2r, sb + L_YA + (4 * kg + ((lane >> 2) & 3)) * 128 + hw * 32 + (lane & 3) * 8, out + xo);
    return;
  }

  waitv<0>();
  if (ctr_arrive(ctl + C_H * 4, lane) == 7 && lane < 8) st8_sc1(rws, WS_F1 + lane * 4096 + b * 16, tag);
  STAMP(16 + wave);
  __builtin_amdgcn_s_setprio(GPRIO);
#define POLL_F1() ctr_wait(ctl + C_GO1 * 4, 4)
  unsigned tra[4];
  s4 wf[2][8];

  const int s = st * 64 + wave * 16 + (lane & 15);
  const size_t xo = (size_t)s * 1280 + (dt2 * 80 + 4 * g) * 2;
  u2 xr[3], stt;
  unsigned b2r;
#define EPI_LOADS()                                                                                                          \
  do {                                                                                                                       \
    _Pragma("unroll") for (int m = 0; m < 3; ++m) xr[m] = *(const u2*)(x + xo + m * 32);                                     \
    stt = ld8_sc1(rws, WS_ST + s * 8);                                                                                       \
    b2r = *(const unsigned short*)(b2 + s * 2);                                                                              \
  } while (0)
#define EPI_PIN() asm volatile("" : "+v"(xr[0]), "+v"(xr[1]), "+v"(xr[2]), "+v"(stt), "+v"(b2r))
  if (has1) {
    {
      const int fsw = 2 * (q >> 1) + 4 * (g & 1);
#pragma unroll
      for (int m = 0; m < 4; ++m) tra[m] = ring + (8 * g + q) * 128 + (((2 * m) ^ fsw) + (p >> 1)) * 16 + (p & 1) * 8;
    }
    u2 bias[4];
#pragma unroll
    for (int m = 0; m < 4; ++m) bias[m] = *(const u2*)(b1 + (ft * 64 + 16 * m + 4 * g) * 2);
    unsigned voh[4];
#pragma unroll
    for (int n = 0; n < 4; ++n) voh[n] = WS_H + (g * 640 + dt * 64 + ((n + wave) & 3) * 16 + (lane & 15)) * 16;
    u4 hq[4][4];
    f4 acc[4][4];
#pragma unroll
    for (int m = 0; m < 4; ++m)
#pragma unroll
      for (int n = 0; n < 4; ++n) acc[m][n] = f4{0.f, 0.f, 0.f, 0.f};
#define H_PIECE(i, n) LD_SC1(hq[(i) & 3][n], voh[n], KS1(i) * 40960u, 0)
#define H_LOAD(i) do { H_PIECE(i, 0); H_PIECE(i, 1); H_PIECE(i, 2); H_PIECE(i, 3); } while (0)
#define H_WAIT(N, hs) asm volatile("s_waitcnt vmcnt(%4)" : "+v"(hs[0]), "+v"(hs[1]), "+v"(hs[2]), "+v"(hs[3]) : "i"(N) : "memory")
#define G1_RD(i, k) wf[(i) & 1][k] = tr_read(tra[(k) >> 1] + ((i) % 7) * 4096 + ((k) & 1) * 512)
    s4 res[R1][8];
    asm volatile("" : "+v"(bias[0]), "+v"(bias[1]), "+v"(bias[2]), "+v"(bias[3]));
    ctr_wait(ctl + C_H * 4, 8);
    W1_DMA(0); W1_DMA(1); W1_DMA(2); W1_DMA(3); W1_DMA(4); W1_DMA(5); W1_DMA(6);
#pragma unroll
    for (int i = 0; i < R1; ++i) {
      waitv<24>();
#pragma unroll
      for (int k = 0; k < 8; ++k) res[i][k] = tr_read(tra[k >> 1] + i * 4096 + (k & 1) * 512);
      asm volatile("s_waitcnt lgkmcnt(0)" ::: "memory");
      W1_DMA(7 + i);
    }
    STAMP(5 + wave);
    POLL_F1();
    STAMP(9 + wave);
    H_LOAD(0); H_LOAD(1); if (LA > 2) H_LOAD(2);
#define G1_A(i, k) ((i) < R1 ? cat8(res[(i) < R1 ? (i) : 0][(k) & 6], res[(i) < R1 ? (i) : 0][(k) | 1]) : cat8(wf[(i) & 1][(k) & 6], wf[(i) & 1][(k) | 1]))
#define G1_GROUP(i, k)                                                                                                       \
  do {                                                                                                                       \
    acc[(k) >> 1][((k) & 1) * 2] = mfma16(G1_A(i, k), __builtin_bit_cast(bf8, hq[(i) & 3][((k) & 1) * 2]), acc[(k) >> 1][((k) & 1) * 2]); \
    acc[(k) >> 1][((k) & 1) * 2 + 1] = mfma16(G1_A(i, k), __builtin_bit_cast(bf8, hq[(i) & 3][((k) & 1) * 2 + 1]), acc[(k) >> 1][((k) & 1) * 2 + 1]); \
    SCHED_FENCE();                                                                                                           \
    if ((i) + 1 < 16 && (i) + 1 >= R1) G1_RD((i) + 1, k);                                                                    \
    if ((k) < 4) { if (g1_h(i)) H_PIECE((i) + LA, (k) & 3); }                                                                 \
    else if (g1_x(i) && (i) <= 9) W1_PIECE((i) + 6, (k) & 3);                                                                \
    else if (g1_x(i)) W2_PIECE((i) - 13, (k) & 3);                                                                           \
    SCHED_FENCE();                                                                                                           \
  } while (0)
#define G1_STEP(i)                                                                                                           \
  do {                                                                                                                       \
    H_WAIT(g1_n(i), hq[(i) & 3]);                                                                                            \
    G1_GROUP(i, 0); G1_GROUP(i, 1); G1_GROUP(i, 2); G1_GROUP(i, 3); G1_GROUP(i, 4); G1_GROUP(i, 5); G1_GROUP(i, 6); G1_GROUP(i, 7); \
  } while (0)
    G1_STEP(0); G1_STEP(1); G1_STEP(2); G1_STEP(3); G1_STEP(4); G1_STEP(5); G1_STEP(6); G1_STEP(7); G1_STEP(8); G1_STEP(9);
#pragma unroll
    for (int t = 0; t < 4; ++t) vow[t] = (8 * t + (lane >> 3)) * 4096 + st * 128 + (((lane & 7) ^ ((lane >> 3) & 6)) * 16) - 1024 * t;
    G1_STEP(10); G1_STEP(11); G1_STEP(12); G1_STEP(13); G1_STEP(14); G1_STEP(15);
    STAMP(40 + wave);
    EPI_LOADS();
#pragma unroll
    for (int n = 1; n < 4; ++n)
#pragma unroll
      for (int m = 0; m < 4; ++m) lds_st<f4>(ring + (n == 1 ? 6 : n - 2) * 4096 + m * 1024 + lane * 16, acc[m][n]);
    ctr_arrive(ctl + C_G1 * 4, lane);
    STAMP(14);
    ctr_wait(ctl + C_G1 * 4, 4);
    STAMP(29);
#pragma unroll
    for (int n = 1; n < 4; ++n) {
      const unsigned a = sb + ((wave - n) & 3) * L_RING + (n == 1 ? 6 : n - 2) * 4096 + lane * 16;
#pragma unroll
      for (int m = 0; m < 4; ++m) acc[m][n] = lds_ld<f4>(a + m * 1024);
    }
    SCHED_FENCE();
#pragma unroll
    for (int m = 0; m < 4; ++m) acc[m][0] = (acc[m][0] + acc[m][1]) + (acc[m][2] + acc[m][3]);
    unsigned o[8];
#pragma unroll
    for (int m = 0; m < 4; ++m) {
      o[2 * m] = pk(gelu_t(acc[m][0][0] + blo(bias[m].x)), gelu_t(acc[m][0][1] + bhi(bias[m].x)));
      o[2 * m + 1] = pk(gelu_t(acc[m][0][2] + blo(bias[m].y)), gelu_t(acc[m][0][3] + bhi(bias[m].y)));
    }
#pragma unroll
    for (int P = 0; P < 2; ++P)
      st16_sc1(rws, WS_U + ((ft * 2 + P) * 40 + dt * 4 + wave) * 1024 + lane * 16, u4{o[4 * P], o[4 * P + 1], o[4 * P + 2], o[4 * P + 3]});
    ctr_arrive(ctl + C_KV * 4, lane);
    STAMP(13);
    EPI_PIN();
    waitv<0>();
    if (ctr_arrive(ctl + C_G3 * 4, lane) == 3 && lane < 8) st8_sc1(rws, WS_F2 + lane * 4096 + (dt * 24 + ft) * 16, tag);
    STAMP(44 + wave);
    ctr_wait(ctl + C_G3 * 4, 4);
    W2_DMA(3); W2_DMA(4); W2_DMA(5); W2_DMA(6);
  } else {
#pragma unroll
    for (int t = 0; t < 4; ++t) vow[t] = (8 * t + (lane >> 3)) * 4096 + st * 128 + (((lane & 7) ^ ((lane >> 3) & 6)) * 16) - 1024 * t;
    POLL_F1();
    EPI_LOADS();
    EPI_PIN();
    W2_DMA(0); W2_DMA(1); W2_DMA(2); W2_DMA(3); W2_DMA(4); W2_DMA(5); W2_DMA(6);
  }

  asm volatile("" : "+s"(ring), "+s"(rot2));
  {
    const int fsw = 4 * (g & 1) + (q & 2);
#pragma unroll
    for (int n = 0; n < 4; ++n) tra[n] = ring + (4 * g + q) * 128 + (((2 * ((n + wave) & 3)) ^ fsw) + (p >> 1)) * 16 + (p & 1) * 8;
  }
  const unsigned vou0 = WS_U + dt2 * 5120 + lane * 16, vou1 = vou0 + 4096;
  u4 uq[4][5];
  f4 acc[5][4];
#pragma unroll
  for (int m = 0; m < 5; ++m)
#pragma unroll
    for (int n = 0; n < 4; ++n) acc[m][n] = f4{0.f, 0.f, 0.f, 0.f};
#define U_PIECE(j, m) LD_SC1(uq[(j) & 3][m], (m) < 4 ? vou0 : vou1, KS2(j) * 40960u, ((m) & 3) * 1024)
#define U_LOAD(j) do { U_PIECE(j, 0); U_PIECE(j, 1); U_PIECE(j, 2); U_PIECE(j, 3); U_PIECE(j, 4); } while (0)
#define U_WAIT(N, us) asm volatile("s_waitcnt vmcnt(%5)" : "+v"(us[0]), "+v"(us[1]), "+v"(us[2]), "+v"(us[3]), "+v"(us[4]) : "i"(N) : "memory")
#define G2_RD(j, k) wf[(j) & 1][k] = tr_read(tra[(k) >> 1] + (((j) + 2) % 7) * 4096 + ((k) & 1) * 2048)
#define G2_B(j, n) ((j) < R2 ? cat8(res2[(j) < R2 ? (j) : 0][2 * (n)], res2[(j) < R2 ? (j) : 0][2 * (n) + 1]) : cat8(wf[(j) & 1][2 * (n)], wf[(j) & 1][2 * (n) + 1]))
#define G2_MM(j, m, n) acc[m][n] = mfma16(__builtin_bit_cast(bf8, uq[(j) & 3][m]), G2_B(j, n), acc[m][n])
#define G2_GROUP(j, k)                                                                                                       \
  do {                                                                                                                       \
    G2_MM(j, (2 * (k)) % 5, (2 * (k)) / 5); G2_MM(j, (2 * (k) + 1) % 5, (2 * (k) + 1) / 5);                                  \
    SCHED_FENCE();                                                                                                           \
    if ((k) < 8 && (j) + 1 < 12 && (j) + 1 >= R2) G2_RD((j) + 1, (k) & 7);                                                   \
    if ((k) < 5) { if (g2_u(j)) U_PIECE((j) + LA, (k) % 5); }                                                                 \
    else if ((k) < 9 && g2_x(j)) W2_PIECE((j) + 6, ((k) - 5) & 3);                                                           \
    SCHED_FENCE();                                                                                                           \
  } while (0)
#define G2_STEP(j)                                                                                                           \
  do {                                                                                                                       \
    U_WAIT(g2_n(j), uq[(j) & 3]);                                                                                                  \
    G2_GROUP(j, 0); G2_GROUP(j, 1); G2_GROUP(j, 2); G2_GROUP(j, 3); G2_GROUP(j, 4);                                          \
    G2_GROUP(j, 5); G2_GROUP(j, 6); G2_GROUP(j, 7); G2_GROUP(j, 8); G2_GROUP(j, 9);                                          \
  } while (0)
  s4 res2[R2][8];
  static_assert(R2 <= 3, "wait below");
  waitv<16>();
  const unsigned f2o = WS_F2 + xg * 4096 + (((dt2 * 5) >> 2) * 24 + (lane < 48 ? lane : 0)) * 16;
  u2 f2v = {0u, 0u};
  if (wave == 0) asm volatile("buffer_load_dwordx2 %0, %1, %2, 0 offen sc1" : "=v"(f2v) : "v"(f2o), "s"(srdS) : "memory");
#pragma unroll
  for (int j = 0; j < R2; ++j) {
#pragma unroll
    for (int k = 0; k < 8; ++k) res2[j][k] = tr_read(tra[k >> 1] + ((j + 2) % 7) * 4096 + (k & 1) * 2048);
    asm volatile("s_waitcnt lgkmcnt(0)" ::: "memory");
    W2_DMA(7 + j);
  }
  STAMP(39);
  if (wave == 0) {
    asm volatile("s_waitcnt vmcnt(%1)" : "+v"(f2v) : "i"(4 * R2) : "memory");
    if (!__all(f2v.x == tag.x && f2v.y == tag.y)) {
#pragma unroll 1
      for (unsigned n = 0; n < SPIN_CAP; ++n) {
        const u2 a = ld8_sc1(rws, f2o);
        if (__all(a.x == tag.x && a.y == tag.y)) break;
        asm volatile("" ::: "memory");
      }
    }
    asm volatile("" ::: "memory");
    if (lane == 0) lds_st<unsigned>(ctl + C_GO2 * 4, 1u);
  } else {
    ctr_wait(ctl + C_GO2 * 4, 1);
  }
  STAMP(48 + wave);
  U_LOAD(0); U_LOAD(1); if (LA > 2) U_LOAD(2);
  G2_STEP(0); G2_STEP(1); G2_STEP(2); G2_STEP(3); G2_STEP(4); G2_STEP(5); G2_STEP(6); G2_STEP(7); G2_STEP(8); G2_STEP(9); G2_STEP(10); G2_STEP(11);
  STAMP(52 + wave);
#pragma unroll
  for (int n = 0; n < 4; ++n)
#pragma unroll
    for (int m = (n == 0 ? 3 : 0); m < 5; ++m) lds_st<f4>(ring + (n * 5 + m) * 1024 + lane * 16, acc[m][n]);
  f4 own[3] = {acc[0][0], acc[1][0], acc[2][0]};
  raw_barrier();
  STAMP(56 + wave);
#pragma unroll
  for (int w = 1; w < 4; ++w) {
    const unsigned a = sb + ((wave - w) & 3) * L_RING + w * 5120 + lane * 16;
#pragma unroll
    for (int m = 0; m < 3; ++m) own[m] += lds_ld<f4>(a + m * 1024);
  }
  final_tiles<0, 3>(own, xr, stt, b2r, sb + L_YA + (4 * g + q) * 128 + wave * 32 + p * 8, out + xo);
  STAMP(60 + wave);
}

extern "C" void kernel_launch(void* const* d_in, const int* in_sizes, int n_in, void* d_out, int out_size, void* d_ws, size_t ws_size,
                              hipStream_t stream) {
  (void)hipFuncSetAttribute((const void*)fused_kernel, hipFuncAttributeMaxDynamicSharedMemorySize, L_TOTAL);
  fused_kernel<<<256, 512, L_TOTAL, stream>>>((const char*)d_in[0], (const char*)d_in[1], (const char*)d_in[2], (const char*)d_in[3],
                                              (const char*)d_in[4], (const char*)d_in[5], (const char*)d_in[7], (const char*)d_in[8],
                                              (const char*)d_in[9], (const char*)d_in[10], (char*)d_out, (char*)d_ws);
}
```

```cpp
#include <hip/hip_runtime.h>
#include <hip/hip_bf16.h>
#include <stdint.h>

typedef __attribute__((ext_vector_type(8))) __bf16 bf8;
typedef __attribute__((ext_vector_type(2))) __bf16 bf2;
typedef __attribute__((ext_vector_type(4))) short s4;
typedef __attribute__((ext_vector_type(4))) float f4;
typedef __attribute__((ext_vector_type(2))) float f2;
typedef __attribute__((ext_vector_type(4))) unsigned u4;
typedef __attribute__((ext_vector_type(2))) unsigned u2;
#define LAS __attribute__((address_space(3)))
#define DEVI __device__ __forceinline__
#define SCHED_FENCE() __builtin_amdgcn_sched_barrier(0)

#define PF_W2 0
#define CONVQ 4
#define HPOL " sc1"
#define MAP1 0
#define R1 5
#define R2 3
#define THR 5
#define GPRIO 0
#define HPRIO 0
#define KVFIRST 1
#define LA 3
#define CODEPF 1
#define R2WAIT 1
#define HPOLL2 0
#define TLBPF 1
#define OUTPOL ""
#define FILTPF 1
#define XPAIR 0
#define XPRIO 0
#define SPDELAY 1
#define SPR 2
#define NT_FIRST 0

constexpr unsigned WS_EPOCH = 0;
constexpr unsigned WS_F1 = 4096;
constexpr unsigned WS_F2 = WS_F1 + 32768;
constexpr unsigned WS_F4 = WS_F2 + 32768;
constexpr unsigned WS_F3 = WS_F4 + 32768;
constexpr unsigned WS_ST = WS_F3 + 8192;
constexpr unsigned WS_H = WS_ST + 16384;
constexpr unsigned WS_KV = WS_H + 2621440;
constexpr unsigned WS_YA = WS_KV + 2621440;
constexpr unsigned WS_U = WS_YA + 2621440;
constexpr unsigned WS_END = WS_U + 1966080;
constexpr size_t WS_DBG = WS_END;

#define STAMP(idx) do {} while (0)

constexpr unsigned L_RING = 28672;
constexpr unsigned CV_CP = 4160;
constexpr unsigned CV_X = 4 * CV_CP;
constexpr unsigned CV_BYTES = 21248;
constexpr unsigned L_CONV = 4 * L_RING;
constexpr unsigned L_HBUF = L_CONV;
constexpr unsigned L_KBUF = L_CONV + 10752;
constexpr unsigned L_VBUF = L_KBUF + 11264;
constexpr unsigned L_YA = L_CONV + CV_BYTES;
constexpr unsigned L_CTL = L_CONV + 2 * CV_BYTES;
constexpr unsigned L_TOTAL = L_CTL + 128;
enum { C_H = 0, C_GO1, C_G1, C_G2, C_G3, C_GO2, C_P0, C_P1, C_Q0, C_Q1, C_KV, C_K2 };
constexpr unsigned SPIN_CAP = 1u << 16;

DEVI float blo(unsigned u) { return __uint_as_float(u << 16); }
DEVI float bhi(unsigned u) { return __uint_as_float(u & 0xffff0000u); }
DEVI unsigned pk(float a, float b) { f2 v = {a, b}; return __builtin_bit_cast(unsigned, __builtin_convertvector(v, bf2)); }
DEVI float rbf(float a) { return blo(pk(a, a)); }
template <int CTRL, int RM> DEVI float dppf(float x) {
  return __builtin_bit_cast(float, __builtin_amdgcn_update_dpp(0, __builtin_bit_cast(int, x), CTRL, RM, 0xf, true));
}
template <class T> DEVI T ldnt(const void* p) { return __builtin_nontemporal_load((const T*)p); }
DEVI u4 make_srd(const void* p, unsigned bytes) {
  uint64_t a = (uint64_t)p;
  u4 r = {(unsigned)a, (unsigned)(a >> 32) & 0xffffu, bytes, 0x00020000u};
  return r;
}
DEVI s4 tr_read(unsigned addr) { return __builtin_amdgcn_ds_read_tr16_b64_v4i16((LAS s4*)(size_t)addr); }
DEVI bf8 cat8(s4 a, s4 b) {
  typedef __attribute__((ext_vector_type(8))) short s8;
  s8 v = __builtin_shufflevector(a, b, 0, 1, 2, 3, 4, 5, 6, 7);
  return __builtin_bit_cast(bf8, v);
}
DEVI f4 mfma16(bf8 a, bf8 b, f4 c) { return __builtin_amdgcn_mfma_f32_16x16x32_bf16(a, b, c, 0, 0, 0); }
template <int N> DEVI void waitv() { asm volatile("s_waitcnt vmcnt(%0)" ::"i"(N) : "memory"); }
template <class T> DEVI T lds_ld(unsigned a) { return *(const LAS T*)(size_t)a; }
template <class T> DEVI void lds_st(unsigned a, T v) { *(LAS T*)(size_t)a = v; }
DEVI void raw_barrier() { asm volatile("s_waitcnt lgkmcnt(0)\n\ts_barrier" ::: "memory"); }
DEVI unsigned ctr_arrive(unsigned a, int lane) {
  asm volatile("s_waitcnt lgkmcnt(0)" ::: "memory");
  unsigned old = 0;
  if (lane == 0) old = __hip_atomic_fetch_add((LAS unsigned*)(size_t)a, 1u, __ATOMIC_RELAXED, __HIP_MEMORY_SCOPE_WORKGROUP);
  old = __builtin_amdgcn_readfirstlane(old);
  asm volatile("" ::: "memory");
  return old;
}
DEVI void ctr_wait(unsigned a, unsigned n) {
  while (__builtin_amdgcn_readfirstlane(__hip_atomic_load((LAS unsigned*)(size_t)a, __ATOMIC_RELAXED, __HIP_MEMORY_SCOPE_WORKGROUP)) < n)
    __builtin_amdgcn_s_sleep(1);
  asm volatile("" ::: "memory");
}
typedef __amdgpu_buffer_rsrc_t rsrc_t;
DEVI u4 ld16_sc1(rsrc_t r, unsigned off) { return __builtin_amdgcn_raw_buffer_load_b128(r, off, 0, 16); }
DEVI u2 ld8_sc1(rsrc_t r, unsigned off) { return __builtin_amdgcn_raw_buffer_load_b64(r, off, 0, 16); }
DEVI void st16_sc1(rsrc_t r, unsigned off, u4 v) { __builtin_amdgcn_raw_buffer_store_b128(v, r, off, 0, 16); }
DEVI void st8_sc1(rsrc_t r, unsigned off, u2 v) { __builtin_amdgcn_raw_buffer_store_b64(v, r, off, 0, 16); }

DEVI f4 sel4(bool c, f4 a, f4 b) {
  asm("" : "+v"(a)); asm("" : "+v"(b));
  return c ? a : b;
}
DEVI float gelu_t(float x) {
  const float t = x * __builtin_fmaf(x * x, -0.10294324f, -2.3022082f);
  return x * __builtin_amdgcn_rcpf(1.f + __builtin_amdgcn_exp2f(t));
}

DEVI void ln_row(const unsigned (&xr)[5], unsigned (&hp)[5], float& m1, float& inv1) {
  float e[10], s = 0.f, q = 0.f;
  const float piv = blo(__builtin_amdgcn_readfirstlane(xr[0]));
#pragma unroll
  for (int j = 0; j < 5; ++j) { e[2 * j] = blo(xr[j]); e[2 * j + 1] = bhi(xr[j]); }
#pragma unroll
  for (int j = 0; j < 10; ++j) { const float d = e[j] - piv; s += d; q = __builtin_fmaf(d, d, q); }
  s += dppf<0xB1, 0xf>(s);  q += dppf<0xB1, 0xf>(q);
  s += dppf<0x4E, 0xf>(s);  q += dppf<0x4E, 0xf>(q);
  s += dppf<0x141, 0xf>(s); q += dppf<0x141, 0xf>(q);
  s += dppf<0x140, 0xf>(s); q += dppf<0x140, 0xf>(q);
  s += dppf<0x142, 0xa>(s); q += dppf<0x142, 0xa>(q);
  s += dppf<0x143, 0xc>(s); q += dppf<0x143, 0xc>(q);
  const float md = __builtin_bit_cast(float, __builtin_amdgcn_readlane(__builtin_bit_cast(int, s), 63)) * (1.f / 640.f);
  const float qq = __builtin_bit_cast(float, __builtin_amdgcn_readlane(__builtin_bit_cast(int, q), 63)) * (1.f / 640.f);
  const float var = __builtin_fmaxf(qq - md * md, 0.f), mean = piv + md;
  m1 = rbf(mean);
  inv1 = __builtin_amdgcn_rcpf(rbf(__builtin_amdgcn_sqrtf(rbf(rbf(var) + 1e-5f))));
  const float m2 = rbf((mean - m1) * inv1);
  const float inv2 = __builtin_amdgcn_rcpf(rbf(__builtin_amdgcn_sqrtf(rbf(rbf(var * inv1 * inv1) + 1e-5f))));
  const float A = inv1 * inv2, B = -m2 * inv2;
#pragma unroll
  for (int j = 0; j < 5; ++j) hp[j] = pk(__builtin_fmaf(e[2 * j] - m1, A, B), __builtin_fmaf(e[2 * j + 1] - m1, A, B));
}

struct Filt { u4 c[2]; u2 n[2]; };
DEVI void filt_load(Filt& f, const char* krow, int half, int lane) {
#pragma unroll
  for (int j = 0; j < 2; ++j) {
    const int z = lane + 64 * j + 128 * half;
    f.c[j] = ldnt<u4>(krow + z * 16);
    f.n[j] = ldnt<u2>(krow + (z < 255 ? z * 16 + 16 : 0));
  }
}
DEVI void filt_build(Filt& f, unsigned cv, int half, int lane) {
  const u4 zero = {0u, 0u, 0u, 0u};
  if (half == 1) {
    if (lane == 63) f.n[1] = u2{0u, 0u};
    if (lane < 30) lds_st<u4>(cv + CV_X + 4096 + lane * 16, zero);
  } else {
    if (lane < 4) lds_st<u4>(cv + lane * CV_CP, zero);
    if (lane == 0) {
      const unsigned d4 = f.c[0].x, d5 = f.c[0].y;
      lds_st<u4>(cv + 16, u4{0u, 0u, 0u, d4 << 16});
      lds_st<u4>(cv + CV_CP + 16, u4{0u, 0u, 0u, d4});
      lds_st<u4>(cv + 2 * CV_CP + 16, u4{0u, 0u, d4 << 16, __builtin_amdgcn_alignbit(d5, d4, 16)});
      lds_st<u4>(cv + 3 * CV_CP + 16, u4{0u, 0u, d4, d5});
    }
  }
#pragma unroll
  for (int j = 0; j < 2; ++j) {
    const u4 c = f.c[j]; const u2 n = f.n[j];
    const unsigned a0 = __builtin_amdgcn_alignbit(c.y, c.x, 16), a1 = __builtin_amdgcn_alignbit(c.z, c.y, 16), a2 = __builtin_amdgcn_alignbit(c.w, c.z, 16),
                   a3 = __builtin_amdgcn_alignbit(n.x, c.w, 16), a4 = __builtin_amdgcn_alignbit(n.y, n.x, 16);
    const unsigned p = cv + (lane + 64 * j + 128 * half + 2) * 16;
    lds_st<u4>(p, u4{a0, a1, a2, a3});
    lds_st<u4>(p + CV_CP, u4{c.y, c.z, c.w, n.x});
    lds_st<u4>(p + 2 * CV_CP, u4{a1, a2, a3, a4});
    lds_st<u4>(p + 3 * CV_CP, u4{c.z, c.w, n.x, n.y});
  }
}

DEVI void conv_phases(f4 (&acc)[8], unsigned ab, unsigned bb) {
  bf8 A0[8], B0[8], A1[8], B1[8];
#pragma unroll
  for (int n = 0; n < 8; ++n) acc[n] = f4{0.f, 0.f, 0.f, 0.f};
#define CV_LOAD(A, B, q)                                                                              \
  _Pragma("unroll") for (int p = 0; p < 8; ++p) {                                                      \
    B[p] = *(const volatile LAS bf8*)(size_t)(bb + 3584 - 512 * p + 64 * (q));                          \
    s4 lo = *(const volatile LAS s4*)(size_t)(ab + 512 * p + 64 * (q));                                 \
    s4 hi = *(const volatile LAS s4*)(size_t)(ab + 512 * p + 64 * (q) + 8);                             \
    A[p] = cat8(lo, hi);                                                                               \
  }
#define CV_MMA(A, B)                                                                                   \
  _Pragma("unroll") for (int p = 0; p < 8; ++p)                                                        \
  _Pragma("unroll") for (int m = 0; m + p < 8; ++m) acc[p + m] = mfma16(A[p], B[m], acc[p + m]);
#define CV_SCHED()                                                                                     \
  _Pragma("unroll") for (int z = 0; z < 24; ++z) {                                                     \
    __builtin_amdgcn_sched_group_barrier(0x8, 1, 0);                                                   \
    __builtin_amdgcn_sched_group_barrier(0x100, 1, 0);                                                 \
  }                                                                                                    \
  __builtin_amdgcn_sched_group_barrier(0x8, 12, 0);                                                    \
  SCHED_FENCE();
  CV_LOAD(A0, B0, 0);
  SCHED_FENCE();
#pragma unroll 1
  for (int q = 0; q < CONVQ; q += 2) {
    CV_LOAD(A1, B1, q + 1); CV_MMA(A0, B0); CV_SCHED();
    CV_LOAD(A0, B0, (q + 2) & 3); CV_MMA(A1, B1); CV_SCHED();
  }
}

constexpr int g1_h(int i) { return i < 0 ? 4 : i + LA < 16 ? 4 : 0; }
constexpr int g1_x(int i) { return ((i > R1 && i <= 9) || i >= 13) ? 4 : 0; }
constexpr int g1_n(int i) { int n = g1_x(i - LA); for (int t = i - LA + 1; t < i; ++t) n += g1_h(t) + g1_x(t); return n; }
constexpr int g2_u(int j) { return j < 0 ? 5 : j + LA < 12 ? 5 : 0; }
constexpr int g2_x(int j) { return (j > R2 && j <= 5) ? 4 : 0; }
constexpr int g2_n(int j) { int n = g2_x(j - LA); for (int t = j - LA + 1; t < j; ++t) n += g2_u(t) + g2_x(t); return n; }

template <int M0, int NM>
DEVI void final_tiles(f4 (&own)[NM], const u2 (&xr)[NM], u2 stt, unsigned b2r, unsigned yab, char* op) {
  const float bb = blo(b2r), m1 = __uint_as_float(stt.x), inv1 = __uint_as_float(stt.y);
#pragma unroll
  for (int m = 0; m < NM; ++m) {
    const u2 ya = __builtin_bit_cast(u2, tr_read(yab + (M0 + m) * 2048));
    const float x0 = blo(xr[m].x), x1 = bhi(xr[m].x), x2 = blo(xr[m].y), x3 = bhi(xr[m].y);
    const float r0 = __builtin_fmaf((x0 - m1) * inv1, blo(ya.x), x0) + (own[m][0] + bb), r1 = __builtin_fmaf((x1 - m1) * inv1, bhi(ya.x), x1) + (own[m][1] + bb);
    const float r2 = __builtin_fmaf((x2 - m1) * inv1, blo(ya.y), x2) + (own[m][2] + bb), r3 = __builtin_fmaf((x3 - m1) * inv1, bhi(ya.y), x3) + (own[m][3] + bb);
    const u2 ov = {pk(r0, r1), pk(r2, r3)};
    asm volatile("global_store_dwordx2 %0, %1, off offset:%2" OUTPOL ::"v"(op), "v"(ov), "i"((M0 + m) * 32) : "memory");
  }
}

__global__ __launch_bounds__(512, 2) __attribute__((amdgpu_waves_per_eu(2, 2))) void fused_kernel(
    const char* __restrict__ x, const char* __restrict__ kk, const char* __restrict__ vv, const char* __restrict__ shk,
    const char* __restrict__ ssmk, const char* __restrict__ ssmd, const char* __restrict__ w1, const char* __restrict__ b1,
    const char* __restrict__ w2, const char* __restrict__ b2, char* __restrict__ out, char* ws) {
  extern __shared__ __attribute__((aligned(16))) char smem[];
  const int lane = threadIdx.x & 63, wave = __builtin_amdgcn_readfirstlane(threadIdx.x >> 6);
  const int b = blockIdx.x, xg = b & 7, r = b >> 3;
  const unsigned sb = __builtin_amdgcn_readfirstlane((unsigned)(size_t)smem);
  const unsigned ctl = sb + L_CTL;
  asm volatile("" ::"s"(x), "s"(kk), "s"(vv), "s"(shk), "s"(ssmk), "s"(ssmd), "s"(w1), "s"(b1), "s"(w2), "s"(b2), "s"(out), "s"(ws));
  const rsrc_t rws = __builtin_amdgcn_make_buffer_rsrc(ws, 0, WS_END, 0x00020000);
  if (wave == 0) STAMP(0);
  const int dt2 = r & 7, st = xg * 4 + (r >> 3);

  const u4 srdS = make_srd(ws, WS_END);
  const int g = lane >> 4, q = (lane >> 2) & 3, p = lane & 3;
  unsigned ring = sb + wave * L_RING;
  const bool has1 = r < 30;
  const int dt = MAP1 ? (xg & 1) * 5 + r % 5 : r % 10, ft = MAP1 ? (xg >> 1) * 6 + r / 5 : xg * 3 + r / 10;
  const int rot1 = dt * 16 / 10;
  int rot2 = dt2 * 12 / 8;
  const u4 srdW1 = make_srd(w1, 6291456), srdW2 = make_srd(w2, 6291456);
  unsigned vow[4];
#define DMA1(POL, slot, t, srd, soff)                                                                                          \
  asm volatile("s_mov_b32 m0, %0\n\ts_nop 0\n\tbuffer_load_dwordx4 %1, %2, %3 offen offset:%4" POL " lds" ::"s"(ring + (slot) * 4096), \
               "v"(vow[t]), "s"(srd), "s"(soff), "i"((t) * 1024)                                                               \
               : "memory")
#define KS1(i) (4u * (((i) + rot1) & 15) + wave)
#define KS2(j) (4u * ((j) + rot2 >= 12 ? (j) + rot2 - 12 : (j) + rot2) + wave)
#define W1_PIECE(i, t) do { if ((i) < NT_FIRST) DMA1(" nt", (i) % 7, t, srdW1, KS1(i) * 98304u); else DMA1("", (i) % 7, t, srdW1, KS1(i) * 98304u); } while (0)
#define W2_PIECE(j, t) do { if ((j) < NT_FIRST) DMA1(" nt", ((j) + 2) % 7, t, srdW2, KS2(j) * 131072u); else DMA1("", ((j) + 2) % 7, t, srdW2, KS2(j) * 131072u); } while (0)
#define W1_DMA(i) do { W1_PIECE(i, 0); W1_PIECE(i, 1); W1_PIECE(i, 2); W1_PIECE(i, 3); } while (0)
#define W2_DMA(j) do { W2_PIECE(j, 0); W2_PIECE(j, 1); W2_PIECE(j, 2); W2_PIECE(j, 3); } while (0)
#define W1_THR(i) do { W1_PIECE(i, 0); waitv<THR>(); W1_PIECE(i, 1); waitv<THR>(); W1_PIECE(i, 2); waitv<THR>(); W1_PIECE(i, 3); waitv<THR>(); } while (0)
#define W2_THR(j) do { W2_PIECE(j, 0); waitv<THR>(); W2_PIECE(j, 1); waitv<THR>(); W2_PIECE(j, 2); waitv<THR>(); W2_PIECE(j, 3); waitv<THR>(); } while (0)
#define LD_SC1(dst, voff, soff, imm) \
  asm volatile("buffer_load_dwordx4 %0, %1, %2, %3 offen offset:%4" HPOL : "=&v"(dst) : "v"(voff), "s"(srdS), "s"(soff), "i"(imm) : "memory")
#pragma unroll
  for (int t = 0; t < 4; ++t) vow[t] = (8 * t + (lane >> 3)) * 3072 + ft * 128 + (((lane & 7) ^ (2 * ((lane >> 4) & 1) + 4 * (t & 1))) * 16) - 1024 * t;
  u2 tag;
  unsigned cpf = 0;
  {
    unsigned xr[5];
    asm volatile("buffer_load_dword %0, %5, %6, %7 offen nt\n\tbuffer_load_dword %1, %5, %6, %7 offen offset:256 nt\n\t"
                 "buffer_load_dword %2, %5, %6, %7 offen offset:512 nt\n\tbuffer_load_dword %3, %5, %6, %7 offen offset:768 nt\n\t"
                 "buffer_load_dword %4, %5, %6, %7 offen offset:1024 nt"
                 : "=&v"(xr[0]), "=&v"(xr[1]), "=&v"(xr[2]), "=&v"(xr[3]), "=&v"(xr[4])
                 : "v"(lane * 4), "s"(make_srd(x, 2621440)), "s"((b * 8 + wave) * 1280)
                 : "memory");
    {
      const u4 srdK = make_srd(kk, 2621440), srdV = make_srd(vv, 2621440);
#pragma unroll
      for (int i = 0; i < 3; ++i) {
        const int pc = wave + 8 * i;
        const bool isv = pc >= 11;
        const int n = (isv ? pc - 11 : pc) * 64 + lane, g8 = (n * 6473) >> 19, rem = n - g8 * 81, rr = (rem * 205) >> 11;
        const int row = g8 * 8 + rr, c = rem - rr * 10;
        const int grow = st * 64 + row - (isv ? 0 : 3);
        const bool ok = rem < 80 && grow >= 0 && row < (isv ? 64 : 67);
        const unsigned vo = ok ? grow * 1280 + dt2 * 160 + c * 16 : 0x7fffff00u;
        const unsigned dst = sb + L_KBUF + pc * 1024;
        if (isv) asm volatile("s_mov_b32 m0, %0\n\ts_nop 0\n\tbuffer_load_dwordx4 %1, %2, 0 offen nt lds" ::"s"(dst), "v"(vo), "s"(srdV) : "memory");
        else     asm volatile("s_mov_b32 m0, %0\n\ts_nop 0\n\tbuffer_load_dwordx4 %1, %2, 0 offen nt lds" ::"s"(dst), "v"(vo), "s"(srdK) : "memory");
      }
    }
    u2 ep; u4 shw;
    asm volatile("buffer_load_dwordx2 %0, off, %2, 0 sc1\n\tbuffer_load_dwordx4 %1, %3, %4, 0 offen"
                 : "=&v"(ep), "=&v"(shw)
                 : "s"(srdS), "v"((dt2 * 80 + (wave >= 3 ? wave - 3 : 0) * 16 + (lane >> 3) * 2) * 8), "s"(make_srd(shk, 5120))
                 : "memory");
    static_assert(WS_EPOCH == 0, "epoch load above");
    if (CODEPF && wave == 7) {
      uint64_t pc; unsigned len;
      asm volatile("s_getpc_b64 %0\n.Lfk_here:\n\ts_mov_b32 %1, .Lfk_tail-.Lfk_here" : "=s"(pc), "=s"(len));
      const unsigned o = (r * 16 + (lane & 15)) * 128;
      asm volatile("global_load_dword %0, %1, %2" : "=v"(cpf) : "v"(o < len ? o : 0u), "s"(pc) : "memory");
    }
    if (TLBPF && wave == 6) {
      const int j = r * 16 + (lane & 15);
      const char* p = j < 96 ? w1 + j * 65536 : j < 192 ? w2 + (j - 96) * 65536 : j < 232 ? ssmk + (j - 192) * 65536
                    : j < 272 ? out + (j - 232) * 65536 : j < 424 ? ws + (j - 272) * 65536 : x;
      asm volatile("global_load_dword %0, %1, off sc1" : "=v"(cpf) : "v"(p) : "memory");
    }
    if (wave == 0 && lane < 16) lds_st<unsigned>(ctl + lane * 4, 0u);
    if (wave >= 6) waitv<6>(); else waitv<5>();
    asm volatile("" : "+v"(xr[0]), "+v"(xr[1]), "+v"(xr[2]), "+v"(xr[3]), "+v"(xr[4])::"memory");
    STAMP(1);
    unsigned hp[5]; float m1, inv1;
    ln_row(xr, hp, m1, inv1);
#pragma unroll
    for (int j = 0; j < 5; ++j) lds_st<unsigned>(sb + L_HBUF + wave * 1344 + (lane + 64 * j) * 4, hp[j]);
    if (lane == 0) lds_st<u2>(ctl + 64 + wave * 8, u2{__float_as_uint(m1), __float_as_uint(inv1)});
    STAMP(2);
    if (wave >= 6) waitv<1>(); else waitv<0>();
    asm volatile("" : "+v"(ep), "+v"(shw)::"memory");
    const uint64_t tg = (((uint64_t)ep.y << 32) | ep.x) * 6364136223846793005ull + 1442695040888963407ull;
    tag = u2{(unsigned)__builtin_amdgcn_readfirstlane((unsigned)tg), (unsigned)__builtin_amdgcn_readfirstlane((unsigned)(tg >> 32))};
    STAMP(3);
    raw_barrier();
    if (wave < 3) {
      const unsigned tb = sb + L_HBUF + ((lane >> 2) & 3) * 1344 + (lane >> 4) * 32 + (lane & 3) * 8;
      u4 v[4];
#pragma unroll
      for (int i = 0; i < 4; ++i) { const int it = i < 3 ? wave + 3 * i : 9; v[i] = __builtin_bit_cast(u4, cat8(tr_read(tb + it * 128), tr_read(tb + it * 128 + 4 * 1344))); }
      const u4 sv = lds_ld<u4>(ctl + 64 + (lane & 3) * 16);
#pragma unroll
      for (int i = 0; i < 4; ++i)
        if (i < 3 || wave == 0) st16_sc1(rws, WS_H + (b * 640 + (i < 3 ? wave + 3 * i : 9) * 64 + lane) * 16, v[i]);
      if (wave == 1 && lane < 4) st16_sc1(rws, WS_ST + b * 64 + lane * 16, sv);
    } else {
      const int rg = lane & 7, cp = (wave - 3) * 8 + (lane >> 3);
      const unsigned ka = sb + L_KBUF + rg * 1296 + cp * 4;
      unsigned kr[11], vr[8];
#pragma unroll
      for (int i = 0; i < 11; ++i) kr[i] = lds_ld<unsigned>(ka + i * 160 + (i >> 3) * 16);
#pragma unroll
      for (int i = 0; i < 8; ++i) vr[i] = lds_ld<unsigned>(ka + 11264 + i * 160);
      SCHED_FENCE();
      const f2 w0 = {blo(shw.x), blo(shw.z)}, w1f = {bhi(shw.x), bhi(shw.z)}, w2f = {blo(shw.y), blo(shw.w)}, w3 = {bhi(shw.y), bhi(shw.w)};
      f2 kf[11], o[8];
#pragma unroll
      for (int i = 0; i < 11; ++i) kf[i] = f2{blo(kr[i]), bhi(kr[i])};
#pragma unroll
      for (int i = 0; i < 8; ++i)
        o[i] = __builtin_elementwise_fma(kf[i], w3, __builtin_elementwise_fma(kf[i + 1], w2f, __builtin_elementwise_fma(kf[i + 2], w1f, kf[i + 3] * w0))) * f2{blo(vr[i]), bhi(vr[i])};
#pragma unroll
      for (int c = 0; c < 2; ++c)
        st16_sc1(rws, WS_KV + (dt2 * 80 + cp * 2 + c) * 4096 + (2040 - 64 * st - 8 * rg) * 2,
                 u4{pk(o[7][c], o[6][c]), pk(o[5][c], o[4][c]), pk(o[3][c], o[2][c]), pk(o[1][c], o[0][c])});
    }
    STAMP(4);
  }

  if (wave >= 4) {
    const int hw = wave - 4, pair = hw >> 1, half = hw & 1;
    static_assert(SPR == 2, "channel map below");
    const bool extra = xg < 4 && r < 24 && pair == XPAIR;
    const int xch = 544 + r * 4 + xg;
    const int nrounds = has1 ? (extra ? 2 : 1) : SPR;
    const int ch0 = has1 ? (extra ? xch : 2 * b + pair) : 480 + (b - 240) * 2 * SPR + pair, chs = extra ? 2 * b + pair - xch : 2;
    Filt f;
    filt_load(f, ssmk + (size_t)ch0 * 4096, half, lane);
    unsigned fpf = 0;
    if (FILTPF && nrounds > 1) {
      const int ri = 1 + half < nrounds ? 1 + half : nrounds - 1;
      asm volatile("global_load_dword %0, %1, %2" : "=v"(fpf) : "v"(lane * 64), "s"(ssmk + (size_t)(ch0 + chs * ri) * 4096) : "memory");
      waitv<5>();
    } else {
      waitv<4>();
    }
    asm volatile("" : "+v"(cpf));
    if (ctr_arrive(ctl + C_H * 4, lane) == 7 && lane < 8) st8_sc1(rws, WS_F1 + lane * 4096 + b * 16, tag);
    STAMP(20 + hw);
    unsigned dd0 = *(const unsigned short*)(ssmd + ch0 * 2);
    __builtin_amdgcn_s_setprio(HPRIO);
    ctr_wait(ctl + C_H * 4, 8);
    const unsigned cv = sb + L_CONV + pair * CV_BYTES;
    filt_build(f, cv, half, lane);
    if (hw == 3) {
#pragma unroll 1
      for (unsigned n = 0; n < SPIN_CAP; ++n) {
        bool ok = true;
#pragma unroll
        for (int i = 0; i < 4; ++i) { const u2 a = ld8_sc1(rws, WS_F1 + xg * 4096 + i * 1024 + lane * 16); ok = ok && a.x == tag.x && a.y == tag.y; }
        if (__all(ok)) break;
        asm volatile("" ::: "memory");
      }
      asm volatile("" ::: "memory");
      if (lane == 0) lds_st<unsigned>(ctl + C_G2 * 4, 1u);
      STAMP(24);
    } else {
      ctr_wait(ctl + C_G2 * 4, 1);
    }
    u4 xv[2];
#pragma unroll
    for (int j = 0; j < 2; ++j) xv[j] = ld16_sc1(rws, WS_KV + ch0 * 4096 + (lane + 64 * j + 128 * half) * 16);
    ctr_arrive(ctl + C_GO1 * 4, lane);
    if (b == 0 && threadIdx.x == 448) st8_sc1(rws, WS_EPOCH, tag);
    const int c16 = lane & 15, kg = lane >> 4;
    const unsigned pc = ctl + (C_P0 + pair) * 4;
    const unsigned ab = cv + (c16 & 3) * CV_CP + (c16 >> 2) * 8 + kg * 16 + half * 256;
    const unsigned bb = cv + CV_X + 480 - 32 * c16 + 16 * kg + half * 256;
    asm volatile("" : "+v"(xv[0]), "+v"(xv[1]), "+v"(dd0), "+v"(fpf));
#pragma unroll 1
    for (int round = 0; round < nrounds; ++round) {
      const int ch = ch0 + chs * round;
      if (round) filt_build(f, cv, half, lane);
#pragma unroll
      for (int j = 0; j < 2; ++j) lds_st<u4>(cv + CV_X + (lane + 64 * j + 128 * half) * 16, xv[j]);
      const float dd = blo(dd0);
      if (round + 1 < nrounds) {
        filt_load(f, ssmk + (size_t)(ch + chs) * 4096, half, lane);
#pragma unroll
        for (int j = 0; j < 2; ++j) xv[j] = ld16_sc1(rws, WS_KV + (ch + chs) * 4096 + (lane + 64 * j + 128 * half) * 16);
        dd0 = *(const unsigned short*)(ssmd + (ch + chs) * 2);
        if (!has1 && round + 2 == nrounds) ctr_arrive(ctl + C_KV * 4, lane);
      }
      ctr_arrive(pc, lane);
      ctr_wait(pc, round * 8 + 2);
      if (has1 && round + 1 == nrounds) ctr_wait(ctl + C_KV * 4, 4);
      f4 acc[8];
      if (round == 0) STAMP(30); else STAMP(31);
      if (XPRIO && has1 && round + 1 < nrounds) __builtin_amdgcn_s_setprio(XPRIO);
      conv_phases(acc, ab, bb);
      if (round == 1) STAMP(36);
      if (round == 0) STAMP(25 + hw);
      ctr_arrive(pc, lane);
      ctr_wait(pc, round * 8 + 4);
#pragma unroll
      for (int n = 0; n < 4; ++n) lds_st<f4>(cv + half * 4096 + n * 1024 + lane * 16, sel4(half, acc[n], acc[n + 4]));
      ctr_arrive(pc, lane);
      ctr_wait(pc, round * 8 + 6);
      u2 y[4];
#pragma unroll
      for (int n = 0; n < 4; ++n) {
        const f4 t = sel4(half, acc[n + 4], acc[n]) + lds_ld<f4>(cv + (half ^ 1) * 4096 + n * 1024 + lane * 16);
        const int t0 = 1024 * half + 256 * n + 16 * c16 + 4 * kg;
        const u2 xq = lds_ld<u2>(cv + CV_X + (2044 - t0) * 2);
        y[n] = u2{pk(__builtin_fmaf(bhi(xq.y), dd, t[0]), __builtin_fmaf(blo(xq.y), dd, t[1])),
                  pk(__builtin_fmaf(bhi(xq.x), dd, t[2]), __builtin_fmaf(blo(xq.x), dd, t[3]))};
      }
      asm volatile("" : "+v"(f.c[0]), "+v"(f.c[1]), "+v"(f.n[0]), "+v"(f.n[1]), "+v"(xv[0]), "+v"(xv[1]), "+v"(dd0));
      ctr_arrive(pc, lane);
#pragma unroll
      for (int n = 0; n < 4; n += 2) {
        const auto sx = __builtin_amdgcn_permlane16_swap(y[n].x, y[n + 1].x, false, false);
        const auto sy = __builtin_amdgcn_permlane16_swap(y[n].y, y[n + 1].y, false, false);
        st16_sc1(rws, WS_YA + ch * 4096 + (1024 * half + 256 * (n + (kg & 1)) + 16 * c16 + 8 * (kg >> 1)) * 2, u4{sx[0], sy[0], sx[1], sy[1]});
      }
      if (XPRIO) __builtin_amdgcn_s_setprio(HPRIO);
      ctr_wait(pc, round * 8 + 8);
    }
    waitv<0>();
    if (ctr_arrive(ctl + (C_Q0 + pair) * 4, lane) == 1 && lane < nrounds) st8_sc1(rws, WS_F3 + (ch0 + chs * lane) * 8, tag);
    STAMP(32 + hw);
    const int s = st * 64 + hw * 16 + c16;
    const size_t xo = (size_t)s * 1280 + (dt2 * 80 + 4 * kg) * 2;
    u2 xq[2] = {*(const u2*)(x + xo + 96), *(const u2*)(x + xo + 128)};
    u2 stt = ld8_sc1(rws, WS_ST + s * 8);
    unsigned b2r = *(const unsigned short*)(b2 + s * 2);
    asm volatile("" : "+v"(xq[0]), "+v"(xq[1]), "+v"(stt), "+v"(b2r));
    if (hw == 3) {
#pragma unroll 1
      for (unsigned n = 0; n < SPIN_CAP; ++n) {
        const u4 a = ld16_sc1(rws, WS_F3 + dt2 * 640 + (lane < 40 ? lane : 0) * 16);
        if (__all(a.x == tag.x && a.y == tag.y && a.z == tag.x && a.w == tag.y)) break;
        __builtin_amdgcn_s_sleep(2);
        asm volatile("" ::: "memory");
      }
      asm volatile("" ::: "memory");
      STAMP(37);
      u4 y[10];
#pragma unroll
      for (int i = 0; i < 10; ++i) y[i] = ld16_sc1(rws, WS_YA + (dt2 * 80 + i * 8 + (lane >> 3)) * 4096 + st * 128 + (lane & 7) * 16);
      SCHED_FENCE();
#pragma unroll
      for (int i = 0; i < 10; ++i) lds_st<u4>(sb + L_YA + i * 1024 + lane * 16, y[i]);
      STAMP(38);
    }
    raw_barrier();
    asm volatile(".Lfk_tail:");
    f4 own[2];
#pragma unroll
    for (int m = 0; m < 2; ++m) {
      const unsigned a = sb + (3 + m) * 1024 + lane * 16;
      own[m] = (lds_ld<f4>(a + hw * L_RING) + lds_ld<f4>(a + ((hw - 1) & 3) * L_RING + 5120)) +
               (lds_ld<f4>(a + ((hw - 2) & 3) * L_RING + 10240) + lds_ld<f4>(a + ((hw - 3) & 3) * L_RING + 15360));
    }
    final_tiles<3, 2>(own, xq, stt, b2r, sb + L_YA + (4 * kg + ((lane >> 2) & 3)) * 128 + hw * 32 + (lane & 3) * 8, out + xo);
    return;
  }

  waitv<0>();
  if (ctr_arrive(ctl + C_H * 4, lane) == 7 && lane < 8) st8_sc1(rws, WS_F1 + lane * 4096 + b * 16, tag);
  STAMP(16 + wave);
  __builtin_amdgcn_s_setprio(GPRIO);
#define POLL_F1() ctr_wait(ctl + C_GO1 * 4, 4)
  unsigned tra[4];
  s4 wf[2][8];

  const int s = st * 64 + wave * 16 + (lane & 15);
  const size_t xo = (size_t)s * 1280 + (dt2 * 80 + 4 * g) * 2;
  u2 xr[3], stt;
  unsigned b2r;
#define EPI_LOADS()                                                                                                          \
  do {                                                                                                                       \
    _Pragma("unroll") for (int m = 0; m < 3; ++m) xr[m] = *(const u2*)(x + xo + m * 32);                                     \
    stt = ld8_sc1(rws, WS_ST + s * 8);                                                                                       \
    b2r = *(const unsigned short*)(b2 + s * 2);                                                                              \
  } while (0)
#define EPI_PIN() asm volatile("" : "+v"(xr[0]), "+v"(xr[1]), "+v"(xr[2]), "+v"(stt), "+v"(b2r))
  if (has1) {
    {
      const int fsw = 2 * (q >> 1) + 4 * (g & 1);
#pragma unroll
      for (int m = 0; m < 4; ++m) tra[m] = ring + (8 * g + q) * 128 + (((2 * m) ^ fsw) + (p >> 1)) * 16 + (p & 1) * 8;
    }
    u2 bias[4];
#pragma unroll
    for (int m = 0; m < 4; ++m) bias[m] = *(const u2*)(b1 + (ft * 64 + 16 * m + 4 * g) * 2);
    unsigned voh[4];
#pragma unroll
    for (int n = 0; n < 4; ++n) voh[n] = WS_H + (g * 640 + dt * 64 + ((n + wave) & 3) * 16 + (lane & 15)) * 16;
    u4 hq[4][4];
    f4 acc[4][4];
#pragma unroll
    for (int m = 0; m < 4; ++m)
#pragma unroll
      for (int n = 0; n < 4; ++n) acc[m][n] = f4{0.f, 0.f, 0.f, 0.f};
#define H_PIECE(i, n) LD_SC1(hq[(i) & 3][n], voh[n], KS1(i) * 40960u, 0)
#define H_LOAD(i) do { H_PIECE(i, 0); H_PIECE(i, 1); H_PIECE(i, 2); H_PIECE(i, 3); } while (0)
#define H_WAIT(N, hs) asm volatile("s_waitcnt vmcnt(%4)" : "+v"(hs[0]), "+v"(hs[1]), "+v"(hs[2]), "+v"(hs[3]) : "i"(N) : "memory")
#define G1_RD(i, k) wf[(i) & 1][k] = tr_read(tra[(k) >> 1] + ((i) % 7) * 4096 + ((k) & 1) * 512)
    s4 res[R1][8];
    asm volatile("" : "+v"(bias[0]), "+v"(bias[1]), "+v"(bias[2]), "+v"(bias[3]));
    ctr_wait(ctl + C_H * 4, 8);
    W1_DMA(0); W1_DMA(1); W1_DMA(2); W1_DMA(3); W1_DMA(4); W1_DMA(5); W1_DMA(6);
#pragma unroll
    for (int i = 0; i < R1; ++i) {
      waitv<24>();
#pragma unroll
      for (int k = 0; k < 8; ++k) res[i][k] = tr_read(tra[k >> 1] + i * 4096 + (k & 1) * 512);
      asm volatile("s_waitcnt lgkmcnt(0)" ::: "memory");
      W1_DMA(7 + i);
    }
    STAMP(5 + wave);
    POLL_F1();
    STAMP(9 + wave);
    H_LOAD(0); H_LOAD(1); if (LA > 2) H_LOAD(2);
#define G1_A(i, k) ((i) < R1 ? cat8(res[(i) < R1 ? (i) : 0][(k) & 6], res[(i) < R1 ? (i) : 0][(k) | 1]) : cat8(wf[(i) & 1][(k) & 6], wf[(i) & 1][(k) | 1]))
#define G1_GROUP(i, k)                                                                                                       \
  do {                                                                                                                       \
    acc[(k) >> 1][((k) & 1) * 2] = mfma16(G1_A(i, k), __builtin_bit_cast(bf8, hq[(i) & 3][((k) & 1) * 2]), acc[(k) >> 1][((k) & 1) * 2]); \
    acc[(k) >> 1][((k) & 1) * 2 + 1] = mfma16(G1_A(i, k), __builtin_bit_cast(bf8, hq[(i) & 3][((k) & 1) * 2 + 1]), acc[(k) >> 1][((k) & 1) * 2 + 1]); \
    SCHED_FENCE();                                                                                                           \
    if ((i) + 1 < 16 && (i) + 1 >= R1) G1_RD((i) + 1, k);                                                                    \
    if ((k) < 4) { if (g1_h(i)) H_PIECE((i) + LA, (k) & 3); }                                                                 \
    else if (g1_x(i) && (i) <= 9) W1_PIECE((i) + 6, (k) & 3);                                                                \
    else if (g1_x(i)) W2_PIECE((i) - 13, (k) & 3);                                                                           \
    SCHED_FENCE();                                                                                                           \
  } while (0)
#define G1_STEP(i)                                                                                                           \
  do {                                                                                                                       \
    H_WAIT(g1_n(i), hq[(i) & 3]);                                                                                            \
    G1_GROUP(i, 0); G1_GROUP(i, 1); G1_GROUP(i, 2); G1_GROUP(i, 3); G1_GROUP(i, 4); G1_GROUP(i, 5); G1_GROUP(i, 6); G1_GROUP(i, 7); \
  } while (0)
    G1_STEP(0); G1_STEP(1); G1_STEP(2); G1_STEP(3); G1_STEP(4); G1_STEP(5); G1_STEP(6); G1_STEP(7); G1_STEP(8); G1_STEP(9);
#pragma unroll
    for (int t = 0; t < 4; ++t) vow[t] = (8 * t + (lane >> 3)) * 4096 + st * 128 + (((lane & 7) ^ ((lane >> 3) & 6)) * 16) - 1024 * t;
    G1_STEP(10); G1_STEP(11); G1_STEP(12); G1_STEP(13); G1_STEP(14); G1_STEP(15);
    STAMP(40 + wave);
    EPI_LOADS();
#pragma unroll
    for (int n = 1; n < 4; ++n)
#pragma unroll
      for (int m = 0; m < 4; ++m) lds_st<f4>(ring + (n == 1 ? 6 : n - 2) * 4096 + m * 1024 + lane * 16, acc[m][n]);
    ctr_arrive(ctl + C_G1 * 4, lane);
    STAMP(14);
    ctr_wait(ctl + C_G1 * 4, 4);
    STAMP(29);
#pragma unroll
    for (int n = 1; n < 4; ++n) {
      const unsigned a = sb + ((wave - n) & 3) * L_RING + (n == 1 ? 6 : n - 2) * 4096 + lane * 16;
#pragma unroll
      for (int m = 0; m < 4; ++m) acc[m][n] = lds_ld<f4>(a + m * 1024);
    }
    SCHED_FENCE();
#pragma unroll
    for (int m = 0; m < 4; ++m) acc[m][0] = (acc[m][0] + acc[m][1]) + (acc[m][2] + acc[m][3]);
    unsigned o[8];
#pragma unroll
    for (int m = 0; m < 4; ++m) {
      o[2 * m] = pk(gelu_t(acc[m][0][0] + blo(bias[m].x)), gelu_t(acc[m][0][1] + bhi(bias[m].x)));
      o[2 * m + 1] = pk(gelu_t(acc[m][0][2] + blo(bias[m].y)), gelu_t(acc[m][0][3] + bhi(bias[m].y)));
    }
#pragma unroll
    for (int P = 0; P < 2; ++P)
      st16_sc1(rws, WS_U + ((ft * 2 + P) * 40 + dt * 4 + wave) * 1024 + lane * 16, u4{o[4 * P], o[4 * P + 1], o[4 * P + 2], o[4 * P + 3]});
    ctr_arrive(ctl + C_KV * 4, lane);
    STAMP(13);
    EPI_PIN();
    waitv<0>();
    if (ctr_arrive(ctl + C_G3 * 4, lane) == 3 && lane < 8) st8_sc1(rws, WS_F2 + lane * 4096 + (dt * 24 + ft) * 16, tag);
    STAMP(44 + wave);
    ctr_wait(ctl + C_G3 * 4, 4);
    W2_DMA(3); W2_DMA(4); W2_DMA(5); W2_DMA(6);
  } else {
#pragma unroll
    for (int t = 0; t < 4; ++t) vow[t] = (8 * t + (lane >> 3)) * 4096 + st * 128 + (((lane & 7) ^ ((lane >> 3) & 6)) * 16) - 1024 * t;
    POLL_F1();
    EPI_LOADS();
    EPI_PIN();
    if (SPDELAY) ctr_wait(ctl + C_KV * 4, 4);
    W2_DMA(0); W2_DMA(1); W2_DMA(2); W2_DMA(3); W2_DMA(4); W2_DMA(5); W2_DMA(6);
  }

  asm volatile("" : "+s"(ring), "+s"(rot2));
  {
    const int fsw = 4 * (g & 1) + (q & 2);
#pragma unroll
    for (int n = 0; n < 4; ++n) tra[n] = ring + (4 * g + q) * 128 + (((2 * ((n + wave) & 3)) ^ fsw) + (p >> 1)) * 16 + (p & 1) * 8;
  }
  const unsigned vou0 = WS_U + dt2 * 5120 + lane * 16, vou1 = vou0 + 4096;
  u4 uq[4][5];
  f4 acc[5][4];
#pragma unroll
  for (int m = 0; m < 5; ++m)
#pragma unroll
    for (int n = 0; n < 4; ++n) acc[m][n] = f4{0.f, 0.f, 0.f, 0.f};
#define U_PIECE(j, m) LD_SC1(uq[(j) & 3][m], (m) < 4 ? vou0 : vou1, KS2(j) * 40960u, ((m) & 3) * 1024)
#define U_LOAD(j) do { U_PIECE(j, 0); U_PIECE(j, 1); U_PIECE(j, 2); U_PIECE(j, 3); U_PIECE(j, 4); } while (0)
#define U_WAIT(N, us) asm volatile("s_waitcnt vmcnt(%5)" : "+v"(us[0]), "+v"(us[1]), "+v"(us[2]), "+v"(us[3]), "+v"(us[4]) : "i"(N) : "memory")
#define G2_RD(j, k) wf[(j) & 1][k] = tr_read(tra[(k) >> 1] + (((j) + 2) % 7) * 4096 + ((k) & 1) * 2048)
#define G2_B(j, n) ((j) < R2 ? cat8(res2[(j) < R2 ? (j) : 0][2 * (n)], res2[(j) < R2 ? (j) : 0][2 * (n) + 1]) : cat8(wf[(j) & 1][2 * (n)], wf[(j) & 1][2 * (n) + 1]))
#define G2_MM(j, m, n) acc[m][n] = mfma16(__builtin_bit_cast(bf8, uq[(j) & 3][m]), G2_B(j, n), acc[m][n])
#define G2_GROUP(j, k)                                                                                                       \
  do {                                                                                                                       \
    G2_MM(j, (2 * (k)) % 5, (2 * (k)) / 5); G2_MM(j, (2 * (k) + 1) % 5, (2 * (k) + 1) / 5);                                  \
    SCHED_FENCE();                                                                                                           \
    if ((k) < 8 && (j) + 1 < 12 && (j) + 1 >= R2) G2_RD((j) + 1, (k) & 7);                                                   \
    if ((k) < 5) { if (g2_u(j)) U_PIECE((j) + LA, (k) % 5); }                                                                 \
    else if ((k) < 9 && g2_x(j)) W2_PIECE((j) + 6, ((k) - 5) & 3);                                                           \
    SCHED_FENCE();                                                                                                           \
  } while (0)
#define G2_STEP(j)                                                                                                           \
  do {                                                                                                                       \
    U_WAIT(g2_n(j), uq[(j) & 3]);                                                                                                  \
    G2_GROUP(j, 0); G2_GROUP(j, 1); G2_GROUP(j, 2); G2_GROUP(j, 3); G2_GROUP(j, 4);                                          \
    G2_GROUP(j, 5); G2_GROUP(j, 6); G2_GROUP(j, 7); G2_GROUP(j, 8); G2_GROUP(j, 9);                                          \
  } while (0)
  s4 res2[R2][8];
  static_assert(R2 <= 3, "wait below");
  waitv<16>();
  const unsigned f2o = WS_F2 + xg * 4096 + (((dt2 * 5) >> 2) * 24 + (lane < 48 ? lane : 0)) * 16;
  u2 f2v = {0u, 0u};
  if (wave == 0) asm volatile("buffer_load_dwordx2 %0, %1, %2, 0 offen sc1" : "=v"(f2v) : "v"(f2o), "s"(srdS) : "memory");
#pragma unroll
  for (int j = 0; j < R2; ++j) {
#pragma unroll
    for (int k = 0; k < 8; ++k) res2[j][k] = tr_read(tra[k >> 1] + ((j + 2) % 7) * 4096 + (k & 1) * 2048);
    asm volatile("s_waitcnt lgkmcnt(0)" ::: "memory");
    W2_DMA(7 + j);
  }
  STAMP(39);
  if (wave == 0) {
    asm volatile("s_waitcnt vmcnt(%1)" : "+v"(f2v) : "i"(4 * R2) : "memory");
    if (!__all(f2v.x == tag.x && f2v.y == tag.y)) {
#pragma unroll 1
      for (unsigned n = 0; n < SPIN_CAP; ++n) {
        const u2 a = ld8_sc1(rws, f2o);
        if (__all(a.x == tag.x && a.y == tag.y)) break;
        asm volatile("" ::: "memory");
      }
    }
    asm volatile("" ::: "memory");
    if (lane == 0) lds_st<unsigned>(ctl + C_GO2 * 4, 1u);
  } else {
    ctr_wait(ctl + C_GO2 * 4, 1);
  }
  STAMP(48 + wave);
  U_LOAD(0); U_LOAD(1); if (LA > 2) U_LOAD(2);
  G2_STEP(0); G2_STEP(1); G2_STEP(2); G2_STEP(3); G2_STEP(4); G2_STEP(5); G2_STEP(6); G2_STEP(7); G2_STEP(8); G2_STEP(9); G2_STEP(10); G2_STEP(11);
  STAMP(52 + wave);
#pragma unroll
  for (int n = 0; n < 4; ++n)
#pragma unroll
    for (int m = (n == 0 ? 3 : 0); m < 5; ++m) lds_st<f4>(ring + (n * 5 + m) * 1024 + lane * 16, acc[m][n]);
  f4 own[3] = {acc[0][0], acc[1][0], acc[2][0]};
  raw_barrier();
  STAMP(56 + wave);
#pragma unroll
  for (int w = 1; w < 4; ++w) {
    const unsigned a = sb + ((wave - w) & 3) * L_RING + w * 5120 + lane * 16;
#pragma unroll
    for (int m = 0; m < 3; ++m) own[m] += lds_ld<f4>(a + m * 1024);
  }
  final_tiles<0, 3>(own, xr, stt, b2r, sb + L_YA + (4 * g + q) * 128 + wave * 32 + p * 8, out + xo);
  STAMP(60 + wave);
}

extern "C" void kernel_launch(void* const* d_in, const int* in_sizes, int n_in, void* d_out, int out_size, void* d_ws, size_t ws_size,
                              hipStream_t stream) {
  (void)hipFuncSetAttribute((const void*)fused_kernel, hipFuncAttributeMaxDynamicSharedMemorySize, L_TOTAL);
  fused_kernel<<<256, 512, L_TOTAL, stream>>>((const char*)d_in[0], (const char*)d_in[1], (const char*)d_in[2], (const char*)d_in[3],
                                              (const char*)d_in[4], (const char*)d_in[5], (const char*)d_in[7], (const char*)d_in[8],
                                              (const char*)d_in[9], (const char*)d_in[10], (char*)d_out, (char*)d_ws);
}
```

```cpp
#include <hip/hip_runtime.h>
#include <hip/hip_bf16.h>
#include <stdint.h>

typedef __attribute__((ext_vector_type(8))) __bf16 bfx8;
typedef __attribute__((ext_vector_type(2))) __bf16 bf2;
typedef __attribute__((ext_vector_type(4))) short s4;
typedef __attribute__((ext_vector_type(4))) float f4;
typedef __attribute__((ext_vector_type(2))) float f2;
typedef __attribute__((ext_vector_type(4))) unsigned u4;
typedef __attribute__((ext_vector_type(2))) unsigned u2;
#define LAS __attribute__((address_space(3)))
#define DEVI __device__ __forceinline__
#define SCHED_FENCE() __builtin_amdgcn_sched_barrier(0)

#define R1 5
#define R2 3
#define LA 3
#define SPR 2

constexpr unsigned WS_EPOCH = 0;
constexpr unsigned WS_F1 = 4096;
constexpr unsigned WS_F2 = WS_F1 + 32768;
constexpr unsigned WS_F4 = WS_F2 + 32768;
constexpr unsigned WS_F3 = WS_F4 + 32768;
constexpr unsigned WS_ST = WS_F3 + 8192;
constexpr unsigned WS_H = WS_ST + 16384;
constexpr unsigned WS_KV = WS_H + 2621440;
constexpr unsigned WS_YA = WS_KV + 2621440;
constexpr unsigned WS_U = WS_YA + 2621440;
constexpr unsigned WS_END = WS_U + 1966080;


constexpr unsigned L_RING = 28672;
constexpr unsigned CV_CP = 4160;
constexpr unsigned CV_X = 4 * CV_CP;
constexpr unsigned CV_BYTES = 21248;
constexpr unsigned L_CONV = 4 * L_RING;
constexpr unsigned L_HBUF = L_CONV;
constexpr unsigned L_KBUF = L_CONV + 10752;
constexpr unsigned L_VBUF = L_KBUF + 11264;
constexpr unsigned L_YA = L_CONV + CV_BYTES;
constexpr unsigned L_CTL = L_CONV + 2 * CV_BYTES;
constexpr unsigned L_TOTAL = L_CTL + 128;
enum { C_H = 0, C_GO1, C_G1, C_G2, C_G3, C_GO2, C_P0, C_P1, C_Q0, C_Q1, C_KV, C_K2 };
constexpr unsigned SPIN_CAP = 1u << 16;

DEVI float blo(unsigned u) { return __uint_as_float(u << 16); }
DEVI float bhi(unsigned u) { return __uint_as_float(u & 0xffff0000u); }
DEVI unsigned pk(float a, float b) { f2 v = {a, b}; return __builtin_bit_cast(unsigned, __builtin_convertvector(v, bf2)); }
DEVI float rbf(float a) { return blo(pk(a, a)); }
template <int CTRL, int RM> DEVI float dppf(float x) {
  return __builtin_bit_cast(float, __builtin_amdgcn_update_dpp(0, __builtin_bit_cast(int, x), CTRL, RM, 0xf, true));
}
template <class T> DEVI T ldnt(const void* p) { return __builtin_nontemporal_load((const T*)p); }
DEVI u4 make_srd(const void* p, unsigned bytes) {
  uint64_t a = (uint64_t)p;
  u4 r = {(unsigned)a, (unsigned)(a >> 32) & 0xffffu, bytes, 0x00020000u};
  return r;
}
DEVI s4 tr_read(unsigned addr) { return __builtin_amdgcn_ds_read_tr16_b64_v4i16((LAS s4*)(size_t)addr); }
DEVI bfx8 cat8(s4 a, s4 b) {
  typedef __attribute__((ext_vector_type(8))) short s8;
  s8 v = __builtin_shufflevector(a, b, 0, 1, 2, 3, 4, 5, 6, 7);
  return __builtin_bit_cast(bfx8, v);
}
DEVI f4 mfma16(bfx8 a, bfx8 b, f4 c) { return __builtin_amdgcn_mfma_f32_16x16x32_bf16(a, b, c, 0, 0, 0); }
template <int N> DEVI void waitv() { asm volatile("s_waitcnt vmcnt(%0)" ::"i"(N) : "memory"); }
template <class T> DEVI T lds_ld(unsigned a) { return *(const LAS T*)(size_t)a; }
template <class T> DEVI void lds_st(unsigned a, T v) { *(LAS T*)(size_t)a = v; }
DEVI void raw_barrier() { asm volatile("s_waitcnt lgkmcnt(0)\n\ts_barrier" ::: "memory"); }
DEVI unsigned ctr_arrive(unsigned a, int lane) {
  asm volatile("s_waitcnt lgkmcnt(0)" ::: "memory");
  unsigned old = 0;
  if (lane == 0) old = __hip_atomic_fetch_add((LAS unsigned*)(size_t)a, 1u, __ATOMIC_RELAXED, __HIP_MEMORY_SCOPE_WORKGROUP);
  old = __builtin_amdgcn_readfirstlane(old);
  asm volatile("" ::: "memory");
  return old;
}
DEVI void ctr_wait(unsigned a, unsigned n) {
  while (__builtin_amdgcn_readfirstlane(__hip_atomic_load((LAS unsigned*)(size_t)a, __ATOMIC_RELAXED, __HIP_MEMORY_SCOPE_WORKGROUP)) < n)
    __builtin_amdgcn_s_sleep(1);
  asm volatile("" ::: "memory");
}
typedef __amdgpu_buffer_rsrc_t rsrc_t;
DEVI u4 ld16_sc1(rsrc_t r, unsigned off) { return __builtin_amdgcn_raw_buffer_load_b128(r, off, 0, 16); }
DEVI u2 ld8_sc1(rsrc_t r, unsigned off) { return __builtin_amdgcn_raw_buffer_load_b64(r, off, 0, 16); }
DEVI void st16_sc1(rsrc_t r, unsigned off, u4 v) { __builtin_amdgcn_raw_buffer_store_b128(v, r, off, 0, 16); }
DEVI void st8_sc1(rsrc_t r, unsigned off, u2 v) { __builtin_amdgcn_raw_buffer_store_b64(v, r, off, 0, 16); }

DEVI f4 sel4(bool c, f4 a, f4 b) {
  asm("" : "+v"(a)); asm("" : "+v"(b));
  return c ? a : b;
}
DEVI float gelu_t(float x) {
  const float t = x * __builtin_fmaf(x * x, -0.10294324f, -2.3022082f);
  return x * __builtin_amdgcn_rcpf(1.f + __builtin_amdgcn_exp2f(t));
}

DEVI void ln_row(const unsigned (&xr)[5], unsigned (&hp)[5], float& m1, float& inv1) {
  float e[10], s = 0.f, q = 0.f;
  const float piv = blo(__builtin_amdgcn_readfirstlane(xr[0]));
#pragma unroll
  for (int j = 0; j < 5; ++j) { e[2 * j] = blo(xr[j]); e[2 * j + 1] = bhi(xr[j]); }
#pragma unroll
  for (int j = 0; j < 10; ++j) { const float d = e[j] - piv; s += d; q = __builtin_fmaf(d, d, q); }
  s += dppf<0xB1, 0xf>(s);  q += dppf<0xB1, 0xf>(q);
  s += dppf<0x4E, 0xf>(s);  q += dppf<0x4E, 0xf>(q);
  s += dppf<0x141, 0xf>(s); q += dppf<0x141, 0xf>(q);
  s += dppf<0x140, 0xf>(s); q += dppf<0x140, 0xf>(q);
  s += dppf<0x142, 0xa>(s); q += dppf<0x142, 0xa>(q);
  s += dppf<0x143, 0xc>(s); q += dppf<0x143, 0xc>(q);
  const float md = __builtin_bit_cast(float, __builtin_amdgcn_readlane(__builtin_bit_cast(int, s), 63)) * (1.f / 640.f);
  const float qq = __builtin_bit_cast(float, __builtin_amdgcn_readlane(__builtin_bit_cast(int, q), 63)) * (1.f / 640.f);
  const float var = __builtin_fmaxf(qq - md * md, 0.f), mean = piv + md;
  m1 = rbf(mean);
  inv1 = __builtin_amdgcn_rcpf(rbf(__builtin_amdgcn_sqrtf(rbf(rbf(var) + 1e-5f))));
  const float m2 = rbf((mean - m1) * inv1);
  const float inv2 = __builtin_amdgcn_rcpf(rbf(__builtin_amdgcn_sqrtf(rbf(rbf(var * inv1 * inv1) + 1e-5f))));
  const float A = inv1 * inv2, B = -m2 * inv2;
#pragma unroll
  for (int j = 0; j < 5; ++j) hp[j] = pk(__builtin_fmaf(e[2 * j] - m1, A, B), __builtin_fmaf(e[2 * j + 1] - m1, A, B));
}

struct Filt { u4 c[2]; u2 n[2]; };
DEVI void filt_load(Filt& f, const char* krow, int half, int lane) {
#pragma unroll
  for (int j = 0; j < 2; ++j) {
    const int z = lane + 64 * j + 128 * half;
    f.c[j] = ldnt<u4>(krow + z * 16);
    f.n[j] = ldnt<u2>(krow + (z < 255 ? z * 16 + 16 : 0));
  }
}
DEVI void filt_build(Filt& f, unsigned cv, int half, int lane) {
  const u4 zero = {0u, 0u, 0u, 0u};
  if (half == 1) {
    if (lane == 63) f.n[1] = u2{0u, 0u};
    if (lane < 30) lds_st<u4>(cv + CV_X + 4096 + lane * 16, zero);
  } else {
    if (lane < 4) lds_st<u4>(cv + lane * CV_CP, zero);
    if (lane == 0) {
      const unsigned d4 = f.c[0].x, d5 = f.c[0].y;
      lds_st<u4>(cv + 16, u4{0u, 0u, 0u, d4 << 16});
      lds_st<u4>(cv + CV_CP + 16, u4{0u, 0u, 0u, d4});
      lds_st<u4>(cv + 2 * CV_CP + 16, u4{0u, 0u, d4 << 16, __builtin_amdgcn_alignbit(d5, d4, 16)});
      lds_st<u4>(cv + 3 * CV_CP + 16, u4{0u, 0u, d4, d5});
    }
  }
#pragma unroll
  for (int j = 0; j < 2; ++j) {
    const u4 c = f.c[j]; const u2 n = f.n[j];
    const unsigned a0 = __builtin_amdgcn_alignbit(c.y, c.x, 16), a1 = __builtin_amdgcn_alignbit(c.z, c.y, 16), a2 = __builtin_amdgcn_alignbit(c.w, c.z, 16),
                   a3 = __builtin_amdgcn_alignbit(n.x, c.w, 16), a4 = __builtin_amdgcn_alignbit(n.y, n.x, 16);
    const unsigned p = cv + (lane + 64 * j + 128 * half + 2) * 16;
    lds_st<u4>(p, u4{a0, a1, a2, a3});
    lds_st<u4>(p + CV_CP, u4{c.y, c.z, c.w, n.x});
    lds_st<u4>(p + 2 * CV_CP, u4{a1, a2, a3, a4});
    lds_st<u4>(p + 3 * CV_CP, u4{c.z, c.w, n.x, n.y});
  }
}

DEVI void conv_phases(f4 (&acc)[8], unsigned ab, unsigned bb) {
  bfx8 A0[8], B0[8], A1[8], B1[8];
#pragma unroll
  for (int n = 0; n < 8; ++n) acc[n] = f4{0.f, 0.f, 0.f, 0.f};
#define CV_LOAD(A, B, q)                                                                              \
  _Pragma("unroll") for (int p = 0; p < 8; ++p) {                                                      \
    B[p] = *(const volatile LAS bfx8*)(size_t)(bb + 3584 - 512 * p + 64 * (q));                          \
    s4 lo = *(const volatile LAS s4*)(size_t)(ab + 512 * p + 64 * (q));                                 \
    s4 hi = *(const volatile LAS s4*)(size_t)(ab + 512 * p + 64 * (q) + 8);                             \
    A[p] = cat8(lo, hi);                                                                               \
  }
#define CV_MMA(A, B)                                                                                   \
  _Pragma("unroll") for (int p = 0; p < 8; ++p)                                                        \
  _Pragma("unroll") for (int m = 0; m + p < 8; ++m) acc[p + m] = mfma16(A[p], B[m], acc[p + m]);
#define CV_SCHED()                                                                                     \
  _Pragma("unroll") for (int z = 0; z < 24; ++z) {                                                     \
    __builtin_amdgcn_sched_group_barrier(0x8, 1, 0);                                                   \
    __builtin_amdgcn_sched_group_barrier(0x100, 1, 0);                                                 \
  }                                                                                                    \
  __builtin_amdgcn_sched_group_barrier(0x8, 12, 0);                                                    \
  SCHED_FENCE();
  CV_LOAD(A0, B0, 0);
  SCHED_FENCE();
#pragma unroll 1
  for (int q = 0; q < 4; q += 2) {
    CV_LOAD(A1, B1, q + 1); CV_MMA(A0, B0); CV_SCHED();
    CV_LOAD(A0, B0, (q + 2) & 3); CV_MMA(A1, B1); CV_SCHED();
  }
}

constexpr int g1_h(int i) { return i < 0 ? 4 : i + LA < 16 ? 4 : 0; }
constexpr int g1_x(int i) { return ((i > R1 && i <= 9) || i >= 13) ? 4 : 0; }
constexpr int g1_n(int i) { int n = g1_x(i - LA); for (int t = i - LA + 1; t < i; ++t) n += g1_h(t) + g1_x(t); return n; }
constexpr int g2_u(int j) { return j < 0 ? 5 : j + LA < 12 ? 5 : 0; }
constexpr int g2_x(int j) { return (j > R2 && j <= 5) ? 4 : 0; }
constexpr int g2_n(int j) { int n = g2_x(j - LA); for (int t = j - LA + 1; t < j; ++t) n += g2_u(t) + g2_x(t); return n; }

template <int M0, int NM>
DEVI void final_tiles(f4 (&own)[NM], const u2 (&xr)[NM], u2 stt, unsigned b2r, unsigned yab, char* op) {
  const float bb = blo(b2r), m1 = __uint_as_float(stt.x), inv1 = __uint_as_float(stt.y);
#pragma unroll
  for (int m = 0; m < NM; ++m) {
    const u2 ya = __builtin_bit_cast(u2, tr_read(yab + (M0 + m) * 2048));
    const float x0 = blo(xr[m].x), x1 = bhi(xr[m].x), x2 = blo(xr[m].y), x3 = bhi(xr[m].y);
    const float r0 = __builtin_fmaf((x0 - m1) * inv1, blo(ya.x), x0) + (own[m][0] + bb), r1 = __builtin_fmaf((x1 - m1) * inv1, bhi(ya.x), x1) + (own[m][1] + bb);
    const float r2 = __builtin_fmaf((x2 - m1) * inv1, blo(ya.y), x2) + (own[m][2] + bb), r3 = __builtin_fmaf((x3 - m1) * inv1, bhi(ya.y), x3) + (own[m][3] + bb);
    const u2 ov = {pk(r0, r1), pk(r2, r3)};
    asm volatile("global_store_dwordx2 %0, %1, off offset:%2" ::"v"(op), "v"(ov), "i"((M0 + m) * 32) : "memory");
  }
}

__global__ __launch_bounds__(512, 2) __attribute__((amdgpu_waves_per_eu(2, 2))) void fused_kernel(
    const char* __restrict__ x, const char* __restrict__ kk, const char* __restrict__ vv, const char* __restrict__ shk,
    const char* __restrict__ ssmk, const char* __restrict__ ssmd, const char* __restrict__ w1, const char* __restrict__ b1,
    const char* __restrict__ w2, const char* __restrict__ b2, char* __restrict__ out, char* ws) {
  extern __shared__ __attribute__((aligned(16))) char smem[];
  const int lane = threadIdx.x & 63, wave = __builtin_amdgcn_readfirstlane(threadIdx.x >> 6);
  const int b = blockIdx.x, xg = b & 7, r = b >> 3;
  const unsigned sb = __builtin_amdgcn_readfirstlane((unsigned)(size_t)smem);
  const unsigned ctl = sb + L_CTL;
  asm volatile("" ::"s"(x), "s"(kk), "s"(vv), "s"(shk), "s"(ssmk), "s"(ssmd), "s"(w1), "s"(b1), "s"(w2), "s"(b2), "s"(out), "s"(ws));
  const rsrc_t rws = __builtin_amdgcn_make_buffer_rsrc(ws, 0, WS_END, 0x00020000);
  const int dt2 = r & 7, st = xg * 4 + (r >> 3);

  const u4 srdS = make_srd(ws, WS_END);
  const int g = lane >> 4, q = (lane >> 2) & 3, p = lane & 3;
  unsigned ring = sb + wave * L_RING;
  const bool has1 = r < 30;
  const int dt = r % 10, ft = xg * 3 + r / 10;
  const int rot1 = dt * 16 / 10;
  int rot2 = dt2 * 12 / 8;
  const u4 srdW1 = make_srd(w1, 6291456), srdW2 = make_srd(w2, 6291456);
  unsigned vow[4];
#define DMA1(slot, t, srd, soff)                                                                                               \
  asm volatile("s_mov_b32 m0, %0\n\ts_nop 0\n\tbuffer_load_dwordx4 %1, %2, %3 offen offset:%4 lds" ::"s"(ring + (slot) * 4096), \
               "v"(vow[t]), "s"(srd), "s"(soff), "i"((t) * 1024)                                                               \
               : "memory")
#define KS1(i) (4u * (((i) + rot1) & 15) + wave)
#define KS2(j) (4u * ((j) + rot2 >= 12 ? (j) + rot2 - 12 : (j) + rot2) + wave)
#define W1_PIECE(i, t) DMA1((i) % 7, t, srdW1, KS1(i) * 98304u)
#define W2_PIECE(j, t) DMA1(((j) + 2) % 7, t, srdW2, KS2(j) * 131072u)
#define W1_DMA(i) do { W1_PIECE(i, 0); W1_PIECE(i, 1); W1_PIECE(i, 2); W1_PIECE(i, 3); } while (0)
#define W2_DMA(j) do { W2_PIECE(j, 0); W2_PIECE(j, 1); W2_PIECE(j, 2); W2_PIECE(j, 3); } while (0)
#define LD_SC1(dst, voff, soff, imm) \
  asm volatile("buffer_load_dwordx4 %0, %1, %2, %3 offen offset:%4 sc1" : "=&v"(dst) : "v"(voff), "s"(srdS), "s"(soff), "i"(imm) : "memory")
#pragma unroll
  for (int t = 0; t < 4; ++t) vow[t] = (8 * t + (lane >> 3)) * 3072 + ft * 128 + (((lane & 7) ^ (2 * ((lane >> 4) & 1) + 4 * (t & 1))) * 16) - 1024 * t;
  u2 tag;
  unsigned cpf = 0;
  {
    unsigned xr[5];
    asm volatile("buffer_load_dword %0, %5, %6, %7 offen nt\n\tbuffer_load_dword %1, %5, %6, %7 offen offset:256 nt\n\t"
                 "buffer_load_dword %2, %5, %6, %7 offen offset:512 nt\n\tbuffer_load_dword %3, %5, %6, %7 offen offset:768 nt\n\t"
                 "buffer_load_dword %4, %5, %6, %7 offen offset:1024 nt"
                 : "=&v"(xr[0]), "=&v"(xr[1]), "=&v"(xr[2]), "=&v"(xr[3]), "=&v"(xr[4])
                 : "v"(lane * 4), "s"(make_srd(x, 2621440)), "s"((b * 8 + wave) * 1280)
                 : "memory");
    {
      const u4 srdK = make_srd(kk, 2621440), srdV = make_srd(vv, 2621440);
#pragma unroll
      for (int i = 0; i < 3; ++i) {
        const int pc = wave + 8 * i;
        const bool isv = pc >= 11;
        const int n = (isv ? pc - 11 : pc) * 64 + lane, g8 = (n * 6473) >> 19, rem = n - g8 * 81, rr = (rem * 205) >> 11;
        const int row = g8 * 8 + rr, c = rem - rr * 10;
        const int grow = st * 64 + row - (isv ? 0 : 3);
        const bool ok = rem < 80 && grow >= 0 && row < (isv ? 64 : 67);
        const unsigned vo = ok ? grow * 1280 + dt2 * 160 + c * 16 : 0x7fffff00u;
        const unsigned dst = sb + L_KBUF + pc * 1024;
        if (isv) asm volatile("s_mov_b32 m0, %0\n\ts_nop 0\n\tbuffer_load_dwordx4 %1, %2, 0 offen nt lds" ::"s"(dst), "v"(vo), "s"(srdV) : "memory");
        else     asm volatile("s_mov_b32 m0, %0\n\ts_nop 0\n\tbuffer_load_dwordx4 %1, %2, 0 offen nt lds" ::"s"(dst), "v"(vo), "s"(srdK) : "memory");
      }
    }
    u2 ep; u4 shw;
    asm volatile("buffer_load_dwordx2 %0, off, %2, 0 sc1\n\tbuffer_load_dwordx4 %1, %3, %4, 0 offen"
                 : "=&v"(ep), "=&v"(shw)
                 : "s"(srdS), "v"((dt2 * 80 + (wave >= 3 ? wave - 3 : 0) * 16 + (lane >> 3) * 2) * 8), "s"(make_srd(shk, 5120))
                 : "memory");
    static_assert(WS_EPOCH == 0, "epoch load above");
    if (wave == 7) {
      uint64_t pc; unsigned len;
      asm volatile("s_getpc_b64 %0\n.Lfk_here:\n\ts_mov_b32 %1, .Lfk_tail-.Lfk_here" : "=s"(pc), "=s"(len));
      const unsigned o = (r * 16 + (lane & 15)) * 128;
      asm volatile("global_load_dword %0, %1, %2" : "=v"(cpf) : "v"(o < len ? o : 0u), "s"(pc) : "memory");
    }
    if (wave == 6) {
      const int j = r * 16 + (lane & 15);
      const char* p = j < 96 ? w1 + j * 65536 : j < 192 ? w2 + (j - 96) * 65536 : j < 232 ? ssmk + (j - 192) * 65536
                    : j < 272 ? out + (j - 232) * 65536 : j < 424 ? ws + (j - 272) * 65536 : x;
      asm volatile("global_load_dword %0, %1, off sc1" : "=v"(cpf) : "v"(p) : "memory");
    }
    if (wave == 0 && lane < 16) lds_st<unsigned>(ctl + lane * 4, 0u);
    if (wave >= 6) waitv<6>(); else waitv<5>();
    asm volatile("" : "+v"(xr[0]), "+v"(xr[1]), "+v"(xr[2]), "+v"(xr[3]), "+v"(xr[4])::"memory");
    unsigned hp[5]; float m1, inv1;
    ln_row(xr, hp, m1, inv1);
#pragma unroll
    for (int j = 0; j < 5; ++j) lds_st<unsigned>(sb + L_HBUF + wave * 1344 + (lane + 64 * j) * 4, hp[j]);
    if (lane == 0) lds_st<u2>(ctl + 64 + wave * 8, u2{__float_as_uint(m1), __float_as_uint(inv1)});
    if (wave >= 6) waitv<1>(); else waitv<0>();
    asm volatile("" : "+v"(ep), "+v"(shw)::"memory");
    const uint64_t tg = (((uint64_t)ep.y << 32) | ep.x) * 6364136223846793005ull + 1442695040888963407ull;
    tag = u2{(unsigned)__builtin_amdgcn_readfirstlane((unsigned)tg), (unsigned)__builtin_amdgcn_readfirstlane((unsigned)(tg >> 32))};
    raw_barrier();
    if (wave < 3) {
      const unsigned tb = sb + L_HBUF + ((lane >> 2) & 3) * 1344 + (lane >> 4) * 32 + (lane & 3) * 8;
      u4 v[4];
#pragma unroll
      for (int i = 0; i < 4; ++i) { const int it = i < 3 ? wave + 3 * i : 9; v[i] = __builtin_bit_cast(u4, cat8(tr_read(tb + it * 128), tr_read(tb + it * 128 + 4 * 1344))); }
      const u4 sv = lds_ld<u4>(ctl + 64 + (lane & 3) * 16);
#pragma unroll
      for (int i = 0; i < 4; ++i)
        if (i < 3 || wave == 0) st16_sc1(rws, WS_H + (b * 640 + (i < 3 ? wave + 3 * i : 9) * 64 + lane) * 16, v[i]);
      if (wave == 1 && lane < 4) st16_sc1(rws, WS_ST + b * 64 + lane * 16, sv);
    } else {
      const int rg = lane & 7, cp = (wave - 3) * 8 + (lane >> 3);
      const unsigned ka = sb + L_KBUF + rg * 1296 + cp * 4;
      unsigned kr[11], vr[8];
#pragma unroll
      for (int i = 0; i < 11; ++i) kr[i] = lds_ld<unsigned>(ka + i * 160 + (i >> 3) * 16);
#pragma unroll
      for (int i = 0; i < 8; ++i) vr[i] = lds_ld<unsigned>(ka + 11264 + i * 160);
      SCHED_FENCE();
      const f2 w0 = {blo(shw.x), blo(shw.z)}, w1f = {bhi(shw.x), bhi(shw.z)}, w2f = {blo(shw.y), blo(shw.w)}, w3 = {bhi(shw.y), bhi(shw.w)};
      f2 kf[11], o[8];
#pragma unroll
      for (int i = 0; i < 11; ++i) kf[i] = f2{blo(kr[i]), bhi(kr[i])};
#pragma unroll
      for (int i = 0; i < 8; ++i)
        o[i] = __builtin_elementwise_fma(kf[i], w3, __builtin_elementwise_fma(kf[i + 1], w2f, __builtin_elementwise_fma(kf[i + 2], w1f, kf[i + 3] * w0))) * f2{blo(vr[i]), bhi(vr[i])};
#pragma unroll
      for (int c = 0; c < 2; ++c)
        st16_sc1(rws, WS_KV + (dt2 * 80 + cp * 2 + c) * 4096 + (2040 - 64 * st - 8 * rg) * 2,
                 u4{pk(o[7][c], o[6][c]), pk(o[5][c], o[4][c]), pk(o[3][c], o[2][c]), pk(o[1][c], o[0][c])});
    }
  }

  if (wave >= 4) {
    const int hw = wave - 4, pair = hw >> 1, half = hw & 1;
    static_assert(SPR == 2, "channel map below");
    const bool extra = xg < 4 && r < 24 && pair == 0;
    const int xch = 544 + r * 4 + xg;
    const int nrounds = has1 ? (extra ? 2 : 1) : SPR;
    const int ch0 = has1 ? (extra ? xch : 2 * b + pair) : 480 + (b - 240) * 2 * SPR + pair, chs = extra ? 2 * b + pair - xch : 2;
    Filt f;
    filt_load(f, ssmk + (size_t)ch0 * 4096, half, lane);
    unsigned fpf = 0;
    if (nrounds > 1) {
      const int ri = 1 + half < nrounds ? 1 + half : nrounds - 1;
      asm volatile("global_load_dword %0, %1, %2" : "=v"(fpf) : "v"(lane * 64), "s"(ssmk + (size_t)(ch0 + chs * ri) * 4096) : "memory");
      waitv<5>();
    } else {
      waitv<4>();
    }
    asm volatile("" : "+v"(cpf));
    if (ctr_arrive(ctl + C_H * 4, lane) == 7 && lane < 8) st8_sc1(rws, WS_F1 + lane * 4096 + b * 16, tag);
    unsigned dd0 = *(const unsigned short*)(ssmd + ch0 * 2);
    ctr_wait(ctl + C_H * 4, 8);
    const unsigned cv = sb + L_CONV + pair * CV_BYTES;
    filt_build(f, cv, half, lane);
    if (hw == 3) {
#pragma unroll 1
      for (unsigned n = 0; n < SPIN_CAP; ++n) {
        bool ok = true;
#pragma unroll
        for (int i = 0; i < 4; ++i) { const u2 a = ld8_sc1(rws, WS_F1 + xg * 4096 + i * 1024 + lane * 16); ok = ok && a.x == tag.x && a.y == tag.y; }
        if (__all(ok)) break;
        asm volatile("" ::: "memory");
      }
      asm volatile("" ::: "memory");
      if (lane == 0) lds_st<unsigned>(ctl + C_G2 * 4, 1u);
    } else {
      ctr_wait(ctl + C_G2 * 4, 1);
    }
    u4 xv[2];
#pragma unroll
    for (int j = 0; j < 2; ++j) xv[j] = ld16_sc1(rws, WS_KV + ch0 * 4096 + (lane + 64 * j + 128 * half) * 16);
    ctr_arrive(ctl + C_GO1 * 4, lane);
    if (b == 0 && threadIdx.x == 448) st8_sc1(rws, WS_EPOCH, tag);
    const int c16 = lane & 15, kg = lane >> 4;
    const unsigned pc = ctl + (C_P0 + pair) * 4;
    const unsigned ab = cv + (c16 & 3) * CV_CP + (c16 >> 2) * 8 + kg * 16 + half * 256;
    const unsigned bb = cv + CV_X + 480 - 32 * c16 + 16 * kg + half * 256;
    asm volatile("" : "+v"(xv[0]), "+v"(xv[1]), "+v"(dd0), "+v"(fpf));
#pragma unroll 1
    for (int round = 0; round < nrounds; ++round) {
      const int ch = ch0 + chs * round;
      if (round) filt_build(f, cv, half, lane);
#pragma unroll
      for (int j = 0; j < 2; ++j) lds_st<u4>(cv + CV_X + (lane + 64 * j + 128 * half) * 16, xv[j]);
      const float dd = blo(dd0);
      if (round + 1 < nrounds) {
        filt_load(f, ssmk + (size_t)(ch + chs) * 4096, half, lane);
#pragma unroll
        for (int j = 0; j < 2; ++j) xv[j] = ld16_sc1(rws, WS_KV + (ch + chs) * 4096 + (lane + 64 * j + 128 * half) * 16);
        dd0 = *(const unsigned short*)(ssmd + (ch + chs) * 2);
        if (!has1 && round + 2 == nrounds) ctr_arrive(ctl + C_KV * 4, lane);
      }
      ctr_arrive(pc, lane);
      ctr_wait(pc, round * 8 + 2);
      if (has1 && round + 1 == nrounds) ctr_wait(ctl + C_KV * 4, 4);
      f4 acc[8];
      if (round + 1 == nrounds) __builtin_amdgcn_s_setprio(2);
      conv_phases(acc, ab, bb);
      ctr_arrive(pc, lane);
      ctr_wait(pc, round * 8 + 4);
#pragma unroll
      for (int n = 0; n < 4; ++n) lds_st<f4>(cv + half * 4096 + n * 1024 + lane * 16, sel4(half, acc[n], acc[n + 4]));
      ctr_arrive(pc, lane);
      ctr_wait(pc, round * 8 + 6);
      u2 y[4];
#pragma unroll
      for (int n = 0; n < 4; ++n) {
        const f4 t = sel4(half, acc[n + 4], acc[n]) + lds_ld<f4>(cv + (half ^ 1) * 4096 + n * 1024 + lane * 16);
        const int t0 = 1024 * half + 256 * n + 16 * c16 + 4 * kg;
        const u2 xq = lds_ld<u2>(cv + CV_X + (2044 - t0) * 2);
        y[n] = u2{pk(__builtin_fmaf(bhi(xq.y), dd, t[0]), __builtin_fmaf(blo(xq.y), dd, t[1])),
                  pk(__builtin_fmaf(bhi(xq.x), dd, t[2]), __builtin_fmaf(blo(xq.x), dd, t[3]))};
      }
      asm volatile("" : "+v"(f.c[0]), "+v"(f.c[1]), "+v"(f.n[0]), "+v"(f.n[1]), "+v"(xv[0]), "+v"(xv[1]), "+v"(dd0));
      ctr_arrive(pc, lane);
#pragma unroll
      for (int n = 0; n < 4; n += 2) {
        const auto sx = __builtin_amdgcn_permlane16_swap(y[n].x, y[n + 1].x, false, false);
        const auto sy = __builtin_amdgcn_permlane16_swap(y[n].y, y[n + 1].y, false, false);
        st16_sc1(rws, WS_YA + ch * 4096 + (1024 * half + 256 * (n + (kg & 1)) + 16 * c16 + 8 * (kg >> 1)) * 2, u4{sx[0], sy[0], sx[1], sy[1]});
      }
      ctr_wait(pc, round * 8 + 8);
    }
    waitv<0>();
    if (ctr_arrive(ctl + (C_Q0 + pair) * 4, lane) == 1 && lane < nrounds) st8_sc1(rws, WS_F3 + (ch0 + chs * lane) * 8, tag);
    __builtin_amdgcn_s_setprio(0);
    const int s = st * 64 + hw * 16 + c16;
    const size_t xo = (size_t)s * 1280 + (dt2 * 80 + 4 * kg) * 2;
    u2 xq[2] = {*(const u2*)(x + xo + 96), *(const u2*)(x + xo + 128)};
    u2 stt = ld8_sc1(rws, WS_ST + s * 8);
    unsigned b2r = *(const unsigned short*)(b2 + s * 2);
    asm volatile("" : "+v"(xq[0]), "+v"(xq[1]), "+v"(stt), "+v"(b2r));
    if (hw == 3) {
#pragma unroll 1
      for (unsigned n = 0; n < SPIN_CAP; ++n) {
        const u4 a = ld16_sc1(rws, WS_F3 + dt2 * 640 + (lane < 40 ? lane : 0) * 16);
        if (__all(a.x == tag.x && a.y == tag.y && a.z == tag.x && a.w == tag.y)) break;
        __builtin_amdgcn_s_sleep(2);
        asm volatile("" ::: "memory");
      }
      asm volatile("" ::: "memory");
      u4 y[10];
#pragma unroll
      for (int i = 0; i < 10; ++i) y[i] = ld16_sc1(rws, WS_YA + (dt2 * 80 + i * 8 + (lane >> 3)) * 4096 + st * 128 + (lane & 7) * 16);
      SCHED_FENCE();
#pragma unroll
      for (int i = 0; i < 10; ++i) lds_st<u4>(sb + L_YA + i * 1024 + lane * 16, y[i]);
    }
    raw_barrier();
    asm volatile(".Lfk_tail:");
    f4 own[2];
#pragma unroll
    for (int m = 0; m < 2; ++m) {
      const unsigned a = sb + (3 + m) * 1024 + lane * 16;
      own[m] = (lds_ld<f4>(a + hw * L_RING) + lds_ld<f4>(a + ((hw - 1) & 3) * L_RING + 5120)) +
               (lds_ld<f4>(a + ((hw - 2) & 3) * L_RING + 10240) + lds_ld<f4>(a + ((hw - 3) & 3) * L_RING + 15360));
    }
    final_tiles<3, 2>(own, xq, stt, b2r, sb + L_YA + (4 * kg + ((lane >> 2) & 3)) * 128 + hw * 32 + (lane & 3) * 8, out + xo);
    return;
  }

  waitv<0>();
  if (ctr_arrive(ctl + C_H * 4, lane) == 7 && lane < 8) st8_sc1(rws, WS_F1 + lane * 4096 + b * 16, tag);
#define POLL_F1() ctr_wait(ctl + C_GO1 * 4, 4)
  unsigned tra[4];
  s4 wf[2][8];

  const int s = st * 64 + wave * 16 + (lane & 15);
  const size_t xo = (size_t)s * 1280 + (dt2 * 80 + 4 * g) * 2;
  u2 xr[3], stt;
  unsigned b2r;
#define EPI_LOADS()                                                                                                          \
  do {                                                                                                                       \
    _Pragma("unroll") for (int m = 0; m < 3; ++m) xr[m] = *(const u2*)(x + xo + m * 32);                                     \
    stt = ld8_sc1(rws, WS_ST + s * 8);                                                                                       \
    b2r = *(const unsigned short*)(b2 + s * 2);                                                                              \
  } while (0)
#define EPI_PIN() asm volatile("" : "+v"(xr[0]), "+v"(xr[1]), "+v"(xr[2]), "+v"(stt), "+v"(b2r))
  if (has1) {
    {
      const int fsw = 2 * (q >> 1) + 4 * (g & 1);
#pragma unroll
      for (int m = 0; m < 4; ++m) tra[m] = ring + (8 * g + q) * 128 + (((2 * m) ^ fsw) + (p >> 1)) * 16 + (p & 1) * 8;
    }
    u2 bias[4];
#pragma unroll
    for (int m = 0; m < 4; ++m) bias[m] = *(const u2*)(b1 + (ft * 64 + 16 * m + 4 * g) * 2);
    unsigned voh[4];
#pragma unroll
    for (int n = 0; n < 4; ++n) voh[n] = WS_H + (g * 640 + dt * 64 + ((n + wave) & 3) * 16 + (lane & 15)) * 16;
    u4 hq[4][4];
    f4 acc[4][4];
#pragma unroll
    for (int m = 0; m < 4; ++m)
#pragma unroll
      for (int n = 0; n < 4; ++n) acc[m][n] = f4{0.f, 0.f, 0.f, 0.f};
#define H_PIECE(i, n) LD_SC1(hq[(i) & 3][n], voh[n], KS1(i) * 40960u, 0)
#define H_LOAD(i) do { H_PIECE(i, 0); H_PIECE(i, 1); H_PIECE(i, 2); H_PIECE(i, 3); } while (0)
#define H_WAIT(N, hs) asm volatile("s_waitcnt vmcnt(%4)" : "+v"(hs[0]), "+v"(hs[1]), "+v"(hs[2]), "+v"(hs[3]) : "i"(N) : "memory")
#define G1_RD(i, k) wf[(i) & 1][k] = tr_read(tra[(k) >> 1] + ((i) % 7) * 4096 + ((k) & 1) * 512)
    s4 res[R1][8];
    asm volatile("" : "+v"(bias[0]), "+v"(bias[1]), "+v"(bias[2]), "+v"(bias[3]));
    ctr_wait(ctl + C_H * 4, 8);
    W1_DMA(0); W1_DMA(1); W1_DMA(2); W1_DMA(3); W1_DMA(4); W1_DMA(5); W1_DMA(6);
#pragma unroll
    for (int i = 0; i < R1; ++i) {
      waitv<24>();
#pragma unroll
      for (int k = 0; k < 8; ++k) res[i][k] = tr_read(tra[k >> 1] + i * 4096 + (k & 1) * 512);
      asm volatile("s_waitcnt lgkmcnt(0)" ::: "memory");
      W1_DMA(7 + i);
    }
    POLL_F1();
    H_LOAD(0); H_LOAD(1); if (LA > 2) H_LOAD(2);
#define G1_A(i, k) ((i) < R1 ? cat8(res[(i) < R1 ? (i) : 0][(k) & 6], res[(i) < R1 ? (i) : 0][(k) | 1]) : cat8(wf[(i) & 1][(k) & 6], wf[(i) & 1][(k) | 1]))
#define G1_GROUP(i, k)                                                                                                       \
  do {                                                                                                                       \
    acc[(k) >> 1][((k) & 1) * 2] = mfma16(G1_A(i, k), __builtin_bit_cast(bfx8, hq[(i) & 3][((k) & 1) * 2]), acc[(k) >> 1][((k) & 1) * 2]); \
    acc[(k) >> 1][((k) & 1) * 2 + 1] = mfma16(G1_A(i, k), __builtin_bit_cast(bfx8, hq[(i) & 3][((k) & 1) * 2 + 1]), acc[(k) >> 1][((k) & 1) * 2 + 1]); \
    SCHED_FENCE();                                                                                                           \
    if ((i) + 1 < 16 && (i) + 1 >= R1) G1_RD((i) + 1, k);                                                                    \
    if ((k) < 4) { if (g1_h(i)) H_PIECE((i) + LA, (k) & 3); }                                                                 \
    else if (g1_x(i) && (i) <= 9) W1_PIECE((i) + 6, (k) & 3);                                                                \
    else if (g1_x(i)) W2_PIECE((i) - 13, (k) & 3);                                                                           \
    SCHED_FENCE();                                                                                                           \
  } while (0)
#define G1_STEP(i)                                                                                                           \
  do {                                                                                                                       \
    H_WAIT(g1_n(i), hq[(i) & 3]);                                                                                            \
    G1_GROUP(i, 0); G1_GROUP(i, 1); G1_GROUP(i, 2); G1_GROUP(i, 3); G1_GROUP(i, 4); G1_GROUP(i, 5); G1_GROUP(i, 6); G1_GROUP(i, 7); \
  } while (0)
    G1_STEP(0); G1_STEP(1); G1_STEP(2); G1_STEP(3); G1_STEP(4); G1_STEP(5); G1_STEP(6); G1_STEP(7); G1_STEP(8); G1_STEP(9);
#pragma unroll
    for (int t = 0; t < 4; ++t) vow[t] = (8 * t + (lane >> 3)) * 4096 + st * 128 + (((lane & 7) ^ ((lane >> 3) & 6)) * 16) - 1024 * t;
    G1_STEP(10); G1_STEP(11); G1_STEP(12); G1_STEP(13); G1_STEP(14); G1_STEP(15);
    __builtin_amdgcn_s_setprio(1);
    EPI_LOADS();
#pragma unroll
    for (int n = 1; n < 4; ++n)
#pragma unroll
      for (int m = 0; m < 4; ++m) lds_st<f4>(ring + (n == 1 ? 6 : n - 2) * 4096 + m * 1024 + lane * 16, acc[m][n]);
    ctr_arrive(ctl + C_G1 * 4, lane);
    ctr_wait(ctl + C_G1 * 4, 4);
#pragma unroll
    for (int n = 1; n < 4; ++n) {
      const unsigned a = sb + ((wave - n) & 3) * L_RING + (n == 1 ? 6 : n - 2) * 4096 + lane * 16;
#pragma unroll
      for (int m = 0; m < 4; ++m) acc[m][n] = lds_ld<f4>(a + m * 1024);
    }
    SCHED_FENCE();
#pragma unroll
    for (int m = 0; m < 4; ++m) acc[m][0] = (acc[m][0] + acc[m][1]) + (acc[m][2] + acc[m][3]);
    unsigned o[8];
#pragma unroll
    for (int m = 0; m < 4; ++m) {
      o[2 * m] = pk(gelu_t(acc[m][0][0] + blo(bias[m].x)), gelu_t(acc[m][0][1] + bhi(bias[m].x)));
      o[2 * m + 1] = pk(gelu_t(acc[m][0][2] + blo(bias[m].y)), gelu_t(acc[m][0][3] + bhi(bias[m].y)));
    }
#pragma unroll
    for (int P = 0; P < 2; ++P)
      st16_sc1(rws, WS_U + ((ft * 2 + P) * 40 + dt * 4 + wave) * 1024 + lane * 16, u4{o[4 * P], o[4 * P + 1], o[4 * P + 2], o[4 * P + 3]});
    ctr_arrive(ctl + C_KV * 4, lane);
    EPI_PIN();
    waitv<0>();
    if (ctr_arrive(ctl + C_G3 * 4, lane) == 3 && lane < 8) st8_sc1(rws, WS_F2 + lane * 4096 + (dt * 24 + ft) * 16, tag);
    __builtin_amdgcn_s_setprio(0);
    ctr_wait(ctl + C_G3 * 4, 4);
    W2_DMA(3); W2_DMA(4); W2_DMA(5); W2_DMA(6);
  } else {
#pragma unroll
    for (int t = 0; t < 4; ++t) vow[t] = (8 * t + (lane >> 3)) * 4096 + st * 128 + (((lane & 7) ^ ((lane >> 3) & 6)) * 16) - 1024 * t;
    POLL_F1();
    EPI_LOADS();
    EPI_PIN();
    ctr_wait(ctl + C_KV * 4, 4);
    W2_DMA(0); W2_DMA(1); W2_DMA(2); W2_DMA(3); W2_DMA(4); W2_DMA(5); W2_DMA(6);
  }

  asm volatile("" : "+s"(ring), "+s"(rot2));
  {
    const int fsw = 4 * (g & 1) + (q & 2);
#pragma unroll
    for (int n = 0; n < 4; ++n) tra[n] = ring + (4 * g + q) * 128 + (((2 * ((n + wave) & 3)) ^ fsw) + (p >> 1)) * 16 + (p & 1) * 8;
  }
  const unsigned vou0 = WS_U + dt2 * 5120 + lane * 16, vou1 = vou0 + 4096;
  u4 uq[4][5];
  f4 acc[5][4];
#pragma unroll
  for (int m = 0; m < 5; ++m)
#pragma unroll
    for (int n = 0; n < 4; ++n) acc[m][n] = f4{0.f, 0.f, 0.f, 0.f};
#define U_PIECE(j, m) LD_SC1(uq[(j) & 3][m], (m) < 4 ? vou0 : vou1, KS2(j) * 40960u, ((m) & 3) * 1024)
#define U_LOAD(j) do { U_PIECE(j, 0); U_PIECE(j, 1); U_PIECE(j, 2); U_PIECE(j, 3); U_PIECE(j, 4); } while (0)
#define U_WAIT(N, us) asm volatile("s_waitcnt vmcnt(%5)" : "+v"(us[0]), "+v"(us[1]), "+v"(us[2]), "+v"(us[3]), "+v"(us[4]) : "i"(N) : "memory")
#define G2_RD(j, k) wf[(j) & 1][k] = tr_read(tra[(k) >> 1] + (((j) + 2) % 7) * 4096 + ((k) & 1) * 2048)
#define G2_B(j, n) ((j) < R2 ? cat8(res2[(j) < R2 ? (j) : 0][2 * (n)], res2[(j) < R2 ? (j) : 0][2 * (n) + 1]) : cat8(wf[(j) & 1][2 * (n)], wf[(j) & 1][2 * (n) + 1]))
#define G2_MM(j, m, n) acc[m][n] = mfma16(__builtin_bit_cast(bfx8, uq[(j) & 3][m]), G2_B(j, n), acc[m][n])
#define G2_GROUP(j, k)                                                                                                       \
  do {                                                                                                                       \
    G2_MM(j, (2 * (k)) % 5, (2 * (k)) / 5); G2_MM(j, (2 * (k) + 1) % 5, (2 * (k) + 1) / 5);                                  \
    SCHED_FENCE();                                                                                                           \
    if ((k) < 8 && (j) + 1 < 12 && (j) + 1 >= R2) G2_RD((j) + 1, (k) & 7);                                                   \
    if ((k) < 5) { if (g2_u(j)) U_PIECE((j) + LA, (k) % 5); }                                                                 \
    else if ((k) < 9 && g2_x(j)) W2_PIECE((j) + 6, ((k) - 5) & 3);                                                           \
    SCHED_FENCE();                                                                                                           \
  } while (0)
#define G2_STEP(j)                                                                                                           \
  do {                                                                                                                       \
    U_WAIT(g2_n(j), uq[(j) & 3]);                                                                                                  \
    G2_GROUP(j, 0); G2_GROUP(j, 1); G2_GROUP(j, 2); G2_GROUP(j, 3); G2_GROUP(j, 4);                                          \
    G2_GROUP(j, 5); G2_GROUP(j, 6); G2_GROUP(j, 7); G2_GROUP(j, 8); G2_GROUP(j, 9);                                          \
  } while (0)
  s4 res2[R2][8];
  static_assert(R2 <= 3, "wait below");
  waitv<16>();
  const unsigned f2o = WS_F2 + xg * 4096 + (((dt2 * 5) >> 2) * 24 + (lane < 48 ? lane : 0)) * 16;
  u2 f2v = {0u, 0u};
  if (wave == 0) asm volatile("buffer_load_dwordx2 %0, %1, %2, 0 offen sc1" : "=v"(f2v) : "v"(f2o), "s"(srdS) : "memory");
#pragma unroll
  for (int j = 0; j < R2; ++j) {
#pragma unroll
    for (int k = 0; k < 8; ++k) res2[j][k] = tr_read(tra[k >> 1] + ((j + 2) % 7) * 4096 + (k & 1) * 2048);
    asm volatile("s_waitcnt lgkmcnt(0)" ::: "memory");
    W2_DMA(7 + j);
  }
  if (wave == 0) {
    asm volatile("s_waitcnt vmcnt(%1)" : "+v"(f2v) : "i"(4 * R2) : "memory");
    if (!__all(f2v.x == tag.x && f2v.y == tag.y)) {
#pragma unroll 1
      for (unsigned n = 0; n < SPIN_CAP; ++n) {
        const u2 a = ld8_sc1(rws, f2o);
        if (__all(a.x == tag.x && a.y == tag.y)) break;
        asm volatile("" ::: "memory");
      }
    }
    asm volatile("" ::: "memory");
    if (lane == 0) lds_st<unsigned>(ctl + C_GO2 * 4, 1u);
  } else {
    ctr_wait(ctl + C_GO2 * 4, 1);
  }
  U_LOAD(0); U_LOAD(1); if (LA > 2) U_LOAD(2);
  G2_STEP(0); G2_STEP(1); G2_STEP(2); G2_STEP(3); G2_STEP(4); G2_STEP(5); G2_STEP(6); G2_STEP(7); G2_STEP(8); G2_STEP(9); G2_STEP(10); G2_STEP(11);
#pragma unroll
  for (int n = 0; n < 4; ++n)
#pragma unroll
    for (int m = (n == 0 ? 3 : 0); m < 5; ++m) lds_st<f4>(ring + (n * 5 + m) * 1024 + lane * 16, acc[m][n]);
  f4 own[3] = {acc[0][0], acc[1][0], acc[2][0]};
  raw_barrier();
#pragma unroll
  for (int w = 1; w < 4; ++w) {
    const unsigned a = sb + ((wave - w) & 3) * L_RING + w * 5120 + lane * 16;
#pragma unroll
    for (int m = 0; m < 3; ++m) own[m] += lds_ld<f4>(a + m * 1024);
  }
  final_tiles<0, 3>(own, xr, stt, b2r, sb + L_YA + (4 * g + q) * 128 + wave * 32 + p * 8, out + xo);
}

extern "C" void kernel_launch(void* const* d_in, const int* in_sizes, int n_in, void* d_out, int out_size, void* d_ws, size_t ws_size,
                              hipStream_t stream) {
  (void)hipFuncSetAttribute((const void*)fused_kernel, hipFuncAttributeMaxDynamicSharedMemorySize, L_TOTAL);
  fused_kernel<<<256, 512, L_TOTAL, stream>>>((const char*)d_in[0], (const char*)d_in[1], (const char*)d_in[2], (const char*)d_in[3],
                                              (const char*)d_in[4], (const char*)d_in[5], (const char*)d_in[7], (const char*)d_in[8],
                                              (const char*)d_in[9], (const char*)d_in[10], (char*)d_out, (char*)d_ws);
}
```
